```python
import torch

def _softmax(x, axis=-1):
    return torch.softmax(x, dim=axis)
def _gelu(x):
    return torch.nn.functional.gelu(x, approximate="tanh")
def _silu(x):
    return torch.nn.functional.silu(x)
def _sigmoid(x):
    return torch.sigmoid(x)
def _layernorm(x, axis=-1, eps=1e-5):
    m = x.mean(dim=axis, keepdim=True)
    v = x.var(dim=axis, keepdim=True, unbiased=False)
    return (x - m) / torch.sqrt(v + eps)
def _scatter_add(tgt, idx, src, axis=0):
    return tgt.clone().index_add_(axis, idx, src)
def _segment_sum(src, seg, n):
    out = torch.zeros((n,) + src.shape[1:], dtype=src.dtype)
    return out.index_add_(0, seg, src)
def _pad(x, pw):
    flat = tuple(v for pair in reversed(pw) for v in pair)
    return torch.nn.functional.pad(x, flat)

kernel_name = 'concat_heads__linear_kernel_hedgehog__sliding_block__prompt'

def setup_inputs():

    _w = (lambda a: (lambda t: t.to(torch.bfloat16) if t.is_floating_point() else t)(torch.as_tensor(a)))
    def _A_setup():
        B, S, D, H = 1, 2048, 640, 32
        Dh = D // H
        return {
            "x": _w((lambda *s: torch.randn(*s, dtype=torch.bfloat16) * 0.4)(B, S, D)),
            "wqkv": _w((lambda *s: torch.randn(*s, dtype=torch.bfloat16) * 1.6)(D, 3 * D)),
            "w_phi": _w((lambda *s: torch.randn(*s, dtype=torch.bfloat16) * 1.6)(H, Dh, Dh) * (Dh ** -0.5)),
            "wo": _w((lambda *s: torch.randn(*s, dtype=torch.bfloat16) * 0.2)(D, D)),
        }
    _A_in = _A_setup()

    def _B_setup():
        B, S, D = 1, 2048, 640
        return {
            "x": _w((lambda *s: torch.randn(*s, dtype=torch.bfloat16) * 0.2)(B, S, D)),
            "wqkv": _w((lambda *s: torch.randn(*s, dtype=torch.bfloat16) * 0.4)(D, 3 * D)),
            "wo": _w((lambda *s: torch.randn(*s, dtype=torch.bfloat16) * 0.2)(D, D)),
        }
    _B_in = _B_setup()

    return {
        "A_x": _A_in["x"],
        "A_wqkv": _A_in["wqkv"],
        "A_w_phi": _A_in["w_phi"],
        "A_wo": _A_in["wo"],
        "B_x": _B_in["x"],
        "B_wqkv": _B_in["wqkv"],
        "B_wo": _B_in["wo"],
    }

def reference(A_x, A_wqkv, A_w_phi, A_wo, B_x, B_wqkv, B_wo):
    def _A_ref(x, wqkv, w_phi, wo):
        B, S, D = x.shape
        H = 32; Dh = D // H
        qkv = (lambda x, *d: x.permute(*d))((x @ wqkv).reshape(B, S, 3, H, Dh), 2, 0, 3, 1, 4)
        q, k, v = qkv[0], qkv[1], qkv[2]
        pq = torch.einsum("bhsd,hdf->bhsf", q, w_phi)
        pk = torch.einsum("bhsd,hdf->bhsf", k, w_phi)
        phi_q = torch.concatenate([_softmax(pq, axis=-1), _softmax(-pq, axis=-1)], axis=-1)
        phi_k = torch.concatenate([_softmax(pk, axis=-1), _softmax(-pk, axis=-1)], axis=-1)
        kv = torch.einsum("bhsf,bhsd->bhfd", phi_k, v)
        z  = torch.einsum("bhsf->bhf", phi_k)
        num = torch.einsum("bhqf,bhfd->bhqd", phi_q, kv)
        den = torch.einsum("bhqf,bhf->bhq", phi_q, z)[..., None] + 1e-6
        o = num / den
        return (lambda x, *d: x.permute(*d))(o, 0, 2, 1, 3).reshape(B, S, D) @ wo

    def _B_ref(x, wqkv, wo):
        B, S, D = x.shape
        H = 32; Dh = D // H; W = 256
        qkv = (lambda x, *d: x.permute(*d))((x @ wqkv).reshape(B, S, 3, H, Dh), 2, 0, 3, 1, 4)
        q, k, v = qkv[0], qkv[1], qkv[2]
        scores = torch.einsum("bhqd,bhkd->bhqk", q, k) * (Dh ** -0.5)
        idx = torch.arange(S)
        mask = (idx[:, None] - idx[None, :] >= 0) & (idx[:, None] - idx[None, :] < W)
        scores = torch.where(mask[None, None], scores, -1e9)
        p = _softmax(scores, axis=-1)
        o = torch.einsum("bhqk,bhkd->bhqd", p, v)
        return (lambda x, *d: x.permute(*d))(o, 0, 2, 1, 3).reshape(B, S, D) @ wo

    a = _A_ref(A_x, A_wqkv, A_w_phi, A_wo)
    b = _B_ref(B_x, B_wqkv, B_wo)
    return torch.concatenate([a, b], axis=-1)
```

```cpp
#include <hip/hip_runtime.h>
#include <hip/hip_bf16.h>
typedef unsigned short u16;
__device__ __forceinline__ float b2f(u16 v){ return __uint_as_float(((unsigned)v)<<16); }
__device__ __forceinline__ u16 f2b(float f){ __hip_bfloat16 b = __float2bfloat16(f); return *(u16*)&b; }
__device__ __forceinline__ float rb(float f){ return b2f(f2b(f)); }

__global__ void gemm_naive(const u16* A, const u16* B, u16* C, int M, int N, int K, int ldc){
  __shared__ float sA[16][17], sB[16][17];
  int tx=threadIdx.x, ty=threadIdx.y;
  int row=blockIdx.y*16+ty, col=blockIdx.x*16+tx;
  float acc=0;
  for(int k0=0;k0<K;k0+=16){
    sA[ty][tx]=b2f(A[row*K+k0+tx]);
    sB[ty][tx]=b2f(B[(k0+ty)*N+col]);
    __syncthreads();
    for(int k=0;k<16;k++) acc+=sA[ty][k]*sB[k][tx];
    __syncthreads();
  }
  C[row*ldc+col]=f2b(acc);
}
__global__ void phi_kernel(const u16* qkv, const u16* wphi, float* phi){
  int idx=blockIdx.x*blockDim.x+threadIdx.x;
  if(idx>=2*32*2048) return;
  int s=idx%2048, h=(idx/2048)%32, w=idx/(2048*32);
  float x[20], p[20];
  for(int d=0;d<20;d++) x[d]=b2f(qkv[s*1920+w*640+h*20+d]);
  float mx=-1e30f, mn=1e30f;
  for(int f=0;f<20;f++){ float a=0; for(int d=0;d<20;d++) a+=x[d]*b2f(wphi[h*400+d*20+f]); a=rb(a); p[f]=a; mx=fmaxf(mx,a); mn=fminf(mn,a);}
  float s1=0,s2=0;
  for(int f=0;f<20;f++){ s1+=__expf(p[f]-mx); s2+=__expf(mn-p[f]); }
  float* o=phi+((size_t)(w*32+h)*2048+s)*40;
  for(int f=0;f<20;f++){ o[f]=rb(__expf(p[f]-mx)/s1); o[20+f]=rb(__expf(mn-p[f])/s2); }
}
__global__ void kv_kernel(const u16* qkv, const float* phi, float* kv){
  int h=blockIdx.x; int t=threadIdx.x; if(t>=40*21) return;
  int f=t/21,d=t%21;
  const float* pk=phi+((size_t)(32+h)*2048)*40;
  float a=0;
  for(int s=0;s<2048;s++){ float v= d<20? b2f(qkv[s*1920+1280+h*20+d]):1.f; a+=pk[s*40+f]*v; }
  kv[h*840+t]=rb(a);
}
__global__ void oa_kernel(const float* phi, const float* kv, u16* o){
  int idx=blockIdx.x*blockDim.x+threadIdx.x;
  if(idx>=32*2048) return;
  int s=idx%2048,h=idx/2048;
  const float* pq=phi+((size_t)h*2048+s)*40;
  float den=0; for(int f=0;f<40;f++) den+=pq[f]*kv[h*840+f*21+20];
  den=rb(rb(den)+1e-6f);
  for(int d=0;d<20;d++){ float a=0; for(int f=0;f<40;f++) a+=pq[f]*kv[h*840+f*21+d]; o[s*640+h*20+d]=f2b(rb(a)/den); }
}
__global__ void attn_kernel(const u16* qkv, u16* o){
  int idx=blockIdx.x*blockDim.x+threadIdx.x;
  if(idx>=32*2048) return;
  int s=idx%2048,h=idx/2048;
  float q[20]; for(int d=0;d<20;d++) q[d]=b2f(qkv[s*1920+h*20+d]);
  int lo=s-255<0?0:s-255;
  float mx=-1e30f;
  const float sc=0.22360679774997896f;
  for(int j=lo;j<=s;j++){ float a=0; for(int d=0;d<20;d++) a+=q[d]*b2f(qkv[j*1920+640+h*20+d]); a*=sc; mx=fmaxf(mx,a);}
  float acc[20]; for(int d=0;d<20;d++) acc[d]=0; float l=0;
  for(int j=lo;j<=s;j++){ float a=0; for(int d=0;d<20;d++) a+=q[d]*b2f(qkv[j*1920+640+h*20+d]); a*=sc; float p=__expf(a-mx); l+=p; for(int d=0;d<20;d++) acc[d]+=p*b2f(qkv[j*1920+1280+h*20+d]); }
  for(int d=0;d<20;d++) o[s*640+h*20+d]=f2b(acc[d]/l);
}
extern "C" void kernel_launch(void* const* d_in, const int* in_sizes, int n_in,
                              void* d_out, int out_size, void* d_ws, size_t ws_size,
                              hipStream_t stream) {
  const u16* Ax=(const u16*)d_in[0]; const u16* Aw=(const u16*)d_in[1]; const u16* Aphi=(const u16*)d_in[2]; const u16* Awo=(const u16*)d_in[3];
  const u16* Bx=(const u16*)d_in[4]; const u16* Bw=(const u16*)d_in[5]; const u16* Bwo=(const u16*)d_in[6];
  u16* out=(u16*)d_out;
  char* ws=(char*)d_ws;
  u16* qkvA=(u16*)ws; u16* qkvB=(u16*)(ws+(8<<20)); float* phi=(float*)(ws+(16<<20)); float* kv=(float*)(ws+(48<<20));
  u16* oA=(u16*)(ws+(50<<20)); u16* oB=(u16*)(ws+(54<<20));
  gemm_naive<<<dim3(120,128),dim3(16,16),0,stream>>>(Ax,Aw,qkvA,2048,1920,640,1920);
  gemm_naive<<<dim3(120,128),dim3(16,16),0,stream>>>(Bx,Bw,qkvB,2048,1920,640,1920);
  phi_kernel<<<2*32*2048/256,256,0,stream>>>(qkvA,Aphi,phi);
  kv_kernel<<<32,1024,0,stream>>>(qkvA,phi,kv);
  oa_kernel<<<32*2048/256,256,0,stream>>>(phi,kv,oA);
  attn_kernel<<<32*2048/64,64,0,stream>>>(qkvB,oB);
  gemm_naive<<<dim3(40,128),dim3(16,16),0,stream>>>(oA,Awo,out,2048,640,640,1280);
  gemm_naive<<<dim3(40,128),dim3(16,16),0,stream>>>(oB,Bwo,out+640,2048,640,640,1280);
}
```

```cpp
#include <hip/hip_runtime.h>
#include <hip/hip_bf16.h>

typedef unsigned short u16;
typedef unsigned u32;
typedef short bf16x8 __attribute__((ext_vector_type(8)));
typedef short s16x4 __attribute__((ext_vector_type(4)));
typedef float f32x4 __attribute__((ext_vector_type(4)));
typedef float f32x16 __attribute__((ext_vector_type(16)));
typedef float f32x2 __attribute__((ext_vector_type(2)));
typedef __bf16 bf2_t __attribute__((ext_vector_type(2)));
typedef u32 u32x4 __attribute__((ext_vector_type(4)));
typedef u32 u32x2 __attribute__((ext_vector_type(2)));
#define LAS __attribute__((address_space(3)))
#define GAS __attribute__((address_space(1)))
typedef LAS char* lds_ptr;
typedef __amdgpu_buffer_rsrc_t rsrc_t;
#define DI __device__ __forceinline__
#define RFL(x) __builtin_amdgcn_readfirstlane(x)
#define SBAR() __builtin_amdgcn_sched_barrier(0)
#define RLX_AGENT __ATOMIC_RELAXED, __HIP_MEMORY_SCOPE_AGENT
#define RLX_WG __ATOMIC_RELAXED, __HIP_MEMORY_SCOPE_WORKGROUP

constexpr int S_ = 2048, D_ = 640, N3 = 1920;
constexpr int XB = 128 * 64 * 2, WB = 64 * 240 * 2, STB = XB + WB;
constexpr int L_RAW = 3 * STB;
constexpr int L_ZERO = L_RAW + 3200;
constexpr int L_WTAB = L_RAW + 4096;
constexpr int L_ONES = 159744;
constexpr int L_MISC = 161792;
constexpr int LDS_BYTES = 163840;
constexpr int L_KV = 0, KV_HS = 384 * 80, L_Q = 4 * KV_HS;
constexpr int L_AK = 0, L_AQ = 22528, L_AV = 45056, L_PK = 69632, L_KVT = 112640;
constexpr int L_WO = 0, L_RED = 102400;
constexpr u32 W_EPOCH = 0, W_STARTED = 1024, W_KVFLAG = 4096, W_OFLAG = 32768, W_SLICE = 65536;
constexpr u32 W_PART = 1u << 20, W_KVG = 8u << 20, W_OG = 16u << 20, W_TOTAL = 24u << 20;
constexpr u32 OOB = 0x80000000u;
constexpr u32 SPIN_CAP = 1u << 18;

DI void dma16(rsrc_t r, u32 voff, u32 soff, u32 ldsaddr) {
  asm volatile("s_mov_b32 m0, %0\n\ts_nop 0\n\tbuffer_load_dwordx4 %1, %2, %3 offen lds" ::"s"(ldsaddr), "v"(voff), "s"(r), "s"(soff) : "m0", "memory");
}
DI void dma16c(rsrc_t r, u32 voff, u32 soff, u32 ldsaddr) {
  asm volatile("s_mov_b32 m0, %0\n\ts_nop 0\n\tbuffer_load_dwordx4 %1, %2, %3 offen sc1 lds" ::"s"(ldsaddr), "v"(voff), "s"(r), "s"(soff) : "m0", "memory");
}
DI s16x4 trrd(lds_ptr p) { return __builtin_bit_cast(s16x4, __builtin_amdgcn_ds_read_tr16_b64_v4i16((LAS s16x4*)p)); }
DI bf16x8 cat8(s16x4 a, s16x4 b) { return __builtin_shufflevector(a, b, 0, 1, 2, 3, 4, 5, 6, 7); }
DI u32 cvtpk(float lo, float hi) { f32x2 v = {lo, hi}; return __builtin_bit_cast(u32, __builtin_convertvector(v, bf2_t)); }
template <int N> DI void waitv() { asm volatile("s_waitcnt vmcnt(%0)" ::"i"(N) : "memory"); }
DI void lbar() { asm volatile("s_waitcnt lgkmcnt(0)\n\ts_barrier" ::: "memory"); }
DI void waitl() { asm volatile("s_waitcnt lgkmcnt(0)" ::: "memory"); }
DI bf16x8 ld128(lds_ptr p) { return *(LAS bf16x8*)p; }
DI s16x4 ld64(lds_ptr p) { return *(LAS s16x4*)p; }
#define MFMA16(a, b, c) __builtin_amdgcn_mfma_f32_16x16x32_bf16(a, b, c, 0, 0, 0)
#define MFMA32(a, b, c) __builtin_amdgcn_mfma_f32_32x32x16_bf16(a, b, c, 0, 0, 0)
#define MFMA32K8(a, b, c) __builtin_amdgcn_mfma_f32_32x32x8bf16_1k(a, b, c, 0, 0, 0)
DI float ex2(float x) { return __builtin_amdgcn_exp2f(x); }
DI u32 gload(char* ws, u32 off) { return __hip_atomic_load((GAS u32*)(ws + off), RLX_AGENT); }
DI void gstore(char* ws, u32 off, u32 v) { __hip_atomic_store((GAS u32*)(ws + off), v, RLX_AGENT); }
DI u32 lds_word(lds_ptr p) { return __hip_atomic_load((LAS u32*)p, RLX_WG); }
DI void lds_wait_ge(lds_ptr p, u32 want) {
  for (u32 s = 0; s < SPIN_CAP * 8; ++s) { if (RFL(lds_word(p)) >= want) break; __builtin_amdgcn_s_sleep(1); }
  asm volatile("" ::: "memory");
}

#define STAMP(i)

struct Frags { bf16x8 x[2]; s16x4 wl[5], wh[5]; };

__global__ __launch_bounds__(1024) void fused(const u16* xA, const u16* wA, const u16* wphi, const u16* woA,
                                              const u16* xB, const u16* wB, const u16* woB, u16* out, char* ws) {
  extern __shared__ __attribute__((aligned(16))) char smem[];
  lds_ptr lds = (lds_ptr)smem;
  const u32 lds0 = (u32)(size_t)lds;
  const int b = blockIdx.x, xcd = b & 7, idx = b >> 3;
  const int br = xcd >> 2, rt = (xcd & 3) * 4 + (idx >> 3), hg = idx & 7;
  const int lane = threadIdx.x & 63, wid = RFL(threadIdx.x >> 6);
  const int g = lane >> 4, i16 = lane & 15, q4 = i16 >> 2, p4 = lane & 3, h = lane >> 5, l32 = lane & 31;
  const rsrc_t rws = __builtin_amdgcn_make_buffer_rsrc((void*)ws, (short)0, W_TOTAL, 0x00020000);
  STAMP(0);

  if (wid < 4) {
    const u16* x = br ? xB : xA; const u16* w = br ? wB : wA;
    const rsrc_t rx = __builtin_amdgcn_make_buffer_rsrc((void*)(x + (size_t)rt * 128 * D_), (short)0, 128 * D_ * 2, 0x00020000);
    const rsrc_t rw = __builtin_amdgcn_make_buffer_rsrc((void*)w, (short)0, D_ * N3 * 2, 0x00020000);
    u32 vx[4], vw[8];
#pragma unroll
    for (int i = 0; i < 8; i++) { int c = (wid + 4 * i) * 64 + lane; int p = c / 30, cc = c - p * 30; int seg = cc / 10, c10 = cc - seg * 10;
      int k = (p & 0x33) | ((p & 4) << 1) | ((p & 8) >> 1);
      vw[i] = k * 3840 + seg * 1280 + hg * 160 + c10 * 16; }
#pragma unroll
    for (int i = 0; i < 4; i++) { int r = (wid * 4 + i) * 8 + (lane >> 3), pos = lane & 7; vx[i] = r * 1280 + ((pos ^ ((r >> 1) & 7)) << 4); }
    const bool has8 = wid < 2;
    auto issue = [&](int t, int part) {
      const u32 base = lds0 + (t % 3) * STB;
      if (part == 1) {
#pragma unroll
        for (int i = 0; i < 4; i++) dma16(rw, vw[i], 64 * 3840 * t, base + XB + (wid + 4 * i) * 1024);
      } else {
#pragma unroll
        for (int i = 0; i < 4; i++) dma16(rx, vx[i], 128 * t, base + (wid * 4 + i) * 1024);
#pragma unroll
        for (int i = 4; i < 7; i++) dma16(rw, vw[i], 64 * 3840 * t, base + XB + (wid + 4 * i) * 1024);
        if (has8) dma16(rw, vw[7], 64 * 3840 * t, base + XB + (wid + 28) * 1024);
      }
    };
    issue(0, 1);
#pragma unroll
    for (int s = 0; s < 10; s++) {
      issue(s, 2);
      if (s == 0 && br == 0) {
        const rsrc_t rp = __builtin_amdgcn_make_buffer_rsrc((void*)(wphi + hg * 1600), (short)0, 3200, 0x00020000);
        dma16(rp, wid * 1024 + lane * 16, 0, lds0 + L_RAW + wid * 1024);
      }
      if (s < 9) { issue(s + 1, 1); waitv<4>(); } else waitv<0>();
      lbar();
      if (s == 9 && br == 0) {
        const int pair = wid >> 1, T = wid & 1, rb = g & 1;
        const int fb = 2 * rb + (p4 >> 1);
        const bool okT = (T == 0) || (fb == 0);
        const int f0 = T ? 16 : 4 * fb;
        s16x4 r[5];
#pragma unroll
        for (int u = 0; u < 5; u++) {
          const int cg = (u < 4) ? 4 * (u >> 1) + 2 * h + (u & 1) : 8 + h;
          const int hc = cg >= 5, d0 = 4 * (cg - 5 * hc);
          const bool ok = okT && (hc == (p4 & 1));
          r[u] = trrd(lds + (ok ? L_RAW + (2 * pair + hc) * 800 + (d0 + q4) * 40 + f0 * 2 : L_ZERO));
        }
        lds_ptr tb = lds + L_WTAB + wid * 2560;
        *(LAS bf16x8*)(tb + lane * 16) = cat8(r[0], r[1]);
        *(LAS bf16x8*)(tb + 1024 + lane * 16) = cat8(r[2], r[3]);
        *(LAS s16x4*)(tb + 2048 + lane * 8) = r[4];
      }
    }
    if (b == 0 && wid == 3) {
      const u32 tag = RFL(lds_word(lds + L_MISC));
      for (u32 s = 0; s < SPIN_CAP; ++s) {
        const u32x4 v = __builtin_amdgcn_raw_buffer_load_b128(rws, W_STARTED + lane * 16, 0, 16);
        if (__all(v.x == tag && v.y == tag && v.z == tag && v.w == tag)) break;
        __builtin_amdgcn_s_sleep(2);
      }
      if (lane == 0) gstore(ws, W_EPOCH, tag);
    }
    lbar();
  } else {
    const int cw = wid - 4, mi = cw & 3, nj = cw >> 2;
    if (cw == 0) {
      if (lane == 0) {
        const u32 tag = gload(ws, W_EPOCH) * 0x9E3779B1u + 0x7F4A7C15u;
        gstore(ws, W_STARTED + b * 4, tag);
        *(LAS u32*)(lds + L_MISC) = tag;
      } else if (lane < 16) *(LAS u32*)(lds + L_MISC + lane * 4) = 0;
      if (lane >= 32 && lane < 36) *(LAS u32x2*)(lds + L_ONES + (lane - 32) * 640) = u32x2{0x3F803F80u, 0x3F803F80u};
    }
    const int row = mi * 32 + i16;
    const u32 xa0 = row * 128 + ((g ^ ((row >> 1) & 7)) << 4), xa1 = xa0 ^ 64;
    const u32 wa = XB + (16 * (g >> 1) + 4 * (g & 1) + q4) * 480 + 8 * p4 + nj * 160;
    f32x4 acc[2][5] = {};
    auto rd = [&](Frags& f, int t, int ks, int j) {
      lds_ptr bb = lds + (t % 3) * STB;
      if (j < 2) f.x[j] = ld128(bb + (ks ? xa1 : xa0) + j * 2048);
      else { int n = (j - 2) >> 1; if (j & 1) f.wh[n] = trrd(bb + wa + ks * 15360 + 3840 + n * 32); else f.wl[n] = trrd(bb + wa + ks * 15360 + n * 32); }
    };
    Frags F0, F1;
#pragma unroll
    for (int t = 0; t < 10; t++) {
      lbar();
#pragma unroll
      for (int j = 0; j < 12; j++) rd(F0, t, 0, j);
      SBAR();
#pragma unroll
      for (int i = 0; i < 10; i++) {
        acc[i & 1][i >> 1] = MFMA16(cat8(F0.wl[i >> 1], F0.wh[i >> 1]), F0.x[i & 1], acc[i & 1][i >> 1]);
        if (i < 6) { rd(F1, t, 1, 2 * i); rd(F1, t, 1, 2 * i + 1); }
        SBAR();
      }
#pragma unroll
      for (int i = 0; i < 10; i++) acc[i & 1][i >> 1] = MFMA16(cat8(F1.wl[i >> 1], F1.wh[i >> 1]), F1.x[i & 1], acc[i & 1][i >> 1]);
      SBAR();
    }
    lbar();
    STAMP(1);
    int base, HS, RS; float sc = 1.f;
    if (br) {
      if (nj == 0) { base = L_Q; HS = 6144; RS = 48; sc = 0.22360679774997896f * 1.4426950408889634f; }
      else { base = L_KV + 256 * 80 + (nj - 1) * 40; HS = KV_HS; RS = 80; }
    } else {
      if (nj == 2) { base = L_AV; HS = 6144; RS = 48; }
      else { base = nj ? L_AK : L_AQ; HS = 40; RS = 176; }
    }
#pragma unroll
    for (int n = 0; n < 5; n++) {
      const int c4 = 4 * n + g, hd = (c4 * 13) >> 6, d4 = c4 - 5 * hd;
      lds_ptr a = lds + base + hd * HS + row * RS + d4 * 8;
#pragma unroll
      for (int m = 0; m < 2; m++)
        *(LAS u32x2*)(a + m * 16 * RS) = u32x2{cvtpk(acc[m][n][0] * sc, acc[m][n][1] * sc), cvtpk(acc[m][n][2] * sc, acc[m][n][3] * sc)};
    }
    if (br == 0 && nj == 2) {
#pragma unroll
      for (int m = 0; m < 2; m++) *(LAS u32x2*)(lds + L_AV + g * 6144 + (row + 16 * m) * 48 + 40) = u32x2{0x3F803F80u, 0x3F803F80u};
    }
  }
  lbar();
  STAMP(2);
  const u32 tag = RFL(lds_word(lds + L_MISC));
  const u16* wo = br ? woB : woA;
  const rsrc_t rwo = __builtin_amdgcn_make_buffer_rsrc((void*)wo, (short)0, D_ * D_ * 2, 0x00020000);
  auto wo_dma = [&](int w0, int nw) {
    for (int s = w0; s < 25; s += nw) {
      const int c = s * 64 + lane, prow = c / 10, ch = c - prow * 10;
      const int p = prow & 31, kl = (p & 0x13) | ((p & 4) << 1) | ((p & 8) >> 1);
      const int kap = (prow & ~31) + kl, cidx = kap >> 3, e = kap & 7;
      const int hg2 = cidx >= 10, c1 = cidx - 10 * hg2;
      const int rr = (c1 < 8) ? (c1 >> 1) * 20 + 4 * (c1 & 1) + (e & 3) + 8 * (e >> 2) : (2 * (c1 - 8) + (e >> 2)) * 20 + 16 + (e & 3);
      const u32 vo = (hg2 * 80 + rr) * 1280 + hg * 160 + ch * 16;
#pragma unroll
      for (int rep = 0; rep < 4; rep++) dma16(rwo, vo, rep * 160 * 1280, lds0 + L_WO + rep * 25600 + s * 1024);
    }
  };
  const u32 og = W_OG + br * (128 * 20 * 1024);
  auto og_off = [&](int row, int cidx) -> u32 { return og + (((row >> 4) * 20 + (cidx >> 2)) << 10) + (((cidx & 3) * 16 + (row & 15)) << 4); };

  if (br) {
    const int hd = wid & 3, qi = wid >> 2;
    const int lo = rt == 0 ? 8 : (rt == 1 ? 4 : 0);
    lds_ptr kvb = lds + L_KV + hd * KV_HS;
    if (qi == 0 && rt < 15) {
      const u32 dst = W_KVG + ((rt * 8 + hg) * 4 + hd) * 10240 + lane * 16;
#pragma unroll
      for (int i = 0; i < 10; i++) __builtin_amdgcn_raw_buffer_store_b128(*(LAS u32x4*)(kvb + 256 * 80 + i * 1024 + lane * 16), rws, dst + i * 1024, 0, 16);
    }
    const bf16x8 qa = ld128(lds + L_Q + hd * 6144 + (32 * qi + l32) * 48 + 16 * h);
    s16x4 qb = ld64(lds + L_Q + hd * 6144 + (32 * qi + l32) * 48 + 32);
    if (h) qb = s16x4{0, 0, 0, 0};
    const int koff = l32 * 80 + 16 * h;
    const bool vones = (g & 1) && p4;
    const int vbase = vones ? L_ONES : L_KV + hd * KV_HS + (4 * h + q4) * 80 + 40 + 32 * (g & 1) + 8 * p4;
    const int vstr = vones ? 0 : 2560;
    const int u = l32 - 4 * h;
    const int ktd = 8 + qi;
    auto makeC = [&](int kt, float val) -> f32x16 {
      f32x16 c;
      if (kt == ktd) {
#pragma unroll
        for (int r = 0; r < 16; r++) c[r] = ((r & 3) + 8 * (r >> 2) <= u) ? val : -1e30f;
      } else if (kt == qi) {
#pragma unroll
        for (int r = 0; r < 16; r++) c[r] = ((r & 3) + 8 * (r >> 2) > u) ? val : -1e30f;
      } else {
#pragma unroll
        for (int r = 0; r < 16; r++) c[r] = val;
      }
      return c;
    };
    auto scores = [&](int kt, const f32x16& c) -> f32x16 {
      lds_ptr kp = kvb + kt * 2560 + koff;
      const bf16x8 ka = ld128(kp); const s16x4 kb = ld64(kp + 32 - 8 * h);
      f32x16 s = MFMA32(ka, qa, c);
      return MFMA32K8(kb, qb, s);
    };
    auto maxrange = [&](int hi, int lo_) -> float {
      float m = -1e30f;
      for (int kt = hi; kt >= lo_; --kt) {
        const f32x16 s = scores(kt, makeC(kt, 0.f));
#pragma unroll
        for (int r = 0; r < 16; r += 2) m = fmaxf(fmaxf(m, s[r]), s[r + 1]);
      }
      auto rr = __builtin_amdgcn_permlane32_swap(__float_as_uint(m), __float_as_uint(m), false, false);
      return fmaxf(__uint_as_float(rr[0]), __uint_as_float(rr[1]));
    };
    auto tile = [&](int kt, const f32x16& c, f32x16& o) {
      lds_ptr vp = lds + vbase + kt * vstr;
      const f32x16 s = scores(kt, c);
      const bf16x8 v0 = cat8(trrd(vp), trrd(vp + 640)), v1 = cat8(trrd(vp + 1280), trrd(vp + 1920));
      u32x4 p0, p1;
#pragma unroll
      for (int j = 0; j < 4; j++) { p0[j] = cvtpk(ex2(s[2 * j]), ex2(s[2 * j + 1])); p1[j] = cvtpk(ex2(s[8 + 2 * j]), ex2(s[8 + 2 * j + 1])); }
      o = MFMA32(v0, __builtin_bit_cast(bf16x8, p0), o);
      o = MFMA32(v1, __builtin_bit_cast(bf16x8, p1), o);
    };
    auto run = [&](int hi, int lo_, float m) -> f32x16 {
      f32x16 o = {};
      int kt = hi;
      if (kt == ktd) { tile(kt, makeC(kt, -m), o); --kt; }
      const int le = (lo_ == qi) ? lo_ + 1 : lo_;
      { const f32x16 c = makeC(-1, -m);
        for (; kt >= le; --kt) tile(kt, c, o); }
      if (lo_ == qi) tile(qi, makeC(qi, -m), o);
      return o;
    };
    float m = maxrange(ktd, ktd);
    f32x16 o = run(ktd, 8, m);
    if (__any(!((h ? o[8] : o[12]) < 1.8e19f))) { m = maxrange(ktd, 8); o = run(ktd, 8, m); }
    STAMP(3);
    if (qi == 0) {
      if (rt < 15) { waitv<0>(); if (lane == 0) gstore(ws, W_KVFLAG + (rt * 8 + hg) * 128 + hd * 4, tag); }
      STAMP(4);
      for (int d = 1; d <= 2; d++) {
        if (rt - d < 0) break;
        const u32 fo = W_KVFLAG + ((rt - d) * 8 + hg) * 128 + hd * 4;
        for (u32 s = 0; s < SPIN_CAP; ++s) { if (RFL(gload(ws, fo)) == tag) break; __builtin_amdgcn_s_sleep(1); }
        asm volatile("" ::: "memory");
        const u32 src = W_KVG + (((rt - d) * 8 + hg) * 4 + hd) * 10240;
#pragma unroll
        for (int i = 0; i < 10; i++) dma16c(rws, lane * 16, src + i * 1024, lds0 + L_KV + hd * KV_HS + (2 - d) * 10240 + i * 1024);
      }
      waitv<0>();
      STAMP(5);
      if (lane == 0) __hip_atomic_store((LAS u32*)(lds + L_MISC + 16 + hd * 4), 1u, RLX_WG);
    } else lds_wait_ge(lds + L_MISC + 16 + hd * 4, 1u);
    if (lo < 8) {
      const int l2 = lo > qi ? lo : qi;
      f32x16 o2 = run(7, l2, m);
      if (__any(!((h ? o2[8] : o2[12]) < 1.8e19f))) {
        const float m2 = fmaxf(m, maxrange(7, l2)); const float f = ex2(m - m2);
#pragma unroll
        for (int r = 0; r < 16; r++) o[r] *= f;
        o2 = run(7, l2, m2);
      }
#pragma unroll
      for (int r = 0; r < 16; r++) o[r] += o2[r];
    }
    const float rl = __builtin_amdgcn_rcpf(h ? o[8] : o[12]);
    const int row = rt * 128 + 32 * qi + l32;
    const u32x4 w0 = {cvtpk(o[0] * rl, o[1] * rl), cvtpk(o[2] * rl, o[3] * rl), cvtpk(o[4] * rl, o[5] * rl), cvtpk(o[6] * rl, o[7] * rl)};
    const u32x2 w1 = {cvtpk(o[8] * rl, o[9] * rl), cvtpk(o[10] * rl, o[11] * rl)};
    __builtin_amdgcn_raw_buffer_store_b128(w0, rws, og_off(row, hg * 10 + 2 * hd + h), 0, 16);
    __builtin_amdgcn_raw_buffer_store_b64(w1, rws, h ? OOB : og_off(row, hg * 10 + 8 + (hd >> 1)) + 8 * (hd & 1), 0, 16);
    STAMP(6);
    waitv<0>();
    lbar();
    if (threadIdx.x == 0) gstore(ws, W_OFLAG + (br * 16 + rt) * 128 + hg * 4, tag);
    wo_dma(wid, 16);
  } else {
    const int tg = wid & 3, pair = (wid >> 2) & 1, isq = wid >> 3;
    if (!isq) __builtin_amdgcn_s_setprio(2);
    u32 phi[20];
    {
      lds_ptr ip = lds + (isq ? L_AQ : L_AK) + (32 * tg + l32) * 176 + pair * 80;
      const bf16x8 b0 = ld128(ip + 16 * h), b1 = ld128(ip + 32 + 16 * h); const s16x4 b2 = ld64(ip + 64 + 8 * h);
      lds_ptr tb = lds + L_WTAB + pair * 5120;
      f32x16 t0 = {}, t1 = {};
      t0 = MFMA32(ld128(tb + lane * 16), b0, t0);        t1 = MFMA32(ld128(tb + 2560 + lane * 16), b0, t1);
      t0 = MFMA32(ld128(tb + 1024 + lane * 16), b1, t0); t1 = MFMA32(ld128(tb + 2560 + 1024 + lane * 16), b1, t1);
      t0 = MFMA32K8(ld64(tb + 2048 + lane * 8), b2, t0); t1 = MFMA32K8(ld64(tb + 2560 + 2048 + lane * 8), b2, t1);
      float p[20];
#pragma unroll
      for (int j = 0; j < 10; j++) {
        const u32 w = j < 8 ? cvtpk(t0[2 * j], t0[2 * j + 1]) : cvtpk(t1[2 * j - 16], t1[2 * j - 15]);
        p[2 * j] = __uint_as_float(w << 16); p[2 * j + 1] = __uint_as_float(w & 0xffff0000u);
      }
      float mx = fmaxf(p[0], p[1]), mn = fminf(p[0], p[1]);
#pragma unroll
      for (int j = 2; j < 20; j += 2) { mx = fmaxf(fmaxf(mx, p[j]), p[j + 1]); mn = fminf(fminf(mn, p[j]), p[j + 1]); }
      const float L2E = 1.4426950408889634f; const float a0 = -mx * L2E, a1 = mn * L2E;
      float ep[20], en[20], sp = 0.f, sn = 0.f;
#pragma unroll
      for (int j = 0; j < 20; j++) { ep[j] = ex2(fmaf(p[j], L2E, a0)); en[j] = ex2(fmaf(p[j], -L2E, a1)); sp += ep[j]; sn += en[j]; }
      const float rp = __builtin_amdgcn_rcpf(sp), rn = __builtin_amdgcn_rcpf(sn);
#pragma unroll
      for (int j = 0; j < 10; j++) { phi[j] = cvtpk(ep[2 * j] * rp, ep[2 * j + 1] * rp); phi[10 + j] = cvtpk(en[2 * j] * rn, en[2 * j + 1] * rn); }
    }
    STAMP(3);
    if (!isq) {
      lds_ptr pk = lds + L_PK + (2 * pair + h) * 10240 + (32 * tg + l32) * 80;
#pragma unroll
      for (int j = 0; j < 5; j++) *(LAS u32x4*)(pk + 16 * j) = u32x4{phi[4 * j], phi[4 * j + 1], phi[4 * j + 2], phi[4 * j + 3]};
      waitl();
      if (lane == 0) __hip_atomic_fetch_add((LAS u32*)(lds + L_MISC + 16 + pair * 4), 1u, RLX_WG);
      lds_wait_ge(lds + L_MISC + 16 + pair * 4, 4u);
      const int hd = 2 * pair + (tg >> 1), dt = tg & 1;
      lds_ptr pa = lds + L_PK + hd * 10240 + (8 * g + q4) * 80 + 8 * p4;
      lds_ptr va = lds + L_AV + hd * 6144 + (8 * g + q4) * 48 + 32 * dt + 8 * p4;
      f32x4 kv[3] = {};
#pragma unroll
      for (int ks = 0; ks < 4; ks++) {
        const bf16x8 vb = cat8(trrd(va + ks * 32 * 48), trrd(va + ks * 32 * 48 + 4 * 48));
#pragma unroll
        for (int T = 0; T < 3; T++) kv[T] = MFMA16(cat8(trrd(pa + ks * 2560 + T * 32), trrd(pa + ks * 2560 + 320 + T * 32)), vb, kv[T]);
      }
      const int d = 16 * dt + i16;
      const u32 pbase = W_PART + ((hg * 4 + hd) * 16 + rt) * 6720 + (d * 40 + 4 * g) * 8;
#pragma unroll
      for (int T = 0; T < 3; T++) {
        const u32 o = (d <= 20 && (T < 2 || g < 2)) ? pbase + T * 128 : OOB;
        __builtin_amdgcn_raw_buffer_store_b128(u32x4{__float_as_uint(kv[T][0]), tag, __float_as_uint(kv[T][1]), tag}, rws, o, 0, 16);
        __builtin_amdgcn_raw_buffer_store_b128(u32x4{__float_as_uint(kv[T][2]), tag, __float_as_uint(kv[T][3]), tag}, rws, o + 16, 0, 16);
      }
      STAMP(4);
      if (wid < 2) {
        const int i = wid * 53 + lane; const bool act = lane < 53 && i < 105;
        const u32 src = act ? W_PART + (hg * 4 + (rt >> 2)) * 16 * 6720 + ((rt & 3) * 210 + 2 * i) * 8 : OOB;
        float s0 = 0.f, s1 = 0.f;
        for (u32 s = 0; s < SPIN_CAP; ++s) {
          bool ok = true; s0 = 0.f; s1 = 0.f;
          u32x4 v[16];
#pragma unroll
          for (int r = 0; r < 16; r++) v[r] = __builtin_amdgcn_raw_buffer_load_b128(rws, src, r * 6720, 16);
#pragma unroll
          for (int r = 0; r < 16; r++) { ok &= (v[r].y == tag) & (v[r].w == tag); s0 += __uint_as_float(v[r].x); s1 += __uint_as_float(v[r].z); }
          if (__all(ok || !act)) break;
          __builtin_amdgcn_s_sleep(1);
        }
        __builtin_amdgcn_raw_buffer_store_b64(u32x2{cvtpk(s0, s1), tag}, rws, lane < 53 ? W_SLICE + (hg * 16 + rt) * 1024 + i * 8 : OOB, 0, 16);
        STAMP(5);
      } else if (wid >= 4) {
        const int hh = wid - 4; const bool act = lane < 53;
        const u32 src = act ? W_SLICE + (hg * 16 + hh * 4) * 1024 + lane * 16 : OOB;
        u32x4 v[4];
        for (u32 s = 0; s < SPIN_CAP; ++s) {
          bool ok = true;
#pragma unroll
          for (int r = 0; r < 4; r++) v[r] = __builtin_amdgcn_raw_buffer_load_b128(rws, src, r * 1024, 16);
#pragma unroll
          for (int r = 0; r < 4; r++) ok &= (v[r].y == tag) & (v[r].w == tag);
          if (__all(ok || !act)) break;
          __builtin_amdgcn_s_sleep(1);
        }
        if (act) {
#pragma unroll
          for (int r = 0; r < 4; r++) {
            LAS u32* dp = (LAS u32*)(lds + L_KVT + hh * 1680 + (r * 105 + 2 * lane) * 4);
            dp[0] = v[r].x; if (lane < 52) dp[1] = v[r].z;
          }
        }
        STAMP(5);
      }
      __builtin_amdgcn_s_setprio(0);
    }
    lbar();
    STAMP(6);
    if (!isq) wo_dma(wid, 8);
    else {
      const int hp = (l32 >> 2) & 1, r = (l32 & 3) + 4 * (l32 >> 3);
      lds_ptr a0 = lds + ((h == hp) ? L_KVT + (2 * pair + h) * 1680 + r * 80 : L_ZERO);
      lds_ptr a1 = lds + ((h == hp && r <= 4) ? L_KVT + (2 * pair + h) * 1680 + (16 + r) * 80 : L_ZERO);
      f32x16 t0 = {}, t1 = {};
#pragma unroll
      for (int j = 0; j < 5; j++) {
        const bf16x8 bq = __builtin_bit_cast(bf16x8, u32x4{phi[4 * j], phi[4 * j + 1], phi[4 * j + 2], phi[4 * j + 3]});
        t0 = MFMA32(ld128(a0 + 16 * j), bq, t0); t1 = MFMA32(ld128(a1 + 16 * j), bq, t1);
      }
      const float rl = __builtin_amdgcn_rcpf(t1[4] + 1e-6f);
      const int row = rt * 128 + 32 * tg + l32, hd = 2 * pair + h;
      const u32x4 w0 = {cvtpk(t0[0] * rl, t0[1] * rl), cvtpk(t0[2] * rl, t0[3] * rl), cvtpk(t0[8] * rl, t0[9] * rl), cvtpk(t0[10] * rl, t0[11] * rl)};
      const u32x4 w1 = {cvtpk(t0[4] * rl, t0[5] * rl), cvtpk(t0[6] * rl, t0[7] * rl), cvtpk(t0[12] * rl, t0[13] * rl), cvtpk(t0[14] * rl, t0[15] * rl)};
      const u32x2 w2 = {cvtpk(t1[0] * rl, t1[1] * rl), cvtpk(t1[2] * rl, t1[3] * rl)};
      __builtin_amdgcn_raw_buffer_store_b128(w0, rws, og_off(row, hg * 10 + 2 * hd), 0, 16);
      __builtin_amdgcn_raw_buffer_store_b128(w1, rws, og_off(row, hg * 10 + 2 * hd + 1), 0, 16);
      __builtin_amdgcn_raw_buffer_store_b64(w2, rws, og_off(row, hg * 10 + 8 + pair) + 8 * h, 0, 16);
      waitv<0>();
      if (lane == 0) {
        const u32 old = __hip_atomic_fetch_add((LAS u32*)(lds + L_MISC + 32), 1u, RLX_WG);
        if (old == 7) gstore(ws, W_OFLAG + (br * 16 + rt) * 128 + hg * 4, tag);
      }
    }
  }
  STAMP(7);
  if (wid == 15) {
    for (u32 s = 0; s < SPIN_CAP; ++s) {
      const u32 v = gload(ws, W_OFLAG + (br * 16 + rt) * 128 + (lane & 7) * 4);
      if (__all(v == tag)) break;
      __builtin_amdgcn_s_sleep(1);
    }
  }
  lbar();
  STAMP(8);
  {
    const int m = wid & 7, kh = wid >> 3;
    u32x4 of[10];
#pragma unroll
    for (int s = 0; s < 10; s++) of[s] = __builtin_amdgcn_raw_buffer_load_b128(rws, og + (((rt * 8 + m) * 20 + kh * 10) << 10) + lane * 16, s * 1024, 16);
    asm volatile("s_waitcnt vmcnt(10)\n\ts_barrier" ::: "memory");
    STAMP(9);
    lds_ptr wp = lds + L_WO + kh * 51200 + (16 * (g >> 1) + 4 * (g & 1) + q4) * 160 + 8 * p4;
    f32x4 acc[5] = {};
#pragma unroll
    for (int s = 0; s < 10; s++) {
#pragma unroll
      for (int n = 0; n < 5; n++)
        acc[n] = MFMA16(cat8(trrd(wp + s * 5120 + n * 32), trrd(wp + s * 5120 + 1280 + n * 32)), __builtin_bit_cast(bf16x8, of[s]), acc[n]);
    }
    lds_ptr rp = lds + L_RED + m * 5120 + lane * 16;
    if (kh) {
#pragma unroll
      for (int n = 0; n < 3; n++) *(LAS f32x4*)(rp + n * 1024) = acc[n];
    } else {
#pragma unroll
      for (int n = 3; n < 5; n++) *(LAS f32x4*)(rp + n * 1024) = acc[n];
    }
    lbar();
    u16* op = out + (size_t)(rt * 128 + m * 16 + i16) * 1280 + br * 640 + hg * 80 + 4 * g;
    if (kh) {
#pragma unroll
      for (int n = 3; n < 5; n++) { const f32x4 v = *(LAS f32x4*)(rp + n * 1024) + acc[n]; *(GAS u32x2*)(op + n * 16) = u32x2{cvtpk(v[0], v[1]), cvtpk(v[2], v[3])}; }
    } else {
#pragma unroll
      for (int n = 0; n < 3; n++) { const f32x4 v = acc[n] + *(LAS f32x4*)(rp + n * 1024); *(GAS u32x2*)(op + n * 16) = u32x2{cvtpk(v[0], v[1]), cvtpk(v[2], v[3])}; }
    }
  }
  STAMP(10);
}

extern "C" void kernel_launch(void* const* d_in, const int* in_sizes, int n_in, void* d_out, int out_size, void* d_ws, size_t ws_size, hipStream_t stream) {
  hipFuncSetAttribute((const void*)fused, hipFuncAttributeMaxDynamicSharedMemorySize, LDS_BYTES);
  fused<<<256, 1024, LDS_BYTES, stream>>>((const u16*)d_in[0], (const u16*)d_in[1], (const u16*)d_in[2], (const u16*)d_in[3],
                                          (const u16*)d_in[4], (const u16*)d_in[5], (const u16*)d_in[6], (u16*)d_out, (char*)d_ws);
}
```

```cpp
#include <hip/hip_runtime.h>
#include <hip/hip_bf16.h>

typedef unsigned short u16;
typedef unsigned u32;
typedef short bf16x8 __attribute__((ext_vector_type(8)));
typedef short s16x4 __attribute__((ext_vector_type(4)));
typedef float f32x4 __attribute__((ext_vector_type(4)));
typedef float f32x16 __attribute__((ext_vector_type(16)));
typedef float f32x2 __attribute__((ext_vector_type(2)));
typedef __bf16 bf2_t __attribute__((ext_vector_type(2)));
typedef u32 u32x4 __attribute__((ext_vector_type(4)));
typedef u32 u32x2 __attribute__((ext_vector_type(2)));
#define LAS __attribute__((address_space(3)))
#define GAS __attribute__((address_space(1)))
typedef LAS char* lds_ptr;
typedef __amdgpu_buffer_rsrc_t rsrc_t;
#define DI __device__ __forceinline__
#define RFL(x) __builtin_amdgcn_readfirstlane(x)
#define SBAR() __builtin_amdgcn_sched_barrier(0)
#define PIN(x) asm volatile("" : "+v"(x))
#define RLX_AGENT __ATOMIC_RELAXED, __HIP_MEMORY_SCOPE_AGENT
#define RLX_WG __ATOMIC_RELAXED, __HIP_MEMORY_SCOPE_WORKGROUP
#define XMAP 0
#define HB 6
#define RPM ((12 + HB - 2) / (HB - 1))
#define S1DELAY 8
#define ROT 4

constexpr int S_ = 2048, D_ = 640, N3 = 1920;
constexpr int XB = 128 * 64 * 2, WB = 64 * 240 * 2, STB = XB + WB;
constexpr int L_RAW = 3 * STB;
constexpr int L_ZERO = L_RAW + 3200;
constexpr int L_WTAB = L_RAW + 4096;
constexpr int L_MASK = 158720;
constexpr int L_ONES = 160768;
constexpr int L_MISC = L_ONES + 64;
constexpr int L_DUMMY = 162816;
constexpr int LDS_BYTES = 163840;
constexpr int L_HALO = 0, L_OWN = 81920, L_Q = 122880, LB_WO = 56320, L_RED = 0;
constexpr int L_PK = 0, L_AV = 47104, L_AK = 71680, L_AQ = L_AK + 22528, L_KVT = L_WTAB, LA_WO = 40960;
constexpr int M_TAG = 0, M_C0 = 16, M_C1 = 32, M_C2 = 36, M_C3 = 40, M_C4 = 44, M_C5 = 48, M_C6 = 52, M_C7 = 56, M_C9 = 60, M_WR = 64, M_C8 = 112, M_WV = 116;
constexpr u32 W_EPOCH = 0, W_STARTED = 1024, W_KVFLAG = 4096, W_OFLAG = 32768, W_SLICE = 65536;
constexpr u32 W_PART = 1u << 20, W_KVG = 8u << 20, W_OG = 16u << 20, W_TOTAL = 24u << 20;
constexpr u32 PARTB = 3456;
constexpr u32 OOB = 0x80000000u;
constexpr u32 SPIN_CAP = 1u << 18;

DI void dma16(rsrc_t r, u32 voff, u32 soff, u32 ldsaddr) {
  asm volatile("s_mov_b32 m0, %0\n\ts_nop 0\n\tbuffer_load_dwordx4 %1, %2, %3 offen lds" ::"s"(ldsaddr), "v"(voff), "s"(r), "s"(soff) : "m0", "memory");
}
DI void dma16c(rsrc_t r, u32 voff, u32 soff, u32 ldsaddr) {
  asm volatile("s_mov_b32 m0, %0\n\ts_nop 0\n\tbuffer_load_dwordx4 %1, %2, %3 offen sc1 lds" ::"s"(ldsaddr), "v"(voff), "s"(r), "s"(soff) : "m0", "memory");
}
DI s16x4 trrd(lds_ptr p) { return __builtin_bit_cast(s16x4, __builtin_amdgcn_ds_read_tr16_b64_v4i16((LAS s16x4*)p)); }
DI bf16x8 cat8(s16x4 a, s16x4 b) { return __builtin_shufflevector(a, b, 0, 1, 2, 3, 4, 5, 6, 7); }
DI u32 cvtpk2(f32x2 v) { return __builtin_bit_cast(u32, __builtin_convertvector(v, bf2_t)); }
DI u32 cvtpk(float lo, float hi) { return cvtpk2(f32x2{lo, hi}); }
DI float blo(u32 w) { return __uint_as_float(w << 16); }
DI float bhi(u32 w) { return __uint_as_float(w & 0xffff0000u); }
template <int N> DI void waitv() { asm volatile("s_waitcnt vmcnt(%0)" ::"i"(N) : "memory"); }
DI void lbar() { asm volatile("s_waitcnt lgkmcnt(0)\n\ts_barrier" ::: "memory"); }
DI void waitl() { asm volatile("s_waitcnt lgkmcnt(0)" ::: "memory"); }
DI bf16x8 ld128(lds_ptr p) { return *(LAS bf16x8*)p; }
DI s16x4 ld64(lds_ptr p) { return *(LAS s16x4*)p; }
#define MFMA16(a, b, c) __builtin_amdgcn_mfma_f32_16x16x32_bf16(a, b, c, 0, 0, 0)
#define MFMA32(a, b, c) __builtin_amdgcn_mfma_f32_32x32x16_bf16(a, b, c, 0, 0, 0)
#define MFMA32K8(a, b, c) __builtin_amdgcn_mfma_f32_32x32x8bf16_1k(a, b, c, 0, 0, 0)
DI float ex2(float x) { return __builtin_amdgcn_exp2f(x); }
DI u32 gload(char* ws, u32 off) { return __hip_atomic_load((GAS u32*)(ws + off), RLX_AGENT); }
DI void gstore(char* ws, u32 off, u32 v) { __hip_atomic_store((GAS u32*)(ws + off), v, RLX_AGENT); }
DI u32 lds_word(lds_ptr p) { return __hip_atomic_load((LAS u32*)p, RLX_WG); }
DI void lds_inc(lds_ptr p, int lane) { if (lane == 0) asm volatile("ds_add_u32 %0, %1" ::"v"((u32)(size_t)p), "v"(1u) : "memory"); }
DI u32 lds_add_ret(lds_ptr p) { return __hip_atomic_fetch_add((LAS u32*)p, 1u, RLX_WG); }
DI void lds_wait_ge(lds_ptr p, u32 want) {
#pragma nounroll
  for (u32 s = 0; s < SPIN_CAP * 8; ++s) { if (RFL(lds_word(p)) >= want) break; __builtin_amdgcn_s_sleep(1); }
  asm volatile("" ::: "memory");
}
DI void poll_tag(char* ws, u32 off, u32 tag, bool done) {
#pragma nounroll
  for (u32 s = 0; s < SPIN_CAP; ++s) { const u32 v = gload(ws, off); if (__all(done || v == tag)) break; __builtin_amdgcn_s_sleep(1); }
  asm volatile("" ::: "memory");
}

#define STAMP(i)
#define STAMPV(i)

struct Frags { bf16x8 x[2]; s16x4 wl[5], wh[5]; };

__global__ __launch_bounds__(1024) void fused(const u16* xA, const u16* wA, const u16* wphi, const u16* woA,
                                              const u16* xB, const u16* wB, const u16* woB, u16* out, char* ws) {
  extern __shared__ __attribute__((aligned(16))) char smem[];
  lds_ptr lds = (lds_ptr)smem;
  const u32 lds0 = (u32)(size_t)lds;
  const int b = blockIdx.x, xcd = b & 7, idx = b >> 3;
  const int br = xcd >> 2, rt = (xcd & 3) * 4 + (idx >> 3), hg = idx & 7;
  const int lane = threadIdx.x & 63, wid = RFL(threadIdx.x >> 6);
  const int g = lane >> 4, i16 = lane & 15, q4 = i16 >> 2, p4 = lane & 3, h = lane >> 5, l32 = lane & 31;
  const rsrc_t rws = __builtin_amdgcn_make_buffer_rsrc((void*)ws, (short)0, W_TOTAL, 0x00020000);
  lds_ptr misc = lds + L_MISC;
  STAMP(0);

  const rsrc_t rx = __builtin_amdgcn_make_buffer_rsrc((void*)((br ? xB : xA) + (size_t)rt * 128 * D_), (short)0, 128 * D_ * 2, 0x00020000);
  const rsrc_t rw = __builtin_amdgcn_make_buffer_rsrc((void*)(br ? wB : wA), (short)0, D_ * N3 * 2, 0x00020000);
  const int cls = ((idx >> 3) + idx) & 3;
  const unsigned long long ord = cls == 0 ? 0x9768532410ull : cls == 1 ? 0x8796435201ull : cls == 2 ? 0x9875463102ull : 0x9865742130ull;
  const int rot = (int)(ord & 15);
  const u32 vx0 = (lane >> 3) * 1280 + (((lane & 7) ^ (lane >> 4)) << 4), vx1 = vx0 ^ 64;
  const int wc = lane >= 30 ? lane - 30 : lane;
  const u32 vw = (lane >= 30 ? 3840 : 0) + (wc >= 20 ? 2240 : wc >= 10 ? 1120 : 0) + hg * 160 + wc * 16;
  auto ldx = [&](int i, int kt, u32 buf) { dma16(rx, (i & 1) ? vx1 : vx0, 128 * kt + i * 8 * 1280, buf + i * 1024); };
  auto ldw1 = [&](int j, int kt, u32 buf) { const int p = 2 * j, k = (p & 0x33) | ((p & 4) << 1) | ((p & 8) >> 1); dma16(rw, vw, (64 * kt + k) * 3840, buf + XB + j * 960); };
  const u32 og = W_OG + br * (128 * 20480);
  u32 pc[9] = {};
  auto pre = [&]() {
    auto og_main = [&](int row, int hd, int hf) -> u32 { return og + (row >> 4) * 20480 + ((hg * 2 + (hd >> 1)) << 10) + (((2 * (hd & 1) + hf) * 16 + (row & 15)) << 4); };
    auto og_tail = [&](int row, int hd) -> u32 { return og + (row >> 4) * 20480 + 16384 + hg * 512 + ((hd * 16 + (row & 15)) << 3); };
    if (br) {
      const int hd = wid & 3, qi = wid >> 2, u = 35 - l32 + 4 * h; const bool vones = (g & 1) && p4;
      pc[0] = L_Q + hd * 6144 + (32 * qi + l32) * 48 + 16 * h;
      pc[1] = l32 * 80 + 16 * h;
      pc[2] = h ? L_ONES : l32 * 80 + 32;
      pc[3] = vones ? L_ONES : (4 * h + q4) * 80 + 40 + 32 * (g & 1) + 8 * p4;
      pc[4] = h ? 0 : 2560; pc[5] = vones ? 0 : 2560;
      pc[6] = L_MASK + (u & 3) * 256 + (u & ~3) * 4;
      pc[7] = og_main(rt * 128 + 32 * qi + l32, hd, h);
      pc[8] = h ? OOB : og_tail(rt * 128 + 32 * qi + l32, hd);
    } else {
      const int tg = wid & 3, pair = (wid >> 2) & 1, isq = wid >> 3;
      pc[0] = (isq ? L_AQ : L_AK) + (32 * tg + l32) * 176 + pair * 80 + 16 * h;
      pc[1] = L_WTAB + pair * 5120 + lane * 16;
      pc[2] = L_WTAB + pair * 5120 + 2048 + lane * 8;
      if (isq) {
        const int hp = (l32 >> 2) & 1, r = (l32 & 3) + 4 * (l32 >> 3);
        pc[3] = (h == hp) ? L_KVT + (2 * pair + h) * 1680 + r * 80 : L_ZERO;
        pc[4] = (h == hp && r <= 4) ? L_KVT + (2 * pair + h) * 1680 + (16 + r) * 80 : L_ZERO;
        pc[5] = og_main(rt * 128 + 32 * tg + l32, 2 * pair + h, 0);
        pc[6] = og_tail(rt * 128 + 32 * tg + l32, 2 * pair + h);
        pc[7] = 0;
      } else {
        const int hd = 2 * pair + (tg >> 1), dt = tg & 1;
        pc[3] = L_AV + hd * 6144 + (8 * g + q4) * 48 + 32 * dt + 8 * p4;
        pc[4] = L_PK + (2 * pair + h) * 10240 + (32 * tg + l32) * 80;
        pc[5] = L_PK + hd * 10240 + (8 * g + q4) * 80 + 8 * p4;
        pc[6] = (!dt || i16 <= 4) ? W_PART + ((hg * 4 + hd) * 16 + rt) * PARTB + (dt * 160 + (dt ? g * 5 + i16 : lane)) * 16 : OOB;
        pc[7] = g < 2 ? pc[6] : OOB;
      }
      pc[8] = pc[0] + 64 - 8 * h;
    }
#pragma unroll
    for (int i = 0; i < 9; i++) PIN(pc[i]);
  };
  if (wid < 4) {
    __builtin_amdgcn_s_setprio(3);
    u32 ep = 0;
    if (wid == 3) ep = gload(ws, W_EPOCH);
    if (wid == 2) *(LAS u32*)(misc + lane * 4) = 0;
    int kt = rot, bi = 0;
    __builtin_amdgcn_s_sleep(S1DELAY);
#pragma nounroll
    for (int s = 1; s < 10; s++) {
      bi = bi == 2 ? 0 : bi + 1; kt = (int)(ord >> (4 * s)) & 15;
      const u32 buf = lds0 + bi * STB;
      if (s == 2 && br == 0) {
        const rsrc_t rp = __builtin_amdgcn_make_buffer_rsrc((void*)(wphi + hg * 1600), (short)0, 3200, 0x00020000);
        dma16(rp, wid * 1024 + lane * 16, 0, lds0 + L_RAW + wid * 1024);
      }
#pragma unroll
      for (int i = 0; i < 4; i++) ldx(wid * 4 + i, kt, buf);
      if (s == 1) lbar();
      if (lane < 60) {
#pragma unroll
        for (int j = 0; j < 8; j++) ldw1(wid * 8 + j, kt, buf);
      }
      if (s > 1) {
        waitv<12>();
        __builtin_amdgcn_s_barrier();
      }
      if (s == 9) pre();
      if (s == 9 && br == 0) {
        const int pair = wid >> 1, T = wid & 1, rb = g & 1;
        const int fb = 2 * rb + (p4 >> 1);
        const bool okT = (T == 0) || (fb == 0);
        const int f0 = T ? 16 : 4 * fb;
        s16x4 r[5];
#pragma unroll
        for (int u = 0; u < 5; u++) {
          const int cg = (u < 4) ? 4 * (u >> 1) + 2 * h + (u & 1) : 8 + h;
          const int hc = cg >= 5, d0 = 4 * (cg - 5 * hc);
          const bool ok = okT && (hc == (p4 & 1));
          r[u] = trrd(lds + (ok ? L_RAW + (2 * pair + hc) * 800 + (d0 + q4) * 40 + f0 * 2 : L_ZERO));
        }
        lds_ptr tb = lds + L_WTAB + wid * 2560;
        *(LAS bf16x8*)(tb + lane * 16) = cat8(r[0], r[1]);
        *(LAS bf16x8*)(tb + 1024 + lane * 16) = cat8(r[2], r[3]);
        *(LAS s16x4*)(tb + 2048 + lane * 8) = r[4];
      }
      STAMPV(32 + s - 1);
    }
    {
      const rsrc_t rwo = __builtin_amdgcn_make_buffer_rsrc((void*)(br ? woB : woA), (short)0, D_ * D_ * 2, 0x00020000);
      waitv<0>();
      if (wid == 3 && lane == 0) *(LAS u32*)(misc + M_TAG) = ep * 0x9E3779B1u + 0x7F4A7C15u;
      lbar();
      STAMP(15);
#pragma nounroll
      for (int j = 0; j < 5; j++) {
        const int i = wid * 5 + j;
        const u32 o_ = i < 16 ? og + (rt * 8 + (i >> 1)) * 20480 + hg * 2048 + (i & 1) * 1024 + lane * 16
                              : og + (rt * 8 + 2 * (i - 16) + h) * 20480 + 16384 + hg * 512 + l32 * 16;
        __builtin_amdgcn_raw_buffer_store_b128(u32x4{~0u, ~0u, ~0u, ~0u}, rws, o_, 0, 16);
      }
#pragma unroll
      for (int i = 0; i < 7; i++) dma16(rwo, lane * 16, (idx * 25 + (wid + 4 * i < 25 ? wid + 4 * i : 24)) * 1024, lds0 + L_DUMMY);
    }
    if (br) {
      lds_wait_ge(misc + M_WR + 16 + wid * 4, 1u); lds_wait_ge(misc + M_WR + 32 + wid * 4, 1u);
      waitv<7>();
      if (lane == 0 && lds_add_ret(misc + M_C4) == 3) gstore(ws, W_STARTED + ((br * 16 + rt) * 8 + hg) * 4, lds_word(misc + M_TAG));
      if (wid < 2) __builtin_amdgcn_s_sleep(6);
      const u32 dst = W_KVG + ((rt * 8 + hg) * 4 + wid) * 10240 + lane * 16;
#pragma unroll
      for (int i = 0; i < 10; i++) __builtin_amdgcn_raw_buffer_store_b128(*(LAS u32x4*)(lds + L_OWN + wid * 10240 + i * 1024 + lane * 16), rws, dst, i * 1024, 16);
      const u32 tag = RFL(lds_word(misc + M_TAG)), kvf = W_KVFLAG + (rt * 8 + hg) * 128;
      waitv<0>();
      if (lane == 0) gstore(ws, kvf + wid * 4, tag);
      STAMP(4);
      const int dd = 2 - (wid >> 1);
      lds_wait_ge(misc + M_C8, 12u);
#pragma nounroll
      for (int hf = 1; hf >= 0; --hf) {
        const int tl = 2 * hf + (wid & 1);
        if (rt >= dd) poll_tag(ws, kvf - dd * 1024 + tl * 4, tag, false);
        if (hf) { STAMP(11); } else { STAMP(5); lds_inc(misc + M_C0, lane); }
        if (rt >= dd) {
          const u32 src = W_KVG + (((rt - dd) * 8 + hg) * 4 + tl) * 10240;
#pragma nounroll
          for (int i = 0; i < 10; i++) dma16c(rws, lane * 16, src + i * 1024, lds0 + L_HALO + hf * 40960 + wid * 10240 + i * 1024);
        }
      }
    }
  } else {
    const int cw = wid - 4, mi = cw & 3, nj = cw < 8 ? (cw >> 2) + 1 : 0;
    if (cw < 4) {
#pragma unroll
      for (int i = 0; i < 4; i++) ldx(cw * 4 + i, rot, lds0);
    } else if (lane < 60) {
#pragma unroll
      for (int j = 0; j < 4; j++) ldw1((cw - 4) * 4 + j, rot, lds0);
    }
    if (cw < 8) {
      const int c = cw & 3, x = lane + c - 35;
      *(LAS float*)(lds + L_MASK + cw * 256 + lane * 4) = ((x > 0) != (cw >= 4)) ? -1e30f : 0.f;
    } else if (cw == 8 && lane < 4) *(LAS u32x2*)(lds + L_ONES + lane * 640) = u32x2{0x3F803F80u, 0x3F803F80u};
    pre();
    const int row = mi * 32 + i16;
    const u32 xa0 = row * 128 + ((g ^ ((row >> 1) & 7)) << 4), xa1 = xa0 ^ 64;
    const u32 wa = XB + (16 * (g >> 1) + 4 * (g & 1) + q4) * 480 + 8 * p4 + nj * 160;
    int base, HS, RS;
    if (br) {
      if (nj == 0) { base = L_Q; HS = 6144; RS = 48; }
      else { base = L_OWN + (nj - 1) * 40 + mi * 7680; HS = 2560; RS = 80; }
    } else {
      if (nj == 2) { base = L_AV; HS = 6144; RS = 48; }
      else { base = nj ? L_AK : L_AQ; HS = 40; RS = 176; }
    }
    int ea[5];
#pragma unroll
    for (int n = 0; n < 5; n++) { const int c4 = 4 * n + g, hd = (c4 * 13) >> 6, d4 = c4 - 5 * hd; ea[n] = base + hd * HS + row * RS + d4 * 8; PIN(ea[n]); }
    f32x4 acc[2][5] = {};
    auto rd = [&](Frags& f, lds_ptr bb, int ks, int j) {
      if (j < 2) f.x[j] = ld128(bb + (ks ? xa1 : xa0) + j * 2048);
      else { int n = (j - 2) >> 1; if (j & 1) f.wh[n] = trrd(bb + wa + ks * 15360 + 3840 + n * 32); else f.wl[n] = trrd(bb + wa + ks * 15360 + n * 32); }
    };
#define MM(F, i) acc[(i) & 1][(i) >> 1] = MFMA16(cat8(F.wl[(i) >> 1], F.wh[(i) >> 1]), F.x[(i) & 1], acc[(i) & 1][(i) >> 1])
    Frags F0, F1;
    int boff = 0;
    __builtin_amdgcn_s_setprio(3);
    waitv<0>();
    lbar();
    STAMP(14);
#pragma unroll
    for (int j = 0; j < 12; j++) rd(F0, lds, 0, j);
#pragma nounroll
    for (int t = 0; t < 10; t++) {
      SBAR();
#pragma unroll
      for (int i = 0; i < 10; i++) { MM(F0, i); if (i < 6) { rd(F1, lds + boff, 1, 2 * i); rd(F1, lds + boff, 1, 2 * i + 1); } SBAR(); }
#pragma unroll
      for (int i = 0; i < 10 - HB; i++) MM(F1, i);
      SBAR();
      boff += STB; if (boff == 3 * STB) boff = 0;
      __builtin_amdgcn_s_waitcnt(0xC07F);
      if (t < 9) __builtin_amdgcn_s_barrier();
      asm volatile("" ::: "memory");
#pragma unroll
      for (int i = 10 - HB; i < 10; i++) {
#pragma unroll
        for (int j = (i - 10 + HB) * RPM; j < (i - 9 + HB) * RPM && j < 12; j++) rd(F0, lds + boff, 0, j);
        MM(F1, i); SBAR();
      }
    }
    lds_inc(misc + M_C8, lane);
    STAMP(1);
    if (br && nj == 0) {
#pragma unroll
      for (int n = 0; n < 5; n++)
#pragma unroll
        for (int m = 0; m < 2; m++) { const f32x4 v = acc[m][n] * (0.22360679774997896f * 1.4426950408889634f); *(LAS u32x2*)(lds + ea[n] + m * 16 * 48) = u32x2{cvtpk(v[0], v[1]), cvtpk(v[2], v[3])}; }
    } else {
#pragma unroll
      for (int n = 0; n < 5; n++)
#pragma unroll
        for (int m = 0; m < 2; m++) { const f32x4 v = acc[m][n]; *(LAS u32x2*)(lds + ea[n] + m * 16 * RS) = u32x2{cvtpk(v[0], v[1]), cvtpk(v[2], v[3])}; }
    }
    if (br == 0 && nj == 2) {
#pragma unroll
      for (int m = 0; m < 2; m++) *(LAS u32x2*)(lds + L_AV + g * 6144 + (row + 16 * m) * 48 + 40) = u32x2{0x3F803F80u, 0x3F803F80u};
    }
    lds_inc(misc + M_WR + nj * 16 + mi * 4, lane);
    if (nj == 2) lds_inc(misc + M_WV, lane);
    lds_inc(misc + M_C9, lane);
    __builtin_amdgcn_s_setprio(0);
  }
  f32x16 o = {};
  PIN(o);
  if (br) lds_wait_ge(misc + M_C9, 12u);
  STAMP(2);
  const u32 tag = RFL(lds_word(misc + M_TAG));
  const int l_wo = br ? LB_WO : LA_WO;
  auto started = [&]() { if (lane == 0) gstore(ws, W_STARTED + ((br * 16 + rt) * 8 + hg) * 4, tag); };
  auto guard = [&]() { poll_tag(ws, W_STARTED + ((br * 16 + rt) * 8 + (lane & 7)) * 4, tag, false); };
  const rsrc_t rwo = __builtin_amdgcn_make_buffer_rsrc((void*)(br ? woB : woA), (short)0, D_ * D_ * 2, 0x00020000);
  u32 wvm = 0, wvt = 0; int wj = 0;
  auto wo_init = [&](int j) {
    wj = j;
    int c = j * 64 + lane; PIN(c);
    const int p = c / 10, ch = c - p * 10;
    const int gg = ((p >> 3) & 2) | ((p >> 2) & 1), tt = (p >> 3) & 1, e = p & 3;
    wvm = ((gg >> 1) * 20 + 4 * (gg & 1) + 8 * tt + e) * 1280 + hg * 160 + ch * 16; wvt = (tt * 80 + gg * 20 + 16 + e) * 1280 + hg * 160 + ch * 16;
  };
  auto wo_dma = [&](int k0, int k1) {
#pragma nounroll
    for (int ks = k0; ks < k1; ks++) {
      if (ks < 16) dma16(rwo, wvm, ks * 40 * 1280, lds0 + l_wo + ks * 5120 + wj * 1024);
      else dma16(rwo, wvt, (ks - 16) * 160 * 1280, lds0 + l_wo + ks * 5120 + wj * 1024);
    }
  };
  const bool wow = wid >= 11;

  if (br) {
    const int hd = wid & 3, qi = wid >> 2;
    const u32 kvf = W_KVFLAG + (rt * 8 + hg) * 128;
    const bf16x8 qa = ld128(lds + pc[0]);
    s16x4 qb = ld64(lds + pc[0] + 32);
    if (h) qb = s16x4{0, 0, 0, 0};
    auto ldmask = [&](int pol) -> f32x16 {
      f32x4 t[4];
#pragma unroll
      for (int j = 0; j < 4; j++) t[j] = *(LAS f32x4*)(lds + pc[6] + pol * 1024 + j * 32);
      return __builtin_shufflevector(__builtin_shufflevector(t[0], t[1], 0, 1, 2, 3, 4, 5, 6, 7), __builtin_shufflevector(t[2], t[3], 0, 1, 2, 3, 4, 5, 6, 7),
                                     0, 1, 2, 3, 4, 5, 6, 7, 8, 9, 10, 11, 12, 13, 14, 15);
    };
    auto pk4 = [&](const f32x16& s, int o_) -> bf16x8 {
      u32x4 p;
#pragma unroll
      for (int j = 0; j < 4; j++) p[j] = cvtpk(ex2(s[o_ + 2 * j]), ex2(s[o_ + 2 * j + 1]));
      return __builtin_bit_cast(bf16x8, p);
    };
    lds_ptr ka_p, kb_p, vp;
    auto seek = [&](int slot) { ka_p = lds + pc[1] + slot * 2560; kb_p = lds + pc[2] + slot * pc[4]; vp = lds + pc[3] + slot * pc[5]; };
    f32x16 s; s16x4 c;
    auto step = [&](int d) {
      ka_p += d * 2560; kb_p += __mul24(d, (int)pc[4]);
      const bf16x8 v0 = cat8(trrd(vp), trrd(vp + 640));
      const bf16x8 ka = ld128(ka_p); const s16x4 kb = ld64(kb_p);
      const bf16x8 v1 = cat8(trrd(vp + 1280), trrd(vp + 1920));
      vp += __mul24(d, (int)pc[5]);
      SBAR();
      o = MFMA32(v0, pk4(s, 0), o);
      const bf16x8 p1 = pk4(s, 8);
      SBAR();
      s = MFMA32K8(kb, c, MFMA32(ka, qa, f32x16{}));
      o = MFMA32(v1, p1, o);
      SBAR();
    };
    auto last = [&]() {
      const bf16x8 v0 = cat8(trrd(vp), trrd(vp + 640)), v1 = cat8(trrd(vp + 1280), trrd(vp + 1920));
      o = MFMA32(v0, pk4(s, 0), o);
      o = MFMA32(v1, pk4(s, 8), o);
    };
    lds_inc(misc + M_C5, lane);
    if (qi == 0) __builtin_amdgcn_s_setprio(3);
    seek(L_OWN / 2560 + qi * 4 + hd);
    {
      const s16x4 kb = ld64(kb_p);
      s = MFMA32K8(kb, qb, MFMA32(ld128(ka_p), qa, ldmask(0)));
      float mm = fmaxf(s[0], s[1]);
#pragma unroll
      for (int r = 2; r < 16; r += 2) mm = fmaxf(fmaxf(mm, s[r]), s[r + 1]);
      auto rr = __builtin_amdgcn_permlane32_swap(__float_as_uint(mm), __float_as_uint(mm), false, false);
      const u32 nm = cvtpk(-fmaxf(__uint_as_float(rr[0]), __uint_as_float(rr[1])), 0.f);
      const u32x2 qw = __builtin_bit_cast(u32x2, qb);
      c = __builtin_bit_cast(s16x4, u32x2{h ? nm : qw.x, qw.y});
      s = MFMA32K8(kb, __builtin_bit_cast(s16x4, u32x2{h ? nm : 0u, 0u}), s);
    }
#pragma nounroll
    for (int k = 0; k < qi; ++k) step(-4);
    last();
    lds_inc(misc + M_C1, lane);
    STAMP(3);
    if (qi == 0) { waitv<0>(); lds_inc(misc + M_C7, lane); }
    if (wow) wo_init(wid - 11);
    lds_wait_ge(misc + M_C0, 4u);
    int wk = 20;
    if (rt > 0) {
      int n1 = 2, nn = 4, km = -1;
      if (rt >= 2) { n1 = qi < 3 ? 4 : 3; nn = 8 - qi; km = qi >= 2 ? n1 - 1 : nn - 1; }
      seek(28 + hd);
      s = MFMA32K8(ld64(kb_p), c, MFMA32(ld128(ka_p), qa, f32x16{}));
#pragma nounroll
      for (int k = 1; k < nn; ++k) {
        const int rem = nn - k;
        if (rem >= 6) __builtin_amdgcn_s_setprio(3); else if (rem >= 4) __builtin_amdgcn_s_setprio(2); else if (rem >= 2) __builtin_amdgcn_s_setprio(1); else __builtin_amdgcn_s_setprio(0);
        int d = -4;
        if (k == 2 && wow) { lds_wait_ge(misc + M_C7, 4u); lds_wait_ge(misc + M_C1, 16u); }
        if (k == n1) {
          d = 4 * n1 - 20;
          lds_wait_ge(misc + M_C7, 4u);
          if (wid == 10) guard();
        }
        if (wow && k >= 2 && wk > 5) { wo_dma(wk - 5, wk); wk -= 5; }
        step(d);
        if (k == km) s += ldmask(1);
        if (k == n1) lds_inc(misc + M_C3, lane);
      }
      if (wow) { lds_wait_ge(misc + M_C3, 16u); wo_dma(0, wk); }
      last();
    } else {
      if (wow) { lds_wait_ge(misc + M_C1, 16u); wo_dma(0, 20); }
      if (wid == 10) guard();
    }
    float l = h ? o[8] : o[12];
    if (__builtin_expect(__any(!(l < 1.8e19f)), 0)) {
      const int dd = lane % 3;
      poll_tag(ws, kvf - (rt - dd < 0 ? 0 : dd) * 1024 + 4 * (lane & 3), tag, false);
      float q[20], a[20], mx = -3e38f; l = 0.f;
      const u32x4 qw = __builtin_bit_cast(u32x4, qa); const u32x2 qv = __builtin_bit_cast(u32x2, qb);
#pragma unroll
      for (int j = 0; j < 10; j++) {
        const u32 w = j < 8 ? __shfl(qw[j & 3], l32 + 32 * (j >> 2)) : __shfl(qv[j & 1], l32);
        q[2 * j] = blo(w); q[2 * j + 1] = bhi(w); a[2 * j] = 0.f; a[2 * j + 1] = 0.f;
      }
      const int row = rt * 128 + 32 * qi + l32;
#pragma nounroll
      for (int t = 0; t < 128; ++t) {
        const int j = row - 2 * t - h;
        if (j >= 0) {
          const u32 src = W_KVG + (((j >> 7) * 8 + hg) * 4 + ((j >> 5) & 3)) * 10240 + hd * 2560 + (j & 31) * 80;
          u32 w[20];
#pragma unroll
          for (int i = 0; i < 5; i++) { const u32x4 v = __builtin_amdgcn_raw_buffer_load_b128(rws, src, i * 16, 16); w[4 * i] = v.x; w[4 * i + 1] = v.y; w[4 * i + 2] = v.z; w[4 * i + 3] = v.w; }
          float sc = 0.f;
#pragma unroll
          for (int i = 0; i < 10; i++) sc += q[2 * i] * blo(w[i]) + q[2 * i + 1] * bhi(w[i]);
          const float mn = fmaxf(mx, sc), f = ex2(mx - mn), p = ex2(sc - mn);
          mx = mn; l = l * f + p;
#pragma unroll
          for (int i = 0; i < 10; i++) { a[2 * i] = a[2 * i] * f + p * blo(w[10 + i]); a[2 * i + 1] = a[2 * i + 1] * f + p * bhi(w[10 + i]); }
        }
      }
      const float mo = __shfl_xor(mx, 32), mn = fmaxf(mx, mo), f = ex2(mx - mn);
      l *= f; l += __shfl_xor(l, 32);
#pragma unroll
      for (int i = 0; i < 20; i++) { a[i] *= f; a[i] += __shfl_xor(a[i], 32); }
#pragma unroll
      for (int r = 0; r < 8; r++) o[r] = h ? a[(r & 3) + 8 * (r >> 2) + 4] : a[(r & 3) + 8 * (r >> 2)];
#pragma unroll
      for (int r = 8; r < 12; r++) o[r] = a[8 + r];
    }
    const float rl = __builtin_amdgcn_rcpf(l);
    const u32x4 w0 = {cvtpk(o[0] * rl, o[1] * rl), cvtpk(o[2] * rl, o[3] * rl), cvtpk(o[4] * rl, o[5] * rl), cvtpk(o[6] * rl, o[7] * rl)};
    const u32x2 w1 = {cvtpk(o[8] * rl, o[9] * rl), cvtpk(o[10] * rl, o[11] * rl)};
    __builtin_amdgcn_raw_buffer_store_b128(w0, rws, pc[7], 0, 16);
    __builtin_amdgcn_raw_buffer_store_b64(w1, rws, pc[8], 0, 16);
    STAMP(6);
    __builtin_amdgcn_s_setprio(0);
  } else {
    const int tg = wid & 3, pair = (wid >> 2) & 1, isq = wid >> 3;
    if (!isq) __builtin_amdgcn_s_setprio(2);
    u32 phi[20];
    {
      lds_wait_ge(misc + M_WR + (isq ? 0 : 16) + tg * 4, 1u);
      const bf16x8 b0 = ld128(lds + pc[0]), a00 = ld128(lds + pc[1]), a10 = ld128(lds + pc[1] + 2560);
      const bf16x8 b1 = ld128(lds + pc[0] + 32), a01 = ld128(lds + pc[1] + 1024), a11 = ld128(lds + pc[1] + 2560 + 1024);
      const s16x4 b2 = ld64(lds + pc[8]), a02 = ld64(lds + pc[2]), a12 = ld64(lds + pc[2] + 2560);
      lds_inc(misc + M_C5, lane);
      if (wow) { wo_init(wid - 11); lds_wait_ge(misc + M_C5, 16u); wo_dma(6, 20); waitv<0>(); lds_inc(misc + M_C6, lane); }
      f32x16 t0 = MFMA32(a00, b0, f32x16{}), t1 = MFMA32(a10, b0, f32x16{});
      t0 = MFMA32(a01, b1, t0); t1 = MFMA32(a11, b1, t1);
      t0 = MFMA32K8(a02, b2, t0); t1 = MFMA32K8(a12, b2, t1);
      float p[20];
#pragma unroll
      for (int j = 0; j < 10; j++) {
        const u32 w = j < 8 ? cvtpk(t0[2 * j], t0[2 * j + 1]) : cvtpk(t1[2 * j - 16], t1[2 * j - 15]);
        p[2 * j] = blo(w); p[2 * j + 1] = bhi(w);
      }
      float mx = fmaxf(p[0], p[1]), mn = fminf(p[0], p[1]);
#pragma unroll
      for (int j = 2; j < 20; j += 2) { mx = fmaxf(fmaxf(mx, p[j]), p[j + 1]); mn = fminf(fminf(mn, p[j]), p[j + 1]); }
      const float L2E = 1.4426950408889634f;
      const f32x2 sl = {L2E, -L2E}, of = {-mx * L2E, mn * L2E};
      f32x2 e[20], sa = {0.f, 0.f}, sb = {0.f, 0.f};
#pragma unroll
      for (int j = 0; j < 20; j++) {
        const f32x2 a = __builtin_elementwise_fma(f32x2{p[j], p[j]}, sl, of);
        e[j] = f32x2{ex2(a[0]), ex2(a[1])};
        if (j & 1) sb += e[j]; else sa += e[j];
      }
      sa += sb;
      const f32x2 rr = {__builtin_amdgcn_rcpf(sa[0]), __builtin_amdgcn_rcpf(sa[1])};
#pragma unroll
      for (int j = 0; j < 20; j++) phi[j] = cvtpk2(e[j] * rr);
    }
    if (!isq) {
      lds_wait_ge(misc + M_WV, 4u);
      bf16x8 vb[4];
#pragma unroll
      for (int ks = 0; ks < 4; ks++) vb[ks] = cat8(trrd(lds + pc[3] + ks * 32 * 48), trrd(lds + pc[3] + ks * 32 * 48 + 4 * 48));
      lds_wait_ge(misc + M_C8, 12u);
#pragma unroll
      for (int j = 0; j < 5; j++) *(LAS u32x4*)(lds + pc[4] + 16 * j) = u32x4{phi[4 * j], phi[4 * j + 1], phi[4 * j + 2], phi[4 * j + 3]};
      lds_inc(misc + M_C0 + pair * 4, lane);
      STAMP(3);
      lds_wait_ge(misc + M_C0 + pair * 4, 4u);
      f32x4 kv[3] = {};
#pragma unroll
      for (int ks = 0; ks < 4; ks++)
#pragma unroll
        for (int T = 0; T < 3; T++) kv[T] = MFMA16(cat8(trrd(lds + pc[5] + ks * 2560 + T * 32), trrd(lds + pc[5] + ks * 2560 + 320 + T * 32)), vb[ks], kv[T]);
#pragma unroll
      for (int T = 0; T < 3; T++)
        __builtin_amdgcn_raw_buffer_store_b128(u32x4{cvtpk(kv[T][0], kv[T][1]), tag, cvtpk(kv[T][2], kv[T][3]), tag}, rws, pc[T < 2 ? 6 : 7], T * ((tg & 1) ? 320 : 1024), 16);
      waitv<3>();
      lds_inc(misc + M_C1, lane);
      STAMP(4);
      if (wid >= 1 && wid < 6) { wo_init(wid - 1); lds_wait_ge(misc + M_C1, 8u); wo_dma(0, 6); waitv<0>(); lds_inc(misc + M_C6, lane); }
      if (wid == 0) {
        const int sl = (rt & 3) * 53 + lane; const bool act = lane < 53 && sl < 210;
        const u32 src = act ? W_PART + (hg * 4 + (rt >> 2)) * 16 * PARTB + sl * 16 : OOB;
        float s0 = 0.f, s1 = 0.f, s2 = 0.f, s3 = 0.f;
        lds_wait_ge(misc + M_C6, 10u);
#pragma nounroll
        for (u32 s = 0; s < SPIN_CAP; ++s) {
          bool ok = true; s0 = 0.f; s1 = 0.f; s2 = 0.f; s3 = 0.f;
          u32x4 v[16];
#pragma unroll
          for (int r = 0; r < 16; r++) v[r] = __builtin_amdgcn_raw_buffer_load_b128(rws, src, r * PARTB, 16);
#pragma unroll
          for (int r = 0; r < 16; r++) { ok &= (v[r].y == tag) & (v[r].w == tag); s0 += blo(v[r].x); s1 += bhi(v[r].x); s2 += blo(v[r].z); s3 += bhi(v[r].z); }
          if (__all(ok || !act)) break;
          asm volatile("" ::: "memory");
        }
        __builtin_amdgcn_raw_buffer_store_b128(u32x4{cvtpk(s0, s1), tag, cvtpk(s2, s3), tag}, rws, act ? W_SLICE + (hg * 16 + rt) * 1024 + lane * 16 : OOB, 0, 16);
        lds_inc(misc + M_C4, lane);
        STAMP(5);
      } else if (wid == 1) { started(); lds_wait_ge(misc + M_C4, 1u); guard(); }
      else if (wid >= 4) {
        const int hh = wid - 4;
        const u32 src = W_SLICE + (hg * 16 + hh * 4) * 1024 + lane * 16;
        int da[4];
#pragma unroll
        for (int j = 0; j < 4; j++) {
          const int sl = j * 53 + lane; int d, ph;
          if (sl < 160) { d = sl & 15; ph = (sl >> 4) * 4; }
          else { const int s2 = sl - 160, gg = s2 / 5; d = 16 + s2 - 5 * gg; ph = 4 * gg; }
          da[j] = L_KVT + hh * 1680 + d * 80 + ph * 2;
        }
        lds_wait_ge(misc + M_C5, 16u);
        lds_wait_ge(misc + M_C4, 1u);
        u32x4 v[4];
#pragma nounroll
        for (u32 s = 0; s < SPIN_CAP; ++s) {
          bool ok = true;
#pragma unroll
          for (int r = 0; r < 4; r++) v[r] = __builtin_amdgcn_raw_buffer_load_b128(rws, src, r * 1024, 16);
#pragma unroll
          for (int r = 0; r < 4; r++) ok &= ((v[r].y == tag) & (v[r].w == tag)) | (lane >= (r < 3 ? 53 : 51));
          if (__all(ok)) break;
          asm volatile("" ::: "memory");
        }
#pragma unroll
        for (int r = 0; r < 4; r++) if (lane < (r < 3 ? 53 : 51)) *(LAS u32x2*)(lds + da[r]) = u32x2{v[r].x, v[r].z};
        lds_inc(misc + M_C3, lane);
        STAMP(11);
      }
      __builtin_amdgcn_s_setprio(0);
    } else {
      lds_ptr a0 = lds + pc[3], a1 = lds + pc[4]; const u32 om = pc[5], ot = pc[6];
      STAMP(12);
      lds_wait_ge(misc + M_C3, 4u);
      f32x16 t0 = {}, t1 = {};
#pragma unroll
      for (int j = 0; j < 5; j++) {
        const bf16x8 bq = __builtin_bit_cast(bf16x8, u32x4{phi[4 * j], phi[4 * j + 1], phi[4 * j + 2], phi[4 * j + 3]});
        t1 = MFMA32(ld128(a1 + 16 * j), bq, t1); t0 = MFMA32(ld128(a0 + 16 * j), bq, t0);
      }
      const float rl = __builtin_amdgcn_rcpf(t1[4] + 1e-6f);
      const u32x4 w0 = {cvtpk(t0[0] * rl, t0[1] * rl), cvtpk(t0[2] * rl, t0[3] * rl), cvtpk(t0[8] * rl, t0[9] * rl), cvtpk(t0[10] * rl, t0[11] * rl)};
      const u32x4 w1 = {cvtpk(t0[4] * rl, t0[5] * rl), cvtpk(t0[6] * rl, t0[7] * rl), cvtpk(t0[12] * rl, t0[13] * rl), cvtpk(t0[14] * rl, t0[15] * rl)};
      const u32x2 w2 = {cvtpk(t1[0] * rl, t1[1] * rl), cvtpk(t1[2] * rl, t1[3] * rl)};
      __builtin_amdgcn_raw_buffer_store_b128(w0, rws, om, 0, 16);
      __builtin_amdgcn_raw_buffer_store_b128(w1, rws, om, 256, 16);
      __builtin_amdgcn_raw_buffer_store_b64(w2, rws, ot, 0, 16);
      STAMP(6);
    }
  }
  STAMP(7);
  const u32 ofl = W_OFLAG + (br * 16 + rt) * 128;
  auto published = [&]() { if (lane == 0 && lds_add_ret(misc + M_C2) == 15) gstore(ws, ofl + hg * 4, tag); };
  lbar();
  STAMP(8);
  if (wid >= 8) {
    waitv<0>();
    if (wow && br) lds_inc(misc + M_C6, lane);
    published();
    if (b == 0 && wid == 15) {
#pragma nounroll
      for (u32 s = 0; s < SPIN_CAP; ++s) {
        const u32x4 v = __builtin_amdgcn_raw_buffer_load_b128(rws, W_STARTED + lane * 16, 0, 16);
        if (__all(v.x == tag && v.y == tag && v.z == tag && v.w == tag)) break;
        __builtin_amdgcn_s_sleep(2);
      }
      if (lane == 0) gstore(ws, W_EPOCH, tag);
    }
  } else {
    const int mi = wid & 3, kh = wid >> 2;
    lds_ptr red = lds + L_RED;
    const u32 obm = og + (rt * 8 + mi * 2) * 20480 + kh * 8192 + lane * 16, obt = og + (rt * 8 + mi * 2) * 20480 + kh * 2048 + lane * 8;
    u32x4 of[10][2];
    auto ldo = [&](int s) {
#pragma unroll
      for (int m = 0; m < 2; m++) {
        if (s < 8) of[s][m] = __builtin_amdgcn_raw_buffer_load_b128(rws, obm, m * 20480 + s * 1024, 16);
        else {
          const u32x2 a = __builtin_amdgcn_raw_buffer_load_b64(rws, obt, m * 20480 + (s + 8) * 1024, 16);
          const u32x2 c = __builtin_amdgcn_raw_buffer_load_b64(rws, obt, m * 20480 + (s + 8) * 1024 + 512, 16);
          of[s][m] = u32x4{a.x, a.y, c.x, c.y};
        }
      }
    };
    u32 wm = l_wo + (16 * (g >> 1) + 4 * (g & 1) + q4) * 160 + 8 * p4, wt = wm + (16 + 2 * kh) * 5120; wm += kh * 8 * 5120;
    PIN(wm); PIN(wt);
    bf16x8 wf[5];
    auto ldw = [&](int s, int n) { lds_ptr p = lds + (s < 8 ? wm + s * 5120 : wt + (s - 8) * 5120) + n * 32; wf[n] = cat8(trrd(p), trrd(p + 1280)); };
    f32x4 acc[2][5];
    constexpr int PF = 6;
#pragma nounroll
    for (int pass = 0;; ++pass) {
      if (pass) poll_tag(ws, ofl + (lane & 7) * 4, tag, false);
#pragma nounroll
      for (int tr = 0; tr < 64; ++tr) {
#pragma unroll
        for (int s = 9; s > 5; s--) ldo(s);
        asm volatile("" ::: "memory");
        u32 mx = 0;
#pragma unroll
        for (int s = 8; s < 10; s++)
#pragma unroll
          for (int m = 0; m < 2; m++) mx = max(max(mx, max(of[s][m].x, of[s][m].y)), max(of[s][m].z, of[s][m].w));
        if (pass || !__any(mx == ~0u)) break;
      }
      STAMP(9);
#pragma unroll
      for (int s = 5; s > 9 - PF; s--) ldo(s);
      if (!pass) published();
      lds_wait_ge(misc + M_C6, br ? 5u : 10u);
#pragma unroll
      for (int n = 0; n < 5; n++) { ldw(9, n); acc[0][n] = f32x4{}; acc[1][n] = f32x4{}; }
#pragma unroll
      for (int s = 9; s >= 0; s--) {
        SBAR();
#pragma unroll
        for (int n = 0; n < 5; n++) {
          acc[0][n] = MFMA16(wf[n], __builtin_bit_cast(bf16x8, of[s][0]), acc[0][n]);
          acc[1][n] = MFMA16(wf[n], __builtin_bit_cast(bf16x8, of[s][1]), acc[1][n]);
          if (s > 0) ldw(s - 1, n);
          SBAR();
        }
        if (s - PF >= 0) ldo(s - PF);
      }
      const float chk = acc[0][0][0] + acc[1][0][0];
      if (pass || !__any(chk != chk)) break;
    }
    STAMP(13);
#pragma unroll
    for (int n = 0; n < 5; n++) *(LAS f32x4*)(red + (wid ^ 4) * 5120 + n * 1024 + lane * 16) = kh ? acc[0][n] : acc[1][n];
    lbar();
    lds_ptr mine = red + wid * 5120;
    u32x2 pk[5];
#pragma unroll
    for (int n = 0; n < 5; n++) { const f32x4 v = (kh ? acc[1][n] : acc[0][n]) + *(LAS f32x4*)(mine + n * 1024 + lane * 16); pk[n] = u32x2{cvtpk(v[0], v[1]), cvtpk(v[2], v[3])}; }
    asm volatile("" ::: "memory");
#pragma unroll
    for (int n = 0; n < 5; n++) *(LAS u32x2*)(mine + i16 * 176 + n * 32 + g * 8) = pk[n];
    asm volatile("" ::: "memory");
    const rsrc_t ro = __builtin_amdgcn_make_buffer_rsrc((void*)out, (short)0, S_ * 1280 * 2, 0x00020000);
    const u32 obase = (rt * 128 + mi * 32 + kh * 16) * 2560 + br * 1280 + hg * 160;
#pragma unroll
    for (int i = 0; i < 3; i++) {
      const int c = i * 64 + lane, r = c / 10, ch = c - r * 10;
      const u32x4 v = *(LAS u32x4*)(mine + r * 176 + ch * 16);
      __builtin_amdgcn_raw_buffer_store_b128(v, ro, c < 160 ? obase + r * 2560 + ch * 16 : OOB, 0, 0);
    }
  }
  STAMP(10);
}

extern "C" void kernel_launch(void* const* d_in, const int* in_sizes, int n_in, void* d_out, int out_size, void* d_ws, size_t ws_size, hipStream_t stream) {
  (void)hipFuncSetAttribute((const void*)fused, hipFuncAttributeMaxDynamicSharedMemorySize, LDS_BYTES);
  fused<<<256, 1024, LDS_BYTES, stream>>>((const u16*)d_in[0], (const u16*)d_in[1], (const u16*)d_in[2], (const u16*)d_in[3],
                                          (const u16*)d_in[4], (const u16*)d_in[5], (const u16*)d_in[6], (u16*)d_out, (char*)d_ws);
}
```

```cpp
#include <hip/hip_runtime.h>
#include <hip/hip_bf16.h>

typedef unsigned short u16;
typedef unsigned u32;
typedef short bf16x8 __attribute__((ext_vector_type(8)));
typedef short s16x4 __attribute__((ext_vector_type(4)));
typedef float f32x4 __attribute__((ext_vector_type(4)));
typedef float f32x16 __attribute__((ext_vector_type(16)));
typedef float f32x2 __attribute__((ext_vector_type(2)));
typedef __bf16 bf2_t __attribute__((ext_vector_type(2)));
typedef u32 u32x4 __attribute__((ext_vector_type(4)));
typedef u32 u32x2 __attribute__((ext_vector_type(2)));
#define LAS __attribute__((address_space(3)))
#define GAS __attribute__((address_space(1)))
typedef LAS char* lds_ptr;
typedef __amdgpu_buffer_rsrc_t rsrc_t;
#define DI __device__ __forceinline__
#define RFL(x) __builtin_amdgcn_readfirstlane(x)
#define SBAR() __builtin_amdgcn_sched_barrier(0)
#define PIN(x) asm volatile("" : "+v"(x))
#define RLX_AGENT __ATOMIC_RELAXED, __HIP_MEMORY_SCOPE_AGENT
#define RLX_WG __ATOMIC_RELAXED, __HIP_MEMORY_SCOPE_WORKGROUP
#define XMAP 0
#define HB 10
#define RPM ((12 + HB - 2) / (HB - 1))
#define S1DELAY 8
#define ROT 4

constexpr int S_ = 2048, D_ = 640, N3 = 1920;
constexpr int XB = 128 * 64 * 2, WB = 64 * 240 * 2, STB = XB + WB;
constexpr int L_RAW = 3 * STB;
constexpr int L_ZERO = L_RAW + 3200;
constexpr int L_WTAB = L_RAW + 4096;
constexpr int L_MASK = 158720;
constexpr int L_ONES = 160768;
constexpr int L_MISC = L_ONES + 64;
constexpr int L_DUMMY = 162816;
constexpr int LDS_BYTES = 163840;
constexpr int L_HALO = 0, L_OWN = 81920, L_Q = 122880, LB_WO = 56320, L_RED = 0;
constexpr int L_PK = 0, L_AV = 47104, L_AK = 71680, L_AQ = L_AK + 22528, L_KVT = L_WTAB, LA_WO = 40960;
constexpr int M_TAG = 0, M_C0 = 16, M_C1 = 32, M_C2 = 36, M_C3 = 40, M_C4 = 44, M_C5 = 48, M_C6 = 52, M_C7 = 56, M_C9 = 60, M_WR = 64, M_C8 = 112, M_WV = 116;
constexpr u32 W_EPOCH = 0, W_STARTED = 1024, W_KVFLAG = 4096, W_OFLAG = 32768, W_SLICE = 65536;
constexpr u32 W_PART = 1u << 20, W_KVG = 8u << 20, W_OG = 16u << 20, W_TOTAL = 24u << 20;
constexpr u32 PARTB = 3456;
constexpr u32 OOB = 0x80000000u;
constexpr u32 SPIN_CAP = 1u << 18;

DI void dma16(rsrc_t r, u32 voff, u32 soff, u32 ldsaddr) {
  asm volatile("s_mov_b32 m0, %0\n\ts_nop 0\n\tbuffer_load_dwordx4 %1, %2, %3 offen lds" ::"s"(ldsaddr), "v"(voff), "s"(r), "s"(soff) : "m0", "memory");
}
DI void dma16c(rsrc_t r, u32 voff, u32 soff, u32 ldsaddr) {
  asm volatile("s_mov_b32 m0, %0\n\ts_nop 0\n\tbuffer_load_dwordx4 %1, %2, %3 offen sc1 lds" ::"s"(ldsaddr), "v"(voff), "s"(r), "s"(soff) : "m0", "memory");
}
DI s16x4 trrd(lds_ptr p) { return __builtin_bit_cast(s16x4, __builtin_amdgcn_ds_read_tr16_b64_v4i16((LAS s16x4*)p)); }
DI bf16x8 cat8(s16x4 a, s16x4 b) { return __builtin_shufflevector(a, b, 0, 1, 2, 3, 4, 5, 6, 7); }
DI u32 cvtpk2(f32x2 v) { return __builtin_bit_cast(u32, __builtin_convertvector(v, bf2_t)); }
DI u32 cvtpk(float lo, float hi) { return cvtpk2(f32x2{lo, hi}); }
DI float blo(u32 w) { return __uint_as_float(w << 16); }
DI float bhi(u32 w) { return __uint_as_float(w & 0xffff0000u); }
template <int N> DI void waitv() { asm volatile("s_waitcnt vmcnt(%0)" ::"i"(N) : "memory"); }
DI void lbar() { asm volatile("s_waitcnt lgkmcnt(0)\n\ts_barrier" ::: "memory"); }
DI void waitl() { asm volatile("s_waitcnt lgkmcnt(0)" ::: "memory"); }
DI bf16x8 ld128(lds_ptr p) { return *(LAS bf16x8*)p; }
DI s16x4 ld64(lds_ptr p) { return *(LAS s16x4*)p; }
#define MFMA16(a, b, c) __builtin_amdgcn_mfma_f32_16x16x32_bf16(a, b, c, 0, 0, 0)
#define MFMA32(a, b, c) __builtin_amdgcn_mfma_f32_32x32x16_bf16(a, b, c, 0, 0, 0)
#define MFMA32K8(a, b, c) __builtin_amdgcn_mfma_f32_32x32x8bf16_1k(a, b, c, 0, 0, 0)
DI float ex2(float x) { return __builtin_amdgcn_exp2f(x); }
DI u32 gload(char* ws, u32 off) { return __hip_atomic_load((GAS u32*)(ws + off), RLX_AGENT); }
DI void gstore(char* ws, u32 off, u32 v) { __hip_atomic_store((GAS u32*)(ws + off), v, RLX_AGENT); }
DI u32 lds_word(lds_ptr p) { return __hip_atomic_load((LAS u32*)p, RLX_WG); }
DI void lds_inc(lds_ptr p, int) {
  unsigned long long sv;
  asm volatile("s_mov_b64 %0, exec\n\ts_mov_b64 exec, 1\n\tds_add_u32 %1, %2\n\ts_mov_b64 exec, %0" : "=&s"(sv) : "v"((u32)(size_t)p), "v"(1u) : "memory");
}
DI u32 lds_add_ret(lds_ptr p) { return __hip_atomic_fetch_add((LAS u32*)p, 1u, RLX_WG); }
DI void lds_wait_ge(lds_ptr p, u32 want) {
#pragma nounroll
  for (u32 s = 0; s < SPIN_CAP * 8; ++s) { if (RFL(lds_word(p)) >= want) break; __builtin_amdgcn_s_sleep(1); }
  asm volatile("" ::: "memory");
}
DI void poll_tag(char* ws, u32 off, u32 tag, bool done) {
#pragma nounroll
  for (u32 s = 0; s < SPIN_CAP; ++s) { const u32 v = gload(ws, off); if (__all(done || v == tag)) break; __builtin_amdgcn_s_sleep(1); }
  asm volatile("" ::: "memory");
}

#define STAMP(i)
#define STAMPV(i)

struct Frags { bf16x8 x[2]; s16x4 wl[5], wh[5]; };

__global__ __launch_bounds__(1024) void fused(const u16* xA, const u16* wA, const u16* wphi, const u16* woA,
                                              const u16* xB, const u16* wB, const u16* woB, u16* out, char* ws) {
  extern __shared__ __attribute__((aligned(16))) char smem[];
  unsigned long long pc0; u32 len;
  asm volatile("s_getpc_b64 %0\n.Lcode_ref:\n\ts_mov_b32 %1, .Lcode_end-.Lcode_ref" : "=s"(pc0), "=s"(len));
  lds_ptr lds = (lds_ptr)smem;
  const u32 lds0 = (u32)(size_t)lds;
  const int b = blockIdx.x, xcd = b & 7, idx = b >> 3;
  const int br = xcd >> 2, rt = (xcd & 3) * 4 + (idx >> 3), hg = idx & 7;
  int lane = threadIdx.x & 63; const int wid = RFL(threadIdx.x >> 6);
  int g = lane >> 4, i16 = lane & 15, q4 = i16 >> 2, p4 = lane & 3, h = lane >> 5, l32 = lane & 31;
  auto relane = [&]() { PIN(lane); lane &= 63; g = lane >> 4; i16 = lane & 15; q4 = i16 >> 2; p4 = lane & 3; h = lane >> 5; l32 = lane & 31; };
  const rsrc_t rws = __builtin_amdgcn_make_buffer_rsrc((void*)ws, (short)0, W_TOTAL, 0x00020000);
  lds_ptr misc = lds + L_MISC;
  STAMP(0);

  const rsrc_t rx = __builtin_amdgcn_make_buffer_rsrc((void*)((br ? xB : xA) + (size_t)rt * 128 * D_), (short)0, 128 * D_ * 2, 0x00020000);
  const rsrc_t rw = __builtin_amdgcn_make_buffer_rsrc((void*)(br ? wB : wA), (short)0, D_ * N3 * 2, 0x00020000);
  const int cls = ((idx >> 3) + idx) & 3;
  const unsigned long long ord = cls == 0 ? 0x9768532410ull : cls == 1 ? 0x8796435201ull : cls == 2 ? 0x9875463102ull : 0x9865742130ull;
  const int rot = (int)(ord & 15);
  const u32 vx0 = (lane >> 3) * 1280 + (((lane & 7) ^ (lane >> 4)) << 4), vx1 = vx0 ^ 64;
  const int wc = lane >= 30 ? lane - 30 : lane;
  const u32 vw = (lane >= 30 ? 3840 : 0) + (wc >= 20 ? 2240 : wc >= 10 ? 1120 : 0) + hg * 160 + wc * 16;
  auto ldx = [&](int i, int kt, u32 buf) { dma16(rx, (i & 1) ? vx1 : vx0, 128 * kt + i * 8 * 1280, buf + i * 1024); };
  auto ldw1 = [&](int j, int kt, u32 buf) { const int p = 2 * j, k = (p & 0x33) | ((p & 4) << 1) | ((p & 8) >> 1); dma16(rw, vw, (64 * kt + k) * 3840, buf + XB + j * 960); };
  const u32 og = W_OG + br * (128 * 20480);
  u32 pc[9] = {};
  auto pre = [&]() {
    auto og_main = [&](int row, int hd, int hf) -> u32 { return og + (row >> 4) * 20480 + ((hg * 2 + (hd >> 1)) << 10) + (((2 * (hd & 1) + hf) * 16 + (row & 15)) << 4); };
    auto og_tail = [&](int row, int hd) -> u32 { return og + (row >> 4) * 20480 + 16384 + hg * 512 + ((hd * 16 + (row & 15)) << 3); };
    if (br) {
      const int hd = wid & 3, qi = wid >> 2, u = 35 - l32 + 4 * h; const bool vones = (g & 1) && p4;
      pc[0] = L_Q + hd * 6144 + (32 * qi + l32) * 48 + 16 * h;
      pc[1] = l32 * 80 + 16 * h;
      pc[2] = h ? L_ONES : l32 * 80 + 32;
      pc[3] = vones ? L_ONES : (4 * h + q4) * 80 + 40 + 32 * (g & 1) + 8 * p4;
      pc[4] = h ? 0 : 2560; pc[5] = vones ? 0 : 2560;
      pc[6] = L_MASK + (u & 3) * 256 + (u & ~3) * 4;
      pc[7] = og_main(rt * 128 + 32 * qi + l32, hd, h);
      pc[8] = h ? OOB : og_tail(rt * 128 + 32 * qi + l32, hd);
    } else {
      const int tg = wid & 3, pair = (wid >> 2) & 1, isq = wid >> 3;
      pc[0] = (isq ? L_AQ : L_AK) + (32 * tg + l32) * 176 + pair * 80 + 16 * h;
      pc[1] = L_WTAB + pair * 5120 + lane * 16;
      pc[2] = L_WTAB + pair * 5120 + 2048 + lane * 8;
      if (isq) {
        const int hp = (l32 >> 2) & 1, r = (l32 & 3) + 4 * (l32 >> 3);
        pc[3] = (h == hp) ? L_KVT + (2 * pair + h) * 1680 + r * 80 : L_ZERO;
        pc[4] = (h == hp && r <= 4) ? L_KVT + (2 * pair + h) * 1680 + (16 + r) * 80 : L_ZERO;
        pc[5] = og_main(rt * 128 + 32 * tg + l32, 2 * pair + h, 0);
        pc[6] = og_tail(rt * 128 + 32 * tg + l32, 2 * pair + h);
        pc[7] = 0;
      } else {
        const int hd = 2 * pair + (tg >> 1), dt = tg & 1;
        pc[3] = L_AV + hd * 6144 + (8 * g + q4) * 48 + 32 * dt + 8 * p4;
        pc[4] = L_PK + (2 * pair + h) * 10240 + (32 * tg + l32) * 80;
        pc[5] = L_PK + hd * 10240 + (8 * g + q4) * 80 + 8 * p4;
        pc[6] = (!dt || i16 <= 4) ? W_PART + ((hg * 4 + hd) * 16 + rt) * PARTB + (dt * 160 + (dt ? g * 5 + i16 : lane)) * 16 : OOB;
        pc[7] = g < 2 ? pc[6] : OOB;
      }
      pc[8] = pc[0] + 64 - 8 * h;
    }
#pragma unroll
    for (int i = 0; i < 9; i++) PIN(pc[i]);
  };
  if (wid < 4) {
    __builtin_amdgcn_s_setprio(3);
    u32 ep = 0;
    if (wid == 3) ep = gload(ws, W_EPOCH);
    if (wid == 2) *(LAS u32*)(misc + lane * 4) = 0;
    int kt = rot, bi = 0;
    if (wid == 1) {
      const rsrc_t rc = __builtin_amdgcn_make_buffer_rsrc((void*)(pc0 & ~15ull), (short)0, len & ~15u, 0x00020000);
      dma16(rc, idx * 1024 + lane * 16, 0, lds0 + L_DUMMY);
    }
    __builtin_amdgcn_s_sleep(S1DELAY);
#pragma nounroll
    for (int s = 1; s < 10; s++) {
      bi = bi == 2 ? 0 : bi + 1; kt = (int)(ord >> (4 * s)) & 15;
      const u32 buf = lds0 + bi * STB;
      if (s == 2 && br == 0) {
        const rsrc_t rp = __builtin_amdgcn_make_buffer_rsrc((void*)(wphi + hg * 1600), (short)0, 3200, 0x00020000);
        dma16(rp, wid * 1024 + lane * 16, 0, lds0 + L_RAW + wid * 1024);
      }
#pragma unroll
      for (int i = 0; i < 4; i++) ldx(wid * 4 + i, kt, buf);
      if (s == 1) lbar();
      if (lane < 60) {
#pragma unroll
        for (int j = 0; j < 8; j++) ldw1(wid * 8 + j, kt, buf);
      }
      if (s > 1) {
        waitv<12>();
        __builtin_amdgcn_s_barrier();
      }
      if (s == 9) pre();
      if (s == 9 && br == 0) {
        const int pair = wid >> 1, T = wid & 1, rb = g & 1;
        const int fb = 2 * rb + (p4 >> 1);
        const bool okT = (T == 0) || (fb == 0);
        const int f0 = T ? 16 : 4 * fb;
        s16x4 r[5];
#pragma unroll
        for (int u = 0; u < 5; u++) {
          const int cg = (u < 4) ? 4 * (u >> 1) + 2 * h + (u & 1) : 8 + h;
          const int hc = cg >= 5, d0 = 4 * (cg - 5 * hc);
          const bool ok = okT && (hc == (p4 & 1));
          r[u] = trrd(lds + (ok ? L_RAW + (2 * pair + hc) * 800 + (d0 + q4) * 40 + f0 * 2 : L_ZERO));
        }
        lds_ptr tb = lds + L_WTAB + wid * 2560;
        *(LAS bf16x8*)(tb + lane * 16) = cat8(r[0], r[1]);
        *(LAS bf16x8*)(tb + 1024 + lane * 16) = cat8(r[2], r[3]);
        *(LAS s16x4*)(tb + 2048 + lane * 8) = r[4];
      }
      STAMPV(32 + s - 1);
    }
    {
      const rsrc_t rwo = __builtin_amdgcn_make_buffer_rsrc((void*)(br ? woB : woA), (short)0, D_ * D_ * 2, 0x00020000);
      waitv<0>();
      if (wid == 3 && lane == 0) *(LAS u32*)(misc + M_TAG) = ep * 0x9E3779B1u + 0x7F4A7C15u;
      lbar();
      STAMP(15);
#pragma nounroll
      for (int j = 0; j < 5; j++) {
        const int i = wid * 5 + j;
        const u32 o_ = i < 16 ? og + (rt * 8 + (i >> 1)) * 20480 + hg * 2048 + (i & 1) * 1024 + lane * 16
                              : og + (rt * 8 + 2 * (i - 16) + h) * 20480 + 16384 + hg * 512 + l32 * 16;
        __builtin_amdgcn_raw_buffer_store_b128(u32x4{~0u, ~0u, ~0u, ~0u}, rws, o_, 0, 16);
      }
#pragma unroll
      for (int i = 0; i < 7; i++) dma16(rwo, lane * 16, (idx * 25 + (wid + 4 * i < 25 ? wid + 4 * i : 24)) * 1024, lds0 + L_DUMMY);
    }
    if (br) {
      lds_wait_ge(misc + M_WR + 16 + wid * 4, 1u); lds_wait_ge(misc + M_WR + 32 + wid * 4, 1u);
      waitv<7>();
      if (lane == 0 && lds_add_ret(misc + M_C4) == 3) gstore(ws, W_STARTED + ((br * 16 + rt) * 8 + hg) * 4, lds_word(misc + M_TAG));
      if (wid < 2) __builtin_amdgcn_s_sleep(6);
      const u32 dst = W_KVG + ((rt * 8 + hg) * 4 + wid) * 10240 + lane * 16;
#pragma unroll
      for (int i = 0; i < 10; i++) __builtin_amdgcn_raw_buffer_store_b128(*(LAS u32x4*)(lds + L_OWN + wid * 10240 + i * 1024 + lane * 16), rws, dst, i * 1024, 16);
      const u32 tag = RFL(lds_word(misc + M_TAG)), kvf = W_KVFLAG + (rt * 8 + hg) * 128;
      waitv<0>();
      if (lane == 0) gstore(ws, kvf + wid * 4, tag);
      STAMP(4);
      const int dd = 2 - (wid >> 1);
      lds_wait_ge(misc + M_C8, 12u);
#pragma nounroll
      for (int hf = 1; hf >= 0; --hf) {
        const int tl = 2 * hf + (wid & 1);
        if (rt >= dd) poll_tag(ws, kvf - dd * 1024 + tl * 4, tag, false);
        if (hf) { STAMP(11); } else { STAMP(5); lds_inc(misc + M_C0, lane); }
        if (rt >= dd) {
          const u32 src = W_KVG + (((rt - dd) * 8 + hg) * 4 + tl) * 10240;
#pragma nounroll
          for (int i = 0; i < 10; i++) dma16c(rws, lane * 16, src + i * 1024, lds0 + L_HALO + hf * 40960 + wid * 10240 + i * 1024);
        }
      }
    }
  } else {
    const int cw = wid - 4, mi = cw & 3, nj = cw < 8 ? (cw >> 2) + 1 : 0;
    if (cw < 4) {
#pragma unroll
      for (int i = 0; i < 4; i++) ldx(cw * 4 + i, rot, lds0);
    } else if (lane < 60) {
#pragma unroll
      for (int j = 0; j < 4; j++) ldw1((cw - 4) * 4 + j, rot, lds0);
    }
    if (cw < 8) {
      const int c = cw & 3, x = lane + c - 35;
      *(LAS float*)(lds + L_MASK + cw * 256 + lane * 4) = ((x > 0) != (cw >= 4)) ? -1e30f : 0.f;
    } else if (cw == 8 && lane < 4) *(LAS u32x2*)(lds + L_ONES + lane * 640) = u32x2{0x3F803F80u, 0x3F803F80u};
    pre();
    const int row = mi * 32 + i16;
    const u32 xa0 = row * 128 + ((g ^ ((row >> 1) & 7)) << 4), xa1 = xa0 ^ 64;
    const u32 wa = XB + (16 * (g >> 1) + 4 * (g & 1) + q4) * 480 + 8 * p4 + nj * 160;
    int base, HS, RS;
    if (br) {
      if (nj == 0) { base = L_Q; HS = 6144; RS = 48; }
      else { base = L_OWN + (nj - 1) * 40 + mi * 7680; HS = 2560; RS = 80; }
    } else {
      if (nj == 2) { base = L_AV; HS = 6144; RS = 48; }
      else { base = nj ? L_AK : L_AQ; HS = 40; RS = 176; }
    }
    int ea[5];
#pragma unroll
    for (int n = 0; n < 5; n++) { const int c4 = 4 * n + g, hd = (c4 * 13) >> 6, d4 = c4 - 5 * hd; ea[n] = base + hd * HS + row * RS + d4 * 8; PIN(ea[n]); }
    f32x4 acc[2][5] = {};
    auto rd = [&](Frags& f, lds_ptr bb, int ks, int j) {
      if (j < 2) f.x[j] = ld128(bb + (ks ? xa1 : xa0) + j * 2048);
      else { int n = (j - 2) >> 1; if (j & 1) f.wh[n] = trrd(bb + wa + ks * 15360 + 3840 + n * 32); else f.wl[n] = trrd(bb + wa + ks * 15360 + n * 32); }
    };
#define MM(F, i) acc[(i) & 1][(i) >> 1] = MFMA16(cat8(F.wl[(i) >> 1], F.wh[(i) >> 1]), F.x[(i) & 1], acc[(i) & 1][(i) >> 1])
    Frags F0, F1;
    int boff = 0;
    __builtin_amdgcn_s_setprio(3);
    waitv<0>();
    lbar();
    STAMP(14);
#pragma unroll
    for (int j = 0; j < 12; j++) rd(F0, lds, 0, j);
#pragma unroll
    for (int t = 0; t < 10; t++) {
      SBAR();
#pragma unroll
      for (int i = 0; i < 10; i++) { MM(F0, i); if (i < 6) { rd(F1, lds + boff, 1, 2 * i); rd(F1, lds + boff, 1, 2 * i + 1); } SBAR(); }
#pragma unroll
      for (int i = 0; i < 10 - HB; i++) MM(F1, i);
      SBAR();
      boff = ((t + 1) % 3) * STB; asm volatile("" : "+s"(boff));
      if (t < 9) { __builtin_amdgcn_s_waitcnt(0xC07F); __builtin_amdgcn_s_barrier(); } else lds_inc(misc + M_C8, lane);
      asm volatile("" ::: "memory");
#pragma unroll
      for (int i = 10 - HB; i < 10; i++) {
#pragma unroll
        for (int j = (i - 10 + HB) * RPM; j < (i - 9 + HB) * RPM && j < 12; j++) if (t < 9) rd(F0, lds + boff, 0, j);
        MM(F1, i); SBAR();
      }
    }
    STAMP(1);
    relane();
    if (br && nj == 0) {
#pragma unroll
      for (int n = 0; n < 5; n++)
#pragma unroll
        for (int m = 0; m < 2; m++) { const f32x4 v = acc[m][n] * (0.22360679774997896f * 1.4426950408889634f); *(LAS u32x2*)(lds + ea[n] + m * 16 * 48) = u32x2{cvtpk(v[0], v[1]), cvtpk(v[2], v[3])}; }
    } else {
#pragma unroll
      for (int n = 0; n < 5; n++)
#pragma unroll
        for (int m = 0; m < 2; m++) { const f32x4 v = acc[m][n]; *(LAS u32x2*)(lds + ea[n] + m * 16 * RS) = u32x2{cvtpk(v[0], v[1]), cvtpk(v[2], v[3])}; }
    }
    if (br == 0 && nj == 2) {
#pragma unroll
      for (int m = 0; m < 2; m++) *(LAS u32x2*)(lds + L_AV + g * 6144 + (mi * 32 + i16 + 16 * m) * 48 + 40) = u32x2{0x3F803F80u, 0x3F803F80u};
    }
    lds_inc(misc + M_WR + nj * 16 + mi * 4, lane);
    if (nj == 2) lds_inc(misc + M_WV, lane);
    lds_inc(misc + M_C9, lane);
    __builtin_amdgcn_s_setprio(0);
  }
  relane();
  f32x16 o = {};
  PIN(o);
  if (br) lds_wait_ge(misc + M_C9, 12u);
  STAMP(2);
  const u32 tag = RFL(lds_word(misc + M_TAG));
  const int l_wo = br ? LB_WO : LA_WO;
  auto started = [&]() { if (lane == 0) gstore(ws, W_STARTED + ((br * 16 + rt) * 8 + hg) * 4, tag); };
  auto guard = [&]() { poll_tag(ws, W_STARTED + ((br * 16 + rt) * 8 + (lane & 7)) * 4, tag, false); };
  const rsrc_t rwo = __builtin_amdgcn_make_buffer_rsrc((void*)(br ? woB : woA), (short)0, D_ * D_ * 2, 0x00020000);
  u32 wvm = 0, wvt = 0; int wj = 0;
  auto wo_init = [&](int j) {
    wj = j;
    int c = j * 64 + lane; PIN(c);
    const int p = c / 10, ch = c - p * 10;
    const int gg = ((p >> 3) & 2) | ((p >> 2) & 1), tt = (p >> 3) & 1, e = p & 3;
    wvm = ((gg >> 1) * 20 + 4 * (gg & 1) + 8 * tt + e) * 1280 + hg * 160 + ch * 16; wvt = (tt * 80 + gg * 20 + 16 + e) * 1280 + hg * 160 + ch * 16;
  };
  auto wo_dma = [&](int k0, int k1) {
#pragma nounroll
    for (int ks = k0; ks < k1; ks++) {
      if (ks < 16) dma16(rwo, wvm, ks * 40 * 1280, lds0 + l_wo + ks * 5120 + wj * 1024);
      else dma16(rwo, wvt, (ks - 16) * 160 * 1280, lds0 + l_wo + ks * 5120 + wj * 1024);
    }
  };
  const bool wow = wid >= 11;

  if (br) {
    const int hd = wid & 3, qi = wid >> 2;
    const u32 kvf = W_KVFLAG + (rt * 8 + hg) * 128;
    const bf16x8 qa = ld128(lds + pc[0]);
    s16x4 qb = ld64(lds + pc[0] + 32);
    if (h) qb = s16x4{0, 0, 0, 0};
    auto ldmask = [&](int pol) -> f32x16 {
      f32x4 t[4];
#pragma unroll
      for (int j = 0; j < 4; j++) t[j] = *(LAS f32x4*)(lds + pc[6] + pol * 1024 + j * 32);
      return __builtin_shufflevector(__builtin_shufflevector(t[0], t[1], 0, 1, 2, 3, 4, 5, 6, 7), __builtin_shufflevector(t[2], t[3], 0, 1, 2, 3, 4, 5, 6, 7),
                                     0, 1, 2, 3, 4, 5, 6, 7, 8, 9, 10, 11, 12, 13, 14, 15);
    };
    auto pk4 = [&](const f32x16& s, int o_) -> bf16x8 {
      u32x4 p;
#pragma unroll
      for (int j = 0; j < 4; j++) p[j] = cvtpk(ex2(s[o_ + 2 * j]), ex2(s[o_ + 2 * j + 1]));
      return __builtin_bit_cast(bf16x8, p);
    };
    lds_ptr ka_p, kb_p, vp;
    auto seek = [&](int slot) { ka_p = lds + pc[1] + slot * 2560; kb_p = lds + pc[2] + slot * pc[4]; vp = lds + pc[3] + slot * pc[5]; };
    f32x16 s; s16x4 c;
    auto step = [&](int d) {
      ka_p += d * 2560; kb_p += __mul24(d, (int)pc[4]);
      const bf16x8 v0 = cat8(trrd(vp), trrd(vp + 640));
      const bf16x8 ka = ld128(ka_p); const s16x4 kb = ld64(kb_p);
      const bf16x8 v1 = cat8(trrd(vp + 1280), trrd(vp + 1920));
      vp += __mul24(d, (int)pc[5]);
      SBAR();
      o = MFMA32(v0, pk4(s, 0), o);
      const bf16x8 p1 = pk4(s, 8);
      SBAR();
      s = MFMA32K8(kb, c, MFMA32(ka, qa, f32x16{}));
      o = MFMA32(v1, p1, o);
      SBAR();
    };
    auto last = [&]() {
      const bf16x8 v0 = cat8(trrd(vp), trrd(vp + 640)), v1 = cat8(trrd(vp + 1280), trrd(vp + 1920));
      o = MFMA32(v0, pk4(s, 0), o);
      o = MFMA32(v1, pk4(s, 8), o);
    };
    lds_inc(misc + M_C5, lane);
    if (qi == 0) __builtin_amdgcn_s_setprio(3);
    seek(L_OWN / 2560 + qi * 4 + hd);
    {
      const s16x4 kb = ld64(kb_p);
      s = MFMA32K8(kb, qb, MFMA32(ld128(ka_p), qa, ldmask(0)));
      float mm = fmaxf(s[0], s[1]);
#pragma unroll
      for (int r = 2; r < 16; r += 2) mm = fmaxf(fmaxf(mm, s[r]), s[r + 1]);
      auto rr = __builtin_amdgcn_permlane32_swap(__float_as_uint(mm), __float_as_uint(mm), false, false);
      const u32 nm = cvtpk(-fmaxf(__uint_as_float(rr[0]), __uint_as_float(rr[1])), 0.f);
      const u32x2 qw = __builtin_bit_cast(u32x2, qb);
      c = __builtin_bit_cast(s16x4, u32x2{h ? nm : qw.x, qw.y});
      s = MFMA32K8(kb, __builtin_bit_cast(s16x4, u32x2{h ? nm : 0u, 0u}), s);
    }
#pragma nounroll
    for (int k = 0; k < qi; ++k) step(-4);
    last();
    lds_inc(misc + M_C1, lane);
    STAMP(3);
    if (qi == 0) { waitv<0>(); lds_inc(misc + M_C7, lane); }
    if (wow) wo_init(wid - 11);
    lds_wait_ge(misc + M_C0, 4u);
    int wk = 20;
    if (rt > 0) {
      int n1 = 2, nn = 4, km = -1;
      if (rt >= 2) { n1 = qi < 3 ? 4 : 3; nn = 8 - qi; km = qi >= 2 ? n1 - 1 : nn - 1; }
      seek(28 + hd);
      s = MFMA32K8(ld64(kb_p), c, MFMA32(ld128(ka_p), qa, f32x16{}));
      auto step2 = [&](int k, f32x16& sc, f32x16& sn) {
        const int rem = nn - k + (wow ? 2 : 0);
        if (rem >= 6) __builtin_amdgcn_s_setprio(3); else if (rem >= 4) __builtin_amdgcn_s_setprio(2); else if (rem >= 2) __builtin_amdgcn_s_setprio(1); else __builtin_amdgcn_s_setprio(0);
        int d = -4;
        if (k == n1) {
          d = 4 * n1 - 20;
          lds_wait_ge(misc + M_C7, 4u);
          if (wid == 10) guard();
        }
        if (wow && wk > 0) {
          const u32 c1 = RFL(lds_word(misc + M_C1)), c7 = RFL(lds_word(misc + M_C7)), c3 = RFL(lds_word(misc + M_C3));
          const int lim = c3 >= 16 ? 0 : (c1 >= 16 && c7 >= 4) ? 5 : 20;
          const int k0 = wk - 6 > lim ? wk - 6 : lim;
          if (k0 < wk) { wo_dma(k0, wk); wk = k0; }
        }
        ka_p += d * 2560; kb_p += __mul24(d, (int)pc[4]);
        const bf16x8 ka = ld128(ka_p); const s16x4 kb = ld64(kb_p);
        const bf16x8 v0 = cat8(trrd(vp), trrd(vp + 640)), v1 = cat8(trrd(vp + 1280), trrd(vp + 1920));
        vp += __mul24(d, (int)pc[5]);
        SBAR();
        float e[8]; u32x4 p1;
        bf16x8 p0 = pk4(sc, 0); PIN(p0);
        SBAR(); sn = MFMA32(ka, qa, f32x16{}); SBAR();
#pragma unroll
        for (int j = 0; j < 4; j++) e[j] = ex2(sc[8 + j]);
        SBAR(); sn = MFMA32K8(kb, c, sn); SBAR();
#pragma unroll
        for (int j = 4; j < 8; j++) e[j] = ex2(sc[8 + j]);
        SBAR(); o = MFMA32(v0, p0, o); SBAR();
#pragma unroll
        for (int j = 0; j < 4; j++) p1[j] = cvtpk(e[2 * j], e[2 * j + 1]);
        SBAR(); o = MFMA32(v1, __builtin_bit_cast(bf16x8, p1), o); SBAR();
        if (k == km) sn += ldmask(1);
        if (k == n1) lds_inc(misc + M_C3, lane);
      };
      f32x16 s2;
#pragma nounroll
      for (int k = 1;; k += 2) {
        step2(k, s, s2);
        if (k + 1 == nn) { s = s2; break; }
        step2(k + 1, s2, s);
        if (k + 2 == nn) break;
      }
      if (wow && wk > 0) { lds_wait_ge(misc + M_C3, 16u); wo_dma(0, wk); }
      last();
    } else {
      if (wow) { lds_wait_ge(misc + M_C1, 16u); wo_dma(0, 20); }
      if (wid == 10) guard();
    }
    float l = h ? o[8] : o[12];
    if (__builtin_expect(__any(!(l < 1.8e19f)), 0)) {
      const int dd = lane % 3;
      poll_tag(ws, kvf - (rt - dd < 0 ? 0 : dd) * 1024 + 4 * (lane & 3), tag, false);
      float q[20], a[20], mx = -3e38f; l = 0.f;
      const u32x4 qw = __builtin_bit_cast(u32x4, qa); const u32x2 qv = __builtin_bit_cast(u32x2, qb);
#pragma unroll
      for (int j = 0; j < 10; j++) {
        const u32 w = j < 8 ? __shfl(qw[j & 3], l32 + 32 * (j >> 2)) : __shfl(qv[j & 1], l32);
        q[2 * j] = blo(w); q[2 * j + 1] = bhi(w); a[2 * j] = 0.f; a[2 * j + 1] = 0.f;
      }
      const int row = rt * 128 + 32 * qi + l32;
#pragma nounroll
      for (int t = 0; t < 128; ++t) {
        const int j = row - 2 * t - h;
        if (j >= 0) {
          const u32 src = W_KVG + (((j >> 7) * 8 + hg) * 4 + ((j >> 5) & 3)) * 10240 + hd * 2560 + (j & 31) * 80;
          u32 w[20];
#pragma unroll
          for (int i = 0; i < 5; i++) { const u32x4 v = __builtin_amdgcn_raw_buffer_load_b128(rws, src, i * 16, 16); w[4 * i] = v.x; w[4 * i + 1] = v.y; w[4 * i + 2] = v.z; w[4 * i + 3] = v.w; }
          float sc = 0.f;
#pragma unroll
          for (int i = 0; i < 10; i++) sc += q[2 * i] * blo(w[i]) + q[2 * i + 1] * bhi(w[i]);
          const float mn = fmaxf(mx, sc), f = ex2(mx - mn), p = ex2(sc - mn);
          mx = mn; l = l * f + p;
#pragma unroll
          for (int i = 0; i < 10; i++) { a[2 * i] = a[2 * i] * f + p * blo(w[10 + i]); a[2 * i + 1] = a[2 * i + 1] * f + p * bhi(w[10 + i]); }
        }
      }
      const float mo = __shfl_xor(mx, 32), mn = fmaxf(mx, mo), f = ex2(mx - mn);
      l *= f; l += __shfl_xor(l, 32);
#pragma unroll
      for (int i = 0; i < 20; i++) { a[i] *= f; a[i] += __shfl_xor(a[i], 32); }
#pragma unroll
      for (int r = 0; r < 8; r++) o[r] = h ? a[(r & 3) + 8 * (r >> 2) + 4] : a[(r & 3) + 8 * (r >> 2)];
#pragma unroll
      for (int r = 8; r < 12; r++) o[r] = a[8 + r];
    }
    const float rl = __builtin_amdgcn_rcpf(l);
    const u32x4 w0 = {cvtpk(o[0] * rl, o[1] * rl), cvtpk(o[2] * rl, o[3] * rl), cvtpk(o[4] * rl, o[5] * rl), cvtpk(o[6] * rl, o[7] * rl)};
    const u32x2 w1 = {cvtpk(o[8] * rl, o[9] * rl), cvtpk(o[10] * rl, o[11] * rl)};
    __builtin_amdgcn_raw_buffer_store_b128(w0, rws, pc[7], 0, 16);
    __builtin_amdgcn_raw_buffer_store_b64(w1, rws, pc[8], 0, 16);
    STAMP(6);
    __builtin_amdgcn_s_setprio(0);
  } else {
    const int tg = wid & 3, pair = (wid >> 2) & 1, isq = wid >> 3;
    if (!isq) __builtin_amdgcn_s_setprio(2);
    u32 phi[20];
    {
      lds_wait_ge(misc + M_WR + (isq ? 0 : 16) + tg * 4, 1u);
      const bf16x8 b0 = ld128(lds + pc[0]), a00 = ld128(lds + pc[1]), a10 = ld128(lds + pc[1] + 2560);
      const bf16x8 b1 = ld128(lds + pc[0] + 32), a01 = ld128(lds + pc[1] + 1024), a11 = ld128(lds + pc[1] + 2560 + 1024);
      const s16x4 b2 = ld64(lds + pc[8]), a02 = ld64(lds + pc[2]), a12 = ld64(lds + pc[2] + 2560);
      lds_inc(misc + M_C5, lane);
      if (wow) { wo_init(wid - 11); lds_wait_ge(misc + M_C5, 16u); wo_dma(6, 20); waitv<0>(); lds_inc(misc + M_C6, lane); }
      f32x16 t0 = MFMA32(a00, b0, f32x16{}), t1 = MFMA32(a10, b0, f32x16{});
      t0 = MFMA32(a01, b1, t0); t1 = MFMA32(a11, b1, t1);
      t0 = MFMA32K8(a02, b2, t0); t1 = MFMA32K8(a12, b2, t1);
      float p[20];
#pragma unroll
      for (int j = 0; j < 10; j++) {
        const u32 w = j < 8 ? cvtpk(t0[2 * j], t0[2 * j + 1]) : cvtpk(t1[2 * j - 16], t1[2 * j - 15]);
        p[2 * j] = blo(w); p[2 * j + 1] = bhi(w);
      }
      float mx = fmaxf(p[0], p[1]), mn = fminf(p[0], p[1]);
#pragma unroll
      for (int j = 2; j < 20; j += 2) { mx = fmaxf(fmaxf(mx, p[j]), p[j + 1]); mn = fminf(fminf(mn, p[j]), p[j + 1]); }
      const float L2E = 1.4426950408889634f;
      const f32x2 sl = {L2E, -L2E}, of = {-mx * L2E, mn * L2E};
      f32x2 e[20], sa = {0.f, 0.f}, sb = {0.f, 0.f};
#pragma unroll
      for (int j = 0; j < 20; j++) {
        const f32x2 a = __builtin_elementwise_fma(f32x2{p[j], p[j]}, sl, of);
        e[j] = f32x2{ex2(a[0]), ex2(a[1])};
        if (j & 1) sb += e[j]; else sa += e[j];
      }
      sa += sb;
      const f32x2 rr = {__builtin_amdgcn_rcpf(sa[0]), __builtin_amdgcn_rcpf(sa[1])};
#pragma unroll
      for (int j = 0; j < 20; j++) phi[j] = cvtpk2(e[j] * rr);
    }
    if (!isq) {
      lds_wait_ge(misc + M_WV, 4u);
      bf16x8 vb[4];
#pragma unroll
      for (int ks = 0; ks < 4; ks++) vb[ks] = cat8(trrd(lds + pc[3] + ks * 32 * 48), trrd(lds + pc[3] + ks * 32 * 48 + 4 * 48));
      lds_wait_ge(misc + M_C8, 12u);
#pragma unroll
      for (int j = 0; j < 5; j++) *(LAS u32x4*)(lds + pc[4] + 16 * j) = u32x4{phi[4 * j], phi[4 * j + 1], phi[4 * j + 2], phi[4 * j + 3]};
      lds_inc(misc + M_C0 + pair * 4, lane);
      STAMP(3);
      lds_wait_ge(misc + M_C0 + pair * 4, 4u);
      f32x4 kv[3] = {};
#pragma unroll
      for (int ks = 0; ks < 4; ks++)
#pragma unroll
        for (int T = 0; T < 3; T++) kv[T] = MFMA16(cat8(trrd(lds + pc[5] + ks * 2560 + T * 32), trrd(lds + pc[5] + ks * 2560 + 320 + T * 32)), vb[ks], kv[T]);
#pragma unroll
      for (int T = 0; T < 3; T++)
        __builtin_amdgcn_raw_buffer_store_b128(u32x4{cvtpk(kv[T][0], kv[T][1]), tag, cvtpk(kv[T][2], kv[T][3]), tag}, rws, pc[T < 2 ? 6 : 7], T * ((tg & 1) ? 320 : 1024), 16);
      waitv<3>();
      lds_inc(misc + M_C1, lane);
      STAMP(4);
      if (wid >= 1 && wid < 6) { wo_init(wid - 1); lds_wait_ge(misc + M_C1, 8u); wo_dma(0, 6); waitv<0>(); lds_inc(misc + M_C6, lane); }
      if (wid == 0) {
        const int sl = (rt & 3) * 53 + lane; const bool act = lane < 53 && sl < 210;
        const u32 src = act ? W_PART + (hg * 4 + (rt >> 2)) * 16 * PARTB + sl * 16 : OOB;
        float s0 = 0.f, s1 = 0.f, s2 = 0.f, s3 = 0.f;
        lds_wait_ge(misc + M_C6, 10u);
#pragma nounroll
        for (u32 s = 0; s < SPIN_CAP; ++s) {
          bool ok = true; s0 = 0.f; s1 = 0.f; s2 = 0.f; s3 = 0.f;
          u32x4 v[16];
#pragma unroll
          for (int r = 0; r < 16; r++) v[r] = __builtin_amdgcn_raw_buffer_load_b128(rws, src, r * PARTB, 16);
#pragma unroll
          for (int r = 0; r < 16; r++) { ok &= (v[r].y == tag) & (v[r].w == tag); s0 += blo(v[r].x); s1 += bhi(v[r].x); s2 += blo(v[r].z); s3 += bhi(v[r].z); }
          if (__all(ok || !act)) break;
          asm volatile("" ::: "memory");
        }
        __builtin_amdgcn_raw_buffer_store_b128(u32x4{cvtpk(s0, s1), tag, cvtpk(s2, s3), tag}, rws, act ? W_SLICE + (hg * 16 + rt) * 1024 + lane * 16 : OOB, 0, 16);
        lds_inc(misc + M_C4, lane);
        STAMP(5);
      } else if (wid == 1) { started(); lds_wait_ge(misc + M_C4, 1u); guard(); }
      else if (wid >= 4) {
        const int hh = wid - 4;
        const u32 src = W_SLICE + (hg * 16 + hh * 4) * 1024 + lane * 16;
        int da[4];
#pragma unroll
        for (int j = 0; j < 4; j++) {
          const int sl = j * 53 + lane; int d, ph;
          if (sl < 160) { d = sl & 15; ph = (sl >> 4) * 4; }
          else { const int s2 = sl - 160, gg = s2 / 5; d = 16 + s2 - 5 * gg; ph = 4 * gg; }
          da[j] = L_KVT + hh * 1680 + d * 80 + ph * 2;
        }
        lds_wait_ge(misc + M_C5, 16u);
        lds_wait_ge(misc + M_C4, 1u);
        u32x4 v[4];
#pragma nounroll
        for (u32 s = 0; s < SPIN_CAP; ++s) {
          bool ok = true;
#pragma unroll
          for (int r = 0; r < 4; r++) v[r] = __builtin_amdgcn_raw_buffer_load_b128(rws, src, r * 1024, 16);
#pragma unroll
          for (int r = 0; r < 4; r++) ok &= ((v[r].y == tag) & (v[r].w == tag)) | (lane >= (r < 3 ? 53 : 51));
          if (__all(ok)) break;
          asm volatile("" ::: "memory");
        }
#pragma unroll
        for (int r = 0; r < 4; r++) if (lane < (r < 3 ? 53 : 51)) *(LAS u32x2*)(lds + da[r]) = u32x2{v[r].x, v[r].z};
        lds_inc(misc + M_C3, lane);
        STAMP(11);
      }
      __builtin_amdgcn_s_setprio(0);
    } else {
      lds_ptr a0 = lds + pc[3], a1 = lds + pc[4]; const u32 om = pc[5], ot = pc[6];
      STAMP(12);
      lds_wait_ge(misc + M_C3, 4u);
      f32x16 t0 = {}, t1 = {};
#pragma unroll
      for (int j = 0; j < 5; j++) {
        const bf16x8 bq = __builtin_bit_cast(bf16x8, u32x4{phi[4 * j], phi[4 * j + 1], phi[4 * j + 2], phi[4 * j + 3]});
        t1 = MFMA32(ld128(a1 + 16 * j), bq, t1); t0 = MFMA32(ld128(a0 + 16 * j), bq, t0);
      }
      const float rl = __builtin_amdgcn_rcpf(t1[4] + 1e-6f);
      const u32x4 w0 = {cvtpk(t0[0] * rl, t0[1] * rl), cvtpk(t0[2] * rl, t0[3] * rl), cvtpk(t0[8] * rl, t0[9] * rl), cvtpk(t0[10] * rl, t0[11] * rl)};
      const u32x4 w1 = {cvtpk(t0[4] * rl, t0[5] * rl), cvtpk(t0[6] * rl, t0[7] * rl), cvtpk(t0[12] * rl, t0[13] * rl), cvtpk(t0[14] * rl, t0[15] * rl)};
      const u32x2 w2 = {cvtpk(t1[0] * rl, t1[1] * rl), cvtpk(t1[2] * rl, t1[3] * rl)};
      __builtin_amdgcn_raw_buffer_store_b128(w0, rws, om, 0, 16);
      __builtin_amdgcn_raw_buffer_store_b128(w1, rws, om, 256, 16);
      __builtin_amdgcn_raw_buffer_store_b64(w2, rws, ot, 0, 16);
      STAMP(6);
    }
  }
  STAMP(7);
  const u32 ofl = W_OFLAG + (br * 16 + rt) * 128;
  auto published = [&]() { if (lane == 0 && lds_add_ret(misc + M_C2) == 15) gstore(ws, ofl + hg * 4, tag); };
  relane();
  lbar();
  STAMP(8);
  if (wid >= 8) {
    waitv<0>();
    if (wow && br) lds_inc(misc + M_C6, lane);
    published();
    if (b == 0 && wid == 15) {
#pragma nounroll
      for (u32 s = 0; s < SPIN_CAP; ++s) {
        const u32x4 v = __builtin_amdgcn_raw_buffer_load_b128(rws, W_STARTED + lane * 16, 0, 16);
        if (__all(v.x == tag && v.y == tag && v.z == tag && v.w == tag)) break;
        __builtin_amdgcn_s_sleep(2);
      }
      if (lane == 0) gstore(ws, W_EPOCH, tag);
    }
  } else {
    const int mi = wid & 3, kh = wid >> 2;
    lds_ptr red = lds + L_RED;
    const u32 obm = og + (rt * 8 + mi * 2) * 20480 + kh * 8192 + lane * 16, obt = og + (rt * 8 + mi * 2) * 20480 + kh * 2048 + lane * 8;
    u32x4 of[10][2];
    auto ldo = [&](int s) {
#pragma unroll
      for (int m = 0; m < 2; m++) {
        if (s < 8) of[s][m] = __builtin_amdgcn_raw_buffer_load_b128(rws, obm, m * 20480 + s * 1024, 16);
        else {
          const u32x2 a = __builtin_amdgcn_raw_buffer_load_b64(rws, obt, m * 20480 + (s + 8) * 1024, 16);
          const u32x2 c = __builtin_amdgcn_raw_buffer_load_b64(rws, obt, m * 20480 + (s + 8) * 1024 + 512, 16);
          of[s][m] = u32x4{a.x, a.y, c.x, c.y};
        }
      }
    };
    u32 wm = l_wo + (16 * (g >> 1) + 4 * (g & 1) + q4) * 160 + 8 * p4, wt = wm + (16 + 2 * kh) * 5120; wm += kh * 8 * 5120;
    PIN(wm); PIN(wt);
    bf16x8 wf[5];
    auto ldw = [&](int s, int n) { lds_ptr p = lds + (s < 8 ? wm + s * 5120 : wt + (s - 8) * 5120) + n * 32; wf[n] = cat8(trrd(p), trrd(p + 1280)); };
    f32x4 acc[2][5];
    constexpr int PF = 6;
#pragma nounroll
    for (int pass = 0;; ++pass) {
      if (pass) poll_tag(ws, ofl + (lane & 7) * 4, tag, false);
#pragma nounroll
      for (int tr = 0; tr < 64; ++tr) {
#pragma unroll
        for (int s = 9; s > 5; s--) ldo(s);
        asm volatile("" ::: "memory");
        u32 mx = 0;
#pragma unroll
        for (int s = 8; s < 10; s++)
#pragma unroll
          for (int m = 0; m < 2; m++) mx = max(max(mx, max(of[s][m].x, of[s][m].y)), max(of[s][m].z, of[s][m].w));
        if (pass || !__any(mx == ~0u)) break;
      }
      STAMP(9);
#pragma unroll
      for (int s = 5; s > 9 - PF; s--) ldo(s);
      if (!pass) published();
      lds_wait_ge(misc + M_C6, br ? 5u : 10u);
#pragma unroll
      for (int n = 0; n < 5; n++) { ldw(9, n); acc[0][n] = f32x4{}; acc[1][n] = f32x4{}; }
#pragma unroll
      for (int s = 9; s >= 0; s--) {
        SBAR();
#pragma unroll
        for (int n = 0; n < 5; n++) {
          acc[0][n] = MFMA16(wf[n], __builtin_bit_cast(bf16x8, of[s][0]), acc[0][n]);
          acc[1][n] = MFMA16(wf[n], __builtin_bit_cast(bf16x8, of[s][1]), acc[1][n]);
          if (s > 0) ldw(s - 1, n);
          SBAR();
        }
        if (s - PF >= 0) ldo(s - PF);
      }
      const float chk = acc[0][0][0] + acc[1][0][0];
      if (pass || !__any(chk != chk)) break;
    }
    STAMP(13);
    relane();
#pragma unroll
    for (int n = 0; n < 5; n++) *(LAS f32x4*)(red + (wid ^ 4) * 5120 + n * 1024 + lane * 16) = kh ? acc[0][n] : acc[1][n];
    lbar();
    lds_ptr mine = red + wid * 5120;
    u32x2 pk[5];
#pragma unroll
    for (int n = 0; n < 5; n++) { const f32x4 v = (kh ? acc[1][n] : acc[0][n]) + *(LAS f32x4*)(mine + n * 1024 + lane * 16); pk[n] = u32x2{cvtpk(v[0], v[1]), cvtpk(v[2], v[3])}; }
    asm volatile("" ::: "memory");
#pragma unroll
    for (int n = 0; n < 5; n++) *(LAS u32x2*)(mine + i16 * 176 + n * 32 + g * 8) = pk[n];
    asm volatile("" ::: "memory");
    const rsrc_t ro = __builtin_amdgcn_make_buffer_rsrc((void*)out, (short)0, S_ * 1280 * 2, 0x00020000);
    const u32 obase = (rt * 128 + mi * 32 + kh * 16) * 2560 + br * 1280 + hg * 160;
#pragma unroll
    for (int i = 0; i < 3; i++) {
      const int c = i * 64 + lane, r = c / 10, ch = c - r * 10;
      const u32x4 v = *(LAS u32x4*)(mine + r * 176 + ch * 16);
      __builtin_amdgcn_raw_buffer_store_b128(v, ro, c < 160 ? obase + r * 2560 + ch * 16 : OOB, 0, 0);
    }
  }
  asm volatile(".Lcode_end:");
  STAMP(10);
}

extern "C" void kernel_launch(void* const* d_in, const int* in_sizes, int n_in, void* d_out, int out_size, void* d_ws, size_t ws_size, hipStream_t stream) {
  (void)hipFuncSetAttribute((const void*)fused, hipFuncAttributeMaxDynamicSharedMemorySize, LDS_BYTES);
  fused<<<256, 1024, LDS_BYTES, stream>>>((const u16*)d_in[0], (const u16*)d_in[1], (const u16*)d_in[2], (const u16*)d_in[3],
                                          (const u16*)d_in[4], (const u16*)d_in[5], (const u16*)d_in[6], (u16*)d_out, (char*)d_ws);
}
```

```cpp
#include <hip/hip_runtime.h>
#include <hip/hip_bf16.h>
#include <type_traits>

typedef unsigned short u16;
typedef unsigned u32;
typedef short bf16x8 __attribute__((ext_vector_type(8)));
typedef short s16x4 __attribute__((ext_vector_type(4)));
typedef float f32x4 __attribute__((ext_vector_type(4)));
typedef float f32x16 __attribute__((ext_vector_type(16)));
typedef float f32x2 __attribute__((ext_vector_type(2)));
typedef __bf16 bf2_t __attribute__((ext_vector_type(2)));
typedef u32 u32x4 __attribute__((ext_vector_type(4)));
typedef u32 u32x2 __attribute__((ext_vector_type(2)));
#define LAS __attribute__((address_space(3)))
#define GAS __attribute__((address_space(1)))
typedef LAS char* lds_ptr;
typedef __amdgpu_buffer_rsrc_t rsrc_t;
#define DI __device__ __forceinline__
#define RFL(x) __builtin_amdgcn_readfirstlane(x)
#define SBAR() __builtin_amdgcn_sched_barrier(0)
#define LP(x) ((lds_ptr)(size_t)(u32)(x))
#define PIN(x) asm volatile("" : "+v"(x))
#define RLX_AGENT __ATOMIC_RELAXED, __HIP_MEMORY_SCOPE_AGENT
#define RLX_WG __ATOMIC_RELAXED, __HIP_MEMORY_SCOPE_WORKGROUP
#define XMAP 0
#define HB 10
#define RPM ((12 + HB - 2) / (HB - 1))
#define S1DELAY 8
#define P3V 1
#define P3S 12
#define P3L 7
#define ROT 4

constexpr int S_ = 2048, D_ = 640, N3 = 1920;
constexpr int XB = 128 * 64 * 2, WB = 64 * 240 * 2, STB = XB + WB;
constexpr int L_RAW = 3 * STB;
constexpr int L_ZERO = L_RAW + 3200;
constexpr int L_WTAB = L_RAW + 4096;
constexpr int L_MASK = 158720;
constexpr int L_ONES = 160768;
constexpr int L_MISC = L_ONES + 64;
constexpr int L_DUMMY = 162816;
constexpr int LDS_BYTES = 163840;
constexpr int L_HALO = 0, L_OWN = 81920, L_Q = 122880, LB_WO = 56320, L_RED = 0;
constexpr int L_PK = 0, L_AV = 47104, L_AK = 71680, L_AQ = L_AK + 22528, L_KVT = L_WTAB, LA_WO = 40960;
constexpr int M_TAG = 0, M_C0 = 16, M_C1 = 32, M_C2 = 36, M_C3 = 40, M_C4 = 44, M_C5 = 48, M_C6 = 52, M_C7 = 56, M_C9 = 60, M_WR = 64, M_C8 = 112, M_WV = 116, M_P0 = 120, M_P1 = 124, M_D = 128, M_X = 144, M_AD = 176;
constexpr u32 W_EPOCH = 0, W_STARTED = 1024, W_KVFLAG = 4096, W_OFLAG = 32768, W_SLICE = 65536;
constexpr u32 W_PART = 1u << 20, W_KVG = 8u << 20, W_OG = 16u << 20, W_TOTAL = 24u << 20;
constexpr u32 PARTB = 3456;
constexpr u32 OOB = 0x80000000u;
constexpr u32 SPIN_CAP = 1u << 18;

DI void dma16(rsrc_t r, u32 voff, u32 soff, u32 ldsaddr) {
  asm volatile("s_mov_b32 m0, %0\n\ts_nop 0\n\tbuffer_load_dwordx4 %1, %2, %3 offen lds" ::"s"(ldsaddr), "v"(voff), "s"(r), "s"(soff) : "m0", "memory");
}
DI void dma16c(rsrc_t r, u32 voff, u32 soff, u32 ldsaddr) {
  asm volatile("s_mov_b32 m0, %0\n\ts_nop 0\n\tbuffer_load_dwordx4 %1, %2, %3 offen sc1 lds" ::"s"(ldsaddr), "v"(voff), "s"(r), "s"(soff) : "m0", "memory");
}
DI s16x4 trrd(lds_ptr p) { return __builtin_bit_cast(s16x4, __builtin_amdgcn_ds_read_tr16_b64_v4i16((LAS s16x4*)p)); }
DI bf16x8 cat8(s16x4 a, s16x4 b) { return __builtin_shufflevector(a, b, 0, 1, 2, 3, 4, 5, 6, 7); }
DI u32 cvtpk2(f32x2 v) { return __builtin_bit_cast(u32, __builtin_convertvector(v, bf2_t)); }
DI u32 cvtpk(float lo, float hi) { return cvtpk2(f32x2{lo, hi}); }
DI float blo(u32 w) { return __uint_as_float(w << 16); }
DI float bhi(u32 w) { return __uint_as_float(w & 0xffff0000u); }
template <int N> DI void waitv() { asm volatile("s_waitcnt vmcnt(%0)" ::"i"(N) : "memory"); }
DI void lbar() { asm volatile("s_waitcnt lgkmcnt(0)\n\ts_barrier" ::: "memory"); }
DI void waitl() { asm volatile("s_waitcnt lgkmcnt(0)" ::: "memory"); }
DI bf16x8 ld128(lds_ptr p) { return *(LAS bf16x8*)p; }
DI s16x4 ld64(lds_ptr p) { return *(LAS s16x4*)p; }
#define MFMA16(a, b, c) __builtin_amdgcn_mfma_f32_16x16x32_bf16(a, b, c, 0, 0, 0)
#define MFMA32(a, b, c) __builtin_amdgcn_mfma_f32_32x32x16_bf16(a, b, c, 0, 0, 0)
#define MFMA32K8(a, b, c) __builtin_amdgcn_mfma_f32_32x32x8bf16_1k(a, b, c, 0, 0, 0)
DI float ex2(float x) { return __builtin_amdgcn_exp2f(x); }
DI u32 gload(char* ws, u32 off) { return __hip_atomic_load((GAS u32*)(ws + off), RLX_AGENT); }
DI void gstore(char* ws, u32 off, u32 v) { __hip_atomic_store((GAS u32*)(ws + off), v, RLX_AGENT); }
DI u32 lds_word(lds_ptr p) { return __hip_atomic_load((LAS u32*)p, RLX_WG); }
DI void lds_inc(lds_ptr p, int) {
  unsigned long long sv;
  asm volatile("s_mov_b64 %0, exec\n\ts_mov_b64 exec, 1\n\tds_add_u32 %1, %2\n\ts_mov_b64 exec, %0" : "=&s"(sv) : "v"((u32)(size_t)p), "v"(1u) : "memory");
}
DI u32 lds_add_ret(lds_ptr p) { return __hip_atomic_fetch_add((LAS u32*)p, 1u, RLX_WG); }
DI void lds_wait_ge(lds_ptr p, u32 want) {
#pragma nounroll
  for (u32 s = 0; s < SPIN_CAP * 8; ++s) { if (RFL(lds_word(p)) >= want) break; __builtin_amdgcn_s_sleep(1); }
  asm volatile("" ::: "memory");
}
DI void poll_tag(char* ws, u32 off, u32 tag, bool done) {
#pragma nounroll
  for (u32 s = 0; s < SPIN_CAP; ++s) { const u32 v = gload(ws, off); if (__all(done || v == tag)) break; __builtin_amdgcn_s_sleep(1); }
  asm volatile("" ::: "memory");
}

#define STAMP(i)
#define STAMPV(i)

struct Frags { bf16x8 x[2]; s16x4 wl[5], wh[5]; };

__global__ __launch_bounds__(1024) void fused(const u16* xA, const u16* wA, const u16* wphi, const u16* woA,
                                              const u16* xB, const u16* wB, const u16* woB, u16* out, char* ws) {
  extern __shared__ __attribute__((aligned(16))) char smem[];
  unsigned long long pc0; u32 len;
  asm volatile("s_getpc_b64 %0\n.Lcode_ref:\n\ts_mov_b32 %1, .Lcode_end-.Lcode_ref" : "=s"(pc0), "=s"(len));
  lds_ptr lds = (lds_ptr)smem;
  const u32 lds0 = (u32)(size_t)lds;
  const int b = blockIdx.x, xcd = b & 7, idx = b >> 3;
  const int br = xcd >> 2, rt = (xcd & 3) * 4 + (idx >> 3), hg = idx & 7;
  int lane = threadIdx.x & 63; const int wid = RFL(threadIdx.x >> 6);
  int g = lane >> 4, i16 = lane & 15, q4 = i16 >> 2, p4 = lane & 3, h = lane >> 5, l32 = lane & 31;
  auto relane = [&]() { PIN(lane); lane &= 63; g = lane >> 4; i16 = lane & 15; q4 = i16 >> 2; p4 = lane & 3; h = lane >> 5; l32 = lane & 31; };
  const rsrc_t rws = __builtin_amdgcn_make_buffer_rsrc((void*)ws, (short)0, W_TOTAL, 0x00020000);
  lds_ptr misc = lds + L_MISC;
  STAMP(0);

  const rsrc_t rx = __builtin_amdgcn_make_buffer_rsrc((void*)((br ? xB : xA) + (size_t)rt * 128 * D_), (short)0, 128 * D_ * 2, 0x00020000);
  const rsrc_t rw = __builtin_amdgcn_make_buffer_rsrc((void*)(br ? wB : wA), (short)0, D_ * N3 * 2, 0x00020000);
  const int cls = ((idx >> 3) + idx) & 3;
  const unsigned long long orda = (cls & 1) ? 0x8796435201ull : 0x9768532410ull, ordb = (cls & 1) ? 0x9865742130ull : 0x9875463102ull, ord = (cls & 2) ? ordb : orda;
  const int rot = (int)(ord & 15);
  const u32 vx0 = (lane >> 3) * 1280 + (((lane & 7) ^ (lane >> 4)) << 4), vx1 = vx0 ^ 64;
  const int wc = lane >= 30 ? lane - 30 : lane;
  const u32 vw = (lane >= 30 ? 3840 : 0) + (wc >= 20 ? 2240 : wc >= 10 ? 1120 : 0) + hg * 160 + wc * 16;
  auto ldx = [&](int i, int kt, u32 buf) { dma16(rx, (i & 1) ? vx1 : vx0, 128 * kt + i * 8 * 1280, buf + i * 1024); };
  auto ldw1 = [&](int j, int kt, u32 buf) { const int p = 2 * j, k = (p & 0x33) | ((p & 4) << 1) | ((p & 8) >> 1); dma16(rw, vw, (64 * kt + k) * 3840, buf + XB + j * 960); };
  const u32 og = W_OG + br * (128 * 20480);
  u32 pc[9] = {};
  auto pre = [&]() {
    auto og_main = [&](int row, int hd, int hf) -> u32 { return og + (row >> 4) * 20480 + ((hg * 2 + (hd >> 1)) << 10) + (((2 * (hd & 1) + hf) * 16 + (row & 15)) << 4); };
    auto og_tail = [&](int row, int hd) -> u32 { return og + (row >> 4) * 20480 + 16384 + hg * 512 + ((hd * 16 + (row & 15)) << 3); };
    if (br) {
      const int hd = wid & 3, qi = wid < 8 ? 1 - (wid >> 2) : wid >> 2, u = 35 - l32 + 4 * h; const bool vones = (g & 1) && p4;
      pc[0] = L_Q + hd * 6144 + (32 * qi + l32) * 48 + 16 * h;
      pc[1] = hd * 2560 + l32 * 80 + 16 * h;
      pc[2] = h ? L_ONES : hd * 2560 + l32 * 80 + 32;
      pc[3] = vones ? L_ONES : hd * 2560 + (4 * h + q4) * 80 + 40 + 32 * (g & 1) + 8 * p4;
      pc[4] = h ? 0 : 10240; pc[5] = vones ? 0 : 10240;
      pc[6] = L_MASK + (u & 3) * 256 + (u & ~3) * 4;
      pc[7] = og_main(rt * 128 + 32 * qi + l32, hd, h);
      pc[8] = h ? OOB : og_tail(rt * 128 + 32 * qi + l32, hd);
      pc[0] += lds0; pc[1] += lds0; pc[2] += lds0; pc[3] += lds0; pc[6] += lds0;
    } else {
      const int tg = wid & 3, pair = (wid >> 2) & 1, isq = wid >> 3;
      pc[0] = (isq ? L_AQ : L_AK) + (32 * tg + l32) * 176 + pair * 80 + 16 * h;
      pc[1] = L_WTAB + pair * 5120 + lane * 16;
      pc[2] = L_WTAB + pair * 5120 + 2048 + lane * 8;
      if (isq) {
        const int hp = (l32 >> 2) & 1, r = (l32 & 3) + 4 * (l32 >> 3);
        pc[3] = (h == hp) ? L_KVT + (2 * pair + h) * 1680 + r * 80 : L_ZERO;
        pc[4] = (h == hp && r <= 4) ? L_KVT + (2 * pair + h) * 1680 + (16 + r) * 80 : L_ZERO;
        pc[5] = og_main(rt * 128 + 32 * tg + l32, 2 * pair + h, 0);
        pc[6] = og_tail(rt * 128 + 32 * tg + l32, 2 * pair + h);
        pc[7] = 0;
      } else {
        const int hd = 2 * pair + (tg >> 1), dt = tg & 1;
        pc[3] = L_AV + hd * 6144 + (8 * g + q4) * 48 + 32 * dt + 8 * p4;
        pc[4] = L_PK + (2 * pair + h) * 10240 + (32 * tg + l32) * 80;
        pc[5] = L_PK + hd * 10240 + (8 * g + q4) * 80 + 8 * p4;
        pc[6] = (!dt || i16 <= 4) ? W_PART + ((hg * 4 + hd) * 16 + rt) * PARTB + (dt * 160 + (dt ? g * 5 + i16 : lane)) * 16 : OOB;
        pc[7] = g < 2 ? pc[6] : OOB;
      }
      pc[0] += lds0; pc[1] += lds0; pc[2] += lds0; pc[3] += lds0; pc[4] += lds0; if (!isq) pc[5] += lds0;
      pc[8] = pc[0] + 64 - 8 * h;
    }
#pragma unroll
    for (int i = 0; i < 9; i++) PIN(pc[i]);
  };
  if (wid < 4) {
    __builtin_amdgcn_s_setprio(3);
    u32 ep = 0;
    if (wid == 3) ep = gload(ws, W_EPOCH);
    if (wid == 2) *(LAS u32*)(misc + lane * 4) = 0;
    int kt = rot, bi = 0;
    if (wid == 1) {
      const rsrc_t rc = __builtin_amdgcn_make_buffer_rsrc((void*)(pc0 & ~15ull), (short)0, len & ~15u, 0x00020000);
      dma16(rc, idx * 1024 + lane * 16, 0, lds0 + L_DUMMY);
    }
    __builtin_amdgcn_s_barrier();
#pragma nounroll
    for (int s = 1; s < 10; s++) {
      bi = bi == 2 ? 0 : bi + 1; kt = (int)(ord >> (4 * s)) & 15;
      const u32 buf = lds0 + bi * STB;
      if (s == 2 && br == 0) {
        const rsrc_t rp = __builtin_amdgcn_make_buffer_rsrc((void*)(wphi + hg * 1600), (short)0, 3200, 0x00020000);
        dma16(rp, wid * 1024 + lane * 16, 0, lds0 + L_RAW + wid * 1024);
      }
#pragma unroll
      for (int i = 0; i < 4; i++) ldx(wid * 4 + i, kt, buf);
      if (lane < 60) {
#pragma unroll
        for (int j = 0; j < 8; j++) ldw1(wid * 8 + j, kt, buf);
      }
      waitv<12>();
      lbar();
      if (s == 9) pre();
      if (s == 9 && br == 0) {
        const int pair = wid >> 1, T = wid & 1, rb = g & 1;
        const int fb = 2 * rb + (p4 >> 1);
        const bool okT = (T == 0) || (fb == 0);
        const int f0 = T ? 16 : 4 * fb;
        s16x4 r[5];
#pragma unroll
        for (int u = 0; u < 5; u++) {
          const int cg = (u < 4) ? 4 * (u >> 1) + 2 * h + (u & 1) : 8 + h;
          const int hc = cg >= 5, d0 = 4 * (cg - 5 * hc);
          const bool ok = okT && (hc == (p4 & 1));
          r[u] = trrd(lds + (ok ? L_RAW + (2 * pair + hc) * 800 + (d0 + q4) * 40 + f0 * 2 : L_ZERO));
        }
        lds_ptr tb = lds + L_WTAB + wid * 2560;
        *(LAS bf16x8*)(tb + lane * 16) = cat8(r[0], r[1]);
        *(LAS bf16x8*)(tb + 1024 + lane * 16) = cat8(r[2], r[3]);
        *(LAS s16x4*)(tb + 2048 + lane * 8) = r[4];
      }
      STAMPV(32 + s - 1);
    }
    {
      const rsrc_t rwo = __builtin_amdgcn_make_buffer_rsrc((void*)(br ? woB : woA), (short)0, D_ * D_ * 2, 0x00020000);
      waitv<0>();
      if (wid == 3 && lane == 0) *(LAS u32*)(misc + M_TAG) = ep * 0x9E3779B1u + 0x7F4A7C15u;
      lbar();
      STAMP(15);
#pragma nounroll
      for (int j = 0; j < 5; j++) {
        const int i = wid * 5 + j;
        const u32 o_ = i < 16 ? og + (rt * 8 + (i >> 1)) * 20480 + hg * 2048 + (i & 1) * 1024 + lane * 16
                              : og + (rt * 8 + 2 * (i - 16) + h) * 20480 + 16384 + hg * 512 + l32 * 16;
        __builtin_amdgcn_raw_buffer_store_b128(u32x4{~0u, ~0u, ~0u, ~0u}, rws, o_, 0, 16);
      }
#pragma unroll
      for (int i = 0; i < 7; i++) dma16(rwo, lane * 16, (idx * 25 + (wid + 4 * i < 25 ? wid + 4 * i : 24)) * 1024, lds0 + L_DUMMY);
    }
    if (br) {
      waitv<7>();
      if (lane == 0 && lds_add_ret(misc + M_C4) == 3) gstore(ws, W_STARTED + ((br * 16 + rt) * 8 + hg) * 4, lds_word(misc + M_TAG));
    }
  } else {
    const int cw = wid - 4, mi = cw & 3, nj = cw < 8 ? (cw >> 2) + 1 : 0;
    if (cw < 4) {
#pragma unroll
      for (int i = 0; i < 4; i++) ldx(cw * 4 + i, rot, lds0);
    } else if (lane < 60) {
#pragma unroll
      for (int j = 0; j < 4; j++) ldw1((cw - 4) * 4 + j, rot, lds0);
    }
    __builtin_amdgcn_s_barrier();
    if (cw < 8) {
      const int c = cw & 3, x = lane + c - 35;
      *(LAS float*)(lds + L_MASK + cw * 256 + lane * 4) = ((x > 0) != (cw >= 4)) ? -1e30f : 0.f;
    } else if (cw == 8 && lane < 4) *(LAS u32x2*)(lds + L_ONES + lane * 640) = u32x2{0x3F803F80u, 0x3F803F80u};
    pre();
    const int row = mi * 32 + i16;
    const u32 xa0 = row * 128 + ((g ^ ((row >> 1) & 7)) << 4), xa1 = xa0 ^ 64;
    const u32 wa = XB + (16 * (g >> 1) + 4 * (g & 1) + q4) * 480 + 8 * p4 + nj * 160;
    int base, HS, RS;
    if (br) {
      if (nj == 0) { base = L_Q; HS = 6144; RS = 48; }
      else { base = L_OWN + (nj - 1) * 40 + mi * 7680; HS = 2560; RS = 80; }
    } else {
      if (nj == 2) { base = L_AV; HS = 6144; RS = 48; }
      else { base = nj ? L_AK : L_AQ; HS = 40; RS = 176; }
    }
    int ea[5];
#pragma unroll
    for (int n = 0; n < 5; n++) { const int c4 = 4 * n + g, hd = (c4 * 13) >> 6, d4 = c4 - 5 * hd; ea[n] = lds0 + base + hd * HS + row * RS + d4 * 8; PIN(ea[n]); }
    f32x4 acc[2][5] = {};
    auto rd = [&](Frags& f, lds_ptr bb, int ks, int j) {
      if (j < 2) f.x[j] = ld128(bb + (ks ? xa1 : xa0) + j * 2048);
      else { int n = (j - 2) >> 1; if (j & 1) f.wh[n] = trrd(bb + wa + ks * 15360 + 3840 + n * 32); else f.wl[n] = trrd(bb + wa + ks * 15360 + n * 32); }
    };
#define MM(F, i) acc[(i) & 1][(i) >> 1] = MFMA16(cat8(F.wl[(i) >> 1], F.wh[(i) >> 1]), F.x[(i) & 1], acc[(i) & 1][(i) >> 1])
    Frags F0, F1;
    int boff = 0;
    __builtin_amdgcn_s_setprio(3);
    waitv<0>();
    lbar();
    STAMP(14);
#pragma unroll
    for (int j = 0; j < 12; j++) rd(F0, lds, 0, j);
#pragma unroll
    for (int t = 0; t < 10; t++) {
      SBAR();
#pragma unroll
      for (int i = 0; i < 10; i++) { MM(F0, i); if (i < 6) { rd(F1, lds + boff, 1, 2 * i); rd(F1, lds + boff, 1, 2 * i + 1); } SBAR(); }
#pragma unroll
      for (int i = 0; i < 10 - HB; i++) MM(F1, i);
      SBAR();
      boff = ((t + 1) % 3) * STB; asm volatile("" : "+s"(boff));
      if (t < 9) {
        __builtin_amdgcn_s_waitcnt(0xC07F);
        asm volatile("s_barrier" : "+v"(acc[0][0]), "+v"(acc[1][0]), "+v"(acc[0][1]), "+v"(acc[1][1]), "+v"(acc[0][2]), "+v"(acc[1][2]), "+v"(acc[0][3]), "+v"(acc[1][3]), "+v"(acc[0][4]), "+v"(acc[1][4]) :: "memory");
      } else {
        lds_inc(misc + M_C8, lane);
        asm volatile("" : "+v"(acc[0][0]), "+v"(acc[1][0]), "+v"(acc[0][1]), "+v"(acc[1][1]), "+v"(acc[0][2]), "+v"(acc[1][2]), "+v"(acc[0][3]), "+v"(acc[1][3]), "+v"(acc[0][4]), "+v"(acc[1][4]));
      }
      asm volatile("" ::: "memory");
#pragma unroll
      for (int i = 10 - HB; i < 10; i++) {
#pragma unroll
        for (int j = (i - 10 + HB) * RPM; j < (i - 9 + HB) * RPM && j < 12; j++) if (t < 9) rd(F0, lds + boff, 0, j);
        MM(F1, i); SBAR();
      }
    }
    STAMP(1);
    relane();
    u32x2 pk[2][5];
    if (br && nj == 0) {
#pragma unroll
      for (int i = 0; i < 10; i++) { const f32x4 v = acc[i & 1][i >> 1] * (0.22360679774997896f * 1.4426950408889634f); pk[i & 1][i >> 1] = u32x2{cvtpk(v[0], v[1]), cvtpk(v[2], v[3])}; }
    } else {
#pragma unroll
      for (int i = 0; i < 10; i++) { const f32x4 v = acc[i & 1][i >> 1]; pk[i & 1][i >> 1] = u32x2{cvtpk(v[0], v[1]), cvtpk(v[2], v[3])}; asm volatile("" ::: "memory"); }
    }
    int eb[5];
#pragma unroll
    for (int n = 0; n < 5; n++) { eb[n] = ea[n] + 16 * RS; PIN(eb[n]); }
#pragma unroll
    for (int n = 0; n < 5; n++) { *(LAS u32x2*)LP(ea[n]) = pk[0][n]; *(LAS u32x2*)LP(eb[n]) = pk[1][n]; }
    if (br == 0 && nj == 2) {
#pragma unroll
      for (int m = 0; m < 2; m++) *(LAS u32x2*)(lds + L_AV + g * 6144 + (mi * 32 + i16 + 16 * m) * 48 + 40) = u32x2{0x3F803F80u, 0x3F803F80u};
    }
    lds_inc(misc + M_WR + nj * 16 + mi * 4, lane);
    if (nj == 2) lds_inc(misc + M_WV, lane);
    lds_inc(misc + M_C9, lane);
    if (br && nj == 1) {
      lds_wait_ge(misc + M_WR + 32 + mi * 4, 1u);
      if (mi < 2) __builtin_amdgcn_s_sleep(6);
      const u32 dst = W_KVG + ((rt * 8 + hg) * 4 + mi) * 10240 + lane * 16;
      u32x4 v[10];
#pragma unroll
      for (int i = 0; i < 10; i++) v[i] = *(LAS u32x4*)(lds + L_OWN + mi * 10240 + i * 1024 + lane * 16);
      SBAR();
#pragma unroll
      for (int i = 0; i < 10; i++) __builtin_amdgcn_raw_buffer_store_b128(v[i], rws, dst, i * 1024, 16);
    } else if (br) __builtin_amdgcn_s_setprio(0);
  }
  relane();
  f32x16 o = {};
  PIN(o);
  if (br) lds_wait_ge(misc + M_C9, 12u);
  STAMP(2);
  const u32 tag = RFL(lds_word(misc + M_TAG));
  const int l_wo = br ? LB_WO : LA_WO;
  auto started = [&]() { if (lane == 0) gstore(ws, W_STARTED + ((br * 16 + rt) * 8 + hg) * 4, tag); };
  auto guard = [&]() { poll_tag(ws, W_STARTED + ((br * 16 + rt) * 8 + (lane & 7)) * 4, tag, false); lds_inc(misc + M_C6, lane); };
  const rsrc_t rwo = __builtin_amdgcn_make_buffer_rsrc((void*)(br ? woB : woA), (short)0, D_ * D_ * 2, 0x00020000);
  u32 wvm[2] = {}, wvt[2] = {};
  const int wj = wid & 3;
  auto wo_init = [&]() {
#pragma unroll
    for (int u = 0; u < 2; u++) {
      int c = (u ? 4 : wj) * 64 + lane; PIN(c);
      const int p = c / 10, ch = c - p * 10;
      const int gg = ((p >> 3) & 2) | ((p >> 2) & 1), tt = (p >> 3) & 1, e = p & 3;
      wvm[u] = ((gg >> 1) * 20 + 4 * (gg & 1) + 8 * tt + e) * 1280 + hg * 160 + ch * 16; wvt[u] = (tt * 80 + gg * 20 + 16 + e) * 1280 + hg * 160 + ch * 16;
    }
  };
  auto wo_dma = [&](int k0, int k1) {
    if (k1 == 20) {
#pragma unroll
      for (int ks = 16; ks < 20; ks++) dma16(rwo, wvt[0], (ks - 16) * 160 * 1280, lds0 + l_wo + ks * 5120 + wj * 1024);
      dma16(rwo, wvt[1], wj * 160 * 1280, lds0 + l_wo + (16 + wj) * 5120 + 4096);
      k1 = 16;
    }
#pragma nounroll
    for (int ks = k0; ks < k1; ks++) {
      dma16(rwo, wvm[0], ks * 40 * 1280, lds0 + l_wo + ks * 5120 + wj * 1024);
      if ((ks & 3) == wj) dma16(rwo, wvm[1], ks * 40 * 1280, lds0 + l_wo + ks * 5120 + 4096);
    }
  };
  const bool wow = wid >= 12;
  const u32 ofl = W_OFLAG + (br * 16 + rt) * 128;
  auto published = [&](int rg) {
    waitv<0>();
    lds_inc(misc + M_D + rg * 4, lane);
    if (lane == 0 && lds_add_ret(misc + M_C2) == (br ? 15 : 7)) gstore(ws, ofl + hg * 4, tag);
  };

  if (br) {
    const int hd = wid & 3, qi = wid < 8 ? 1 - (wid >> 2) : wid >> 2;
    const u32 kvf = W_KVFLAG + (rt * 8 + hg) * 128;
    const bf16x8 qa = ld128(LP(pc[0]));
    s16x4 qb = ld64(LP(pc[0]) + 32);
    if (h) qb = s16x4{0, 0, 0, 0};
    auto ldmask = [&](int pol) -> f32x16 {
      f32x4 t[4];
#pragma unroll
      for (int j = 0; j < 4; j++) t[j] = *(LAS f32x4*)(LP(pc[6]) + pol * 1024 + j * 32);
      return __builtin_shufflevector(__builtin_shufflevector(t[0], t[1], 0, 1, 2, 3, 4, 5, 6, 7), __builtin_shufflevector(t[2], t[3], 0, 1, 2, 3, 4, 5, 6, 7),
                                     0, 1, 2, 3, 4, 5, 6, 7, 8, 9, 10, 11, 12, 13, 14, 15);
    };
    auto pk4 = [&](const f32x16& s, int o_) -> bf16x8 {
      u32x4 p;
#pragma unroll
      for (int j = 0; j < 4; j++) p[j] = cvtpk(ex2(s[o_ + 2 * j]), ex2(s[o_ + 2 * j + 1]));
      return __builtin_bit_cast(bf16x8, p);
    };
    lds_ptr ka_p, kb_p, vp;
    auto seek = [&](int row) { ka_p = LP(pc[1]) + row * 10240; kb_p = LP(pc[2]) + row * pc[4]; vp = LP(pc[3]) + row * pc[5]; };
    f32x16 s, s2; s16x4 c;
    auto step = [&](f32x16& sc, f32x16& sn) {
      ka_p -= 10240; kb_p -= pc[4];
      const bf16x8 ka = ld128(ka_p); const s16x4 kb = ld64(kb_p);
      const bf16x8 v0 = cat8(trrd(vp), trrd(vp + 640)), v1 = cat8(trrd(vp + 1280), trrd(vp + 1920));
      vp -= pc[5];
      SBAR();
      float e[8]; u32x4 p1;
      bf16x8 p0 = pk4(sc, 0); PIN(p0);
      SBAR(); sn = MFMA32(ka, qa, f32x16{}); SBAR();
#pragma unroll
      for (int j = 0; j < 4; j++) e[j] = ex2(sc[8 + j]);
      SBAR(); sn = MFMA32K8(kb, c, sn); SBAR();
#pragma unroll
      for (int j = 4; j < 8; j++) e[j] = ex2(sc[8 + j]);
      SBAR(); o = MFMA32(v0, p0, o); SBAR();
#pragma unroll
      for (int j = 0; j < 4; j++) p1[j] = cvtpk(e[2 * j], e[2 * j + 1]);
      SBAR(); o = MFMA32(v1, __builtin_bit_cast(bf16x8, p1), o); SBAR();
    };
    auto last = [&](f32x16& sc) {
      const bf16x8 v0 = cat8(trrd(vp), trrd(vp + 640)), v1 = cat8(trrd(vp + 1280), trrd(vp + 1920));
      o = MFMA32(v0, pk4(sc, 0), o);
      o = MFMA32(v1, pk4(sc, 8), o);
    };
    seek(L_OWN / 10240 + qi);
    {
      const s16x4 kb = ld64(kb_p);
      s = MFMA32K8(kb, qb, MFMA32(ld128(ka_p), qa, ldmask(0)));
      float mm = fmaxf(s[0], s[1]);
#pragma unroll
      for (int r = 2; r < 16; r += 2) mm = fmaxf(fmaxf(mm, s[r]), s[r + 1]);
      auto rr = __builtin_amdgcn_permlane32_swap(__float_as_uint(mm), __float_as_uint(mm), false, false);
      const u32 nm = cvtpk(-fmaxf(__uint_as_float(rr[0]), __uint_as_float(rr[1])), 0.f);
      const u32x2 qw = __builtin_bit_cast(u32x2, qb);
      c = __builtin_bit_cast(s16x4, u32x2{h ? nm : qw.x, qw.y});
      s = MFMA32K8(kb, __builtin_bit_cast(s16x4, u32x2{h ? nm : 0u, 0u}), s);
    }
    if (qi >= 2) { step(s, s2); step(s2, s); }
    if (qi & 1) { step(s, s2); last(s2); } else last(s);
    lds_inc(misc + M_C1, lane);
    STAMP(3);
    if (qi == 0) {
      waitv<0>();
      if (lane == 0) gstore(ws, kvf + hd * 4, tag);
      lds_inc(misc + (hd >> 1 ? M_P1 : M_P0), lane);
      STAMP(4);
    }
    if (wid < 4) {
      const int dd = 2 - (wid >> 1);
#pragma nounroll
      for (int hf = 1; hf >= 0; --hf) {
        const int tl = 2 * hf + (wid & 1);
        lds_wait_ge(misc + (hf ? M_P1 : M_P0), 2u);
        if (rt >= dd) poll_tag(ws, kvf - dd * 1024 + tl * 4, tag, false);
        if (hf) { STAMP(11); } else { STAMP(5); lds_inc(misc + M_C0, lane); }
        if (rt >= dd) {
          const u32 src = W_KVG + (((rt - dd) * 8 + hg) * 4 + tl) * 10240;
#pragma unroll
          for (int i = 0; i < 10; i++) dma16c(rws, lane * 16, src + i * 1024, lds0 + L_HALO + hf * 40960 + wid * 10240 + i * 1024);
        }
      }
    }
    if (wow) wo_init();
    lds_wait_ge(misc + M_C0, 4u);
    int wk = 20;
    if (rt > 0) {
      int n1 = 2, nn = 4, km = 0;
      if (rt >= 2) { n1 = qi < 3 ? 4 : 3; nn = 8 - qi; km = qi >= 2 ? n1 - 1 : nn - 1; }
      const u32 ev1 = (1u << n1) | (wid < 4 ? 4u : 0u) | (wow ? ~0u : 0u);
      const u32 ev2 = (1u << n1) | (1u << km);
      if (wow) __builtin_amdgcn_s_setprio(3); else __builtin_amdgcn_s_setprio(2);
      seek(7);
      s = MFMA32K8(ld64(kb_p), c, MFMA32(ld128(ka_p), qa, f32x16{}));
      auto before = [&](int k) {
        if (k == 2 && wid < 4) { waitv<0>(); lds_inc(misc + M_C7, lane); }
        if (k == n1) {
          ka_p -= (4 - n1) * 10240; kb_p -= (4 - n1) * pc[4];
          lds_wait_ge(misc + M_C7, 4u);
          if (wid == 10) guard();
        }
        if (wow && wk > 0) {
          const u32 c1 = RFL(lds_word(misc + M_C1)), c7 = RFL(lds_word(misc + M_C7)), c3 = RFL(lds_word(misc + M_C3));
          const int lim = c3 >= 16 ? 0 : (c1 >= 16 && c7 >= 4) ? 5 : 20;
          const int k0 = wk - 8 > lim ? wk - 8 : lim;
          if (k0 < wk) { wo_dma(k0, wk); wk = k0; }
        }
      };
      auto after = [&](int k, f32x16& sn) {
        if (k == km) sn += ldmask(1);
        if (k == n1) { vp -= (4 - n1) * pc[5]; lds_inc(misc + M_C3, lane); if (!wow) __builtin_amdgcn_s_setprio(1); }
      };
#pragma nounroll
      for (int k = 1;; k += 2) {
        if (__builtin_expect((ev1 >> k) & 1, 0)) before(k);
        step(s, s2);
        if (__builtin_expect((ev2 >> k) & 1, 0)) after(k, s2);
        if (__builtin_expect(k + 1 == nn, 0)) { s = s2; break; }
        if (__builtin_expect((ev1 >> (k + 1)) & 1, 0)) before(k + 1);
        step(s2, s);
        if (__builtin_expect((ev2 >> (k + 1)) & 1, 0)) after(k + 1, s);
        if (__builtin_expect(k + 2 == nn, 0)) break;
      }
      last(s);
    } else {
      if (wid == 10) guard();
    }
    float l = h ? o[8] : o[12];
    if (__builtin_expect(__any(!(l < 1.8e19f)), 0)) {
      const int dd = lane % 3;
      poll_tag(ws, kvf - (rt - dd < 0 ? 0 : dd) * 1024 + 4 * (lane & 3), tag, false);
      float q[20], a[20], mx = -3e38f; l = 0.f;
      const u32x4 qw = __builtin_bit_cast(u32x4, qa); const u32x2 qv = __builtin_bit_cast(u32x2, qb);
#pragma unroll
      for (int j = 0; j < 10; j++) {
        const u32 w = j < 8 ? __shfl(qw[j & 3], l32 + 32 * (j >> 2)) : __shfl(qv[j & 1], l32);
        q[2 * j] = blo(w); q[2 * j + 1] = bhi(w); a[2 * j] = 0.f; a[2 * j + 1] = 0.f;
      }
      const int row = rt * 128 + 32 * qi + l32;
#pragma nounroll
      for (int t = 0; t < 128; ++t) {
        const int j = row - 2 * t - h;
        if (j >= 0) {
          const u32 src = W_KVG + (((j >> 7) * 8 + hg) * 4 + ((j >> 5) & 3)) * 10240 + hd * 2560 + (j & 31) * 80;
          u32 w[20];
#pragma unroll
          for (int i = 0; i < 5; i++) { const u32x4 v = __builtin_amdgcn_raw_buffer_load_b128(rws, src, i * 16, 16); w[4 * i] = v.x; w[4 * i + 1] = v.y; w[4 * i + 2] = v.z; w[4 * i + 3] = v.w; }
          float sc = 0.f;
#pragma unroll
          for (int i = 0; i < 10; i++) sc += q[2 * i] * blo(w[i]) + q[2 * i + 1] * bhi(w[i]);
          const float mn = fmaxf(mx, sc), f = ex2(mx - mn), p = ex2(sc - mn);
          mx = mn; l = l * f + p;
#pragma unroll
          for (int i = 0; i < 10; i++) { a[2 * i] = a[2 * i] * f + p * blo(w[10 + i]); a[2 * i + 1] = a[2 * i + 1] * f + p * bhi(w[10 + i]); }
        }
      }
      const float mo = __shfl_xor(mx, 32), mn = fmaxf(mx, mo), f = ex2(mx - mn);
      l *= f; l += __shfl_xor(l, 32);
#pragma unroll
      for (int i = 0; i < 20; i++) { a[i] *= f; a[i] += __shfl_xor(a[i], 32); }
#pragma unroll
      for (int r = 0; r < 8; r++) o[r] = h ? a[(r & 3) + 8 * (r >> 2) + 4] : a[(r & 3) + 8 * (r >> 2)];
#pragma unroll
      for (int r = 8; r < 12; r++) o[r] = a[8 + r];
    }
    const float rl = __builtin_amdgcn_rcpf(l);
    const u32x4 w0 = {cvtpk(o[0] * rl, o[1] * rl), cvtpk(o[2] * rl, o[3] * rl), cvtpk(o[4] * rl, o[5] * rl), cvtpk(o[6] * rl, o[7] * rl)};
    const u32x2 w1 = {cvtpk(o[8] * rl, o[9] * rl), cvtpk(o[10] * rl, o[11] * rl)};
    __builtin_amdgcn_raw_buffer_store_b128(w0, rws, pc[7], 0, 16);
    __builtin_amdgcn_raw_buffer_store_b64(w1, rws, pc[8], 0, 16);
    STAMP(6);
    lds_inc(misc + M_AD, lane);
    if (wow && wk > 0) { lds_wait_ge(misc + (rt ? M_C3 : M_C1), 16u); wo_dma(0, wk); }
    published(qi);
    if (wow) lds_inc(misc + M_C6, lane);
    __builtin_amdgcn_s_setprio(0);
  } else {
    const int tg = wid & 3, pair = (wid >> 2) & 1, isq = wid >> 3;
    u32 phi[20];
    {
      lds_wait_ge(misc + M_WR + (isq ? 0 : 16) + tg * 4, 1u);
      const bf16x8 b0 = ld128(LP(pc[0])), a00 = ld128(LP(pc[1])), a10 = ld128(LP(pc[1]) + 2560);
      const bf16x8 b1 = ld128(LP(pc[0]) + 32), a01 = ld128(LP(pc[1]) + 1024), a11 = ld128(LP(pc[1]) + 2560 + 1024);
      const s16x4 b2 = ld64(LP(pc[8])), a02 = ld64(LP(pc[2])), a12 = ld64(LP(pc[2]) + 2560);
      lds_inc(misc + M_C5, lane);
      if (wow) { wo_init(); lds_wait_ge(misc + M_C5, 16u); wo_dma(6, 20); waitv<0>(); lds_inc(misc + M_C6, lane); }
      if (isq) __builtin_amdgcn_s_setprio(0); else __builtin_amdgcn_s_setprio(2);
      f32x16 t0 = MFMA32(a00, b0, f32x16{}), t1 = MFMA32(a10, b0, f32x16{});
      t0 = MFMA32(a01, b1, t0); t1 = MFMA32(a11, b1, t1);
      t0 = MFMA32K8(a02, b2, t0); t1 = MFMA32K8(a12, b2, t1);
      float p[20];
#pragma unroll
      for (int j = 0; j < 10; j++) {
        const u32 w = j < 8 ? cvtpk(t0[2 * j], t0[2 * j + 1]) : cvtpk(t1[2 * j - 16], t1[2 * j - 15]);
        p[2 * j] = blo(w); p[2 * j + 1] = bhi(w);
      }
      float mx = fmaxf(p[0], p[1]), mn = fminf(p[0], p[1]);
#pragma unroll
      for (int j = 2; j < 20; j += 2) { mx = fmaxf(fmaxf(mx, p[j]), p[j + 1]); mn = fminf(fminf(mn, p[j]), p[j + 1]); }
      const float L2E = 1.4426950408889634f;
      const f32x2 sl = {L2E, -L2E}, of = {-mx * L2E, mn * L2E};
      f32x2 e[20], sa = {0.f, 0.f}, sb = {0.f, 0.f};
#pragma unroll
      for (int j = 0; j < 20; j++) {
        const f32x2 a = __builtin_elementwise_fma(f32x2{p[j], p[j]}, sl, of);
        e[j] = f32x2{ex2(a[0]), ex2(a[1])};
        if (j & 1) sb += e[j]; else sa += e[j];
      }
      sa += sb;
      const f32x2 rr = {__builtin_amdgcn_rcpf(sa[0]), __builtin_amdgcn_rcpf(sa[1])};
#pragma unroll
      for (int j = 0; j < 20; j++) phi[j] = cvtpk2(e[j] * rr);
    }
    if (!isq) {
      lds_wait_ge(misc + M_WV, 4u);
      bf16x8 vb[4];
#pragma unroll
      for (int ks = 0; ks < 4; ks++) vb[ks] = cat8(trrd(LP(pc[3]) + ks * 32 * 48), trrd(LP(pc[3]) + ks * 32 * 48 + 4 * 48));
      lds_wait_ge(misc + M_C8, 12u);
#pragma unroll
      for (int j = 0; j < 5; j++) *(LAS u32x4*)(LP(pc[4]) + 16 * j) = u32x4{phi[4 * j], phi[4 * j + 1], phi[4 * j + 2], phi[4 * j + 3]};
      lds_inc(misc + M_C0 + pair * 4, lane);
      STAMP(3);
      lds_wait_ge(misc + M_C0 + pair * 4, 4u);
      f32x4 kv[3] = {};
#pragma unroll
      for (int ks = 0; ks < 4; ks++)
#pragma unroll
        for (int T = 0; T < 3; T++) kv[T] = MFMA16(cat8(trrd(LP(pc[5]) + ks * 2560 + T * 32), trrd(LP(pc[5]) + ks * 2560 + 320 + T * 32)), vb[ks], kv[T]);
#pragma unroll
      for (int T = 0; T < 3; T++)
        __builtin_amdgcn_raw_buffer_store_b128(u32x4{cvtpk(kv[T][0], kv[T][1]), tag, cvtpk(kv[T][2], kv[T][3]), tag}, rws, pc[T < 2 ? 6 : 7], T * ((tg & 1) ? 320 : 1024), 16);
      waitv<3>();
      lds_inc(misc + M_C1, lane);
      STAMP(4);
      if (wid >= 4) { wo_init(); lds_wait_ge(misc + M_C1, 8u); wo_dma(0, 6); waitv<0>(); lds_inc(misc + M_C6, lane); }
      if (wid == 0) {
        const int sl = (rt & 3) * 53 + lane; const bool act = lane < 53 && sl < 210;
        const u32 src = act ? W_PART + (hg * 4 + (rt >> 2)) * 16 * PARTB + sl * 16 : OOB;
        float s0 = 0.f, s1 = 0.f, s2 = 0.f, s3 = 0.f;
        lds_wait_ge(misc + M_C6, 8u);
#pragma nounroll
        for (u32 s = 0; s < SPIN_CAP; ++s) {
          bool ok = true; s0 = 0.f; s1 = 0.f; s2 = 0.f; s3 = 0.f;
          u32x4 v[16];
#pragma unroll
          for (int r = 0; r < 16; r++) v[r] = __builtin_amdgcn_raw_buffer_load_b128(rws, src, r * PARTB, 16);
#pragma unroll
          for (int r = 0; r < 16; r++) { ok &= (v[r].y == tag) & (v[r].w == tag); s0 += blo(v[r].x); s1 += bhi(v[r].x); s2 += blo(v[r].z); s3 += bhi(v[r].z); }
          if (__all(ok || !act)) break;
          asm volatile("" ::: "memory");
        }
        __builtin_amdgcn_raw_buffer_store_b128(u32x4{cvtpk(s0, s1), tag, cvtpk(s2, s3), tag}, rws, act ? W_SLICE + (hg * 16 + rt) * 1024 + lane * 16 : OOB, 0, 16);
        waitv<0>();
        lds_inc(misc + M_C4, lane);
        STAMP(5);
      } else if (wid == 1) { lds_wait_ge(misc + M_C1, 8u); started(); lds_wait_ge(misc + M_C4, 1u); guard(); }
      else if (wid >= 4) {
        const int hh = wid - 4;
        const u32 src = W_SLICE + (hg * 16 + hh * 4) * 1024 + lane * 16;
        int da[4];
#pragma unroll
        for (int j = 0; j < 4; j++) {
          const int sl = j * 53 + lane; int d, ph;
          if (sl < 160) { d = sl & 15; ph = (sl >> 4) * 4; }
          else { const int s2 = sl - 160, gg = s2 / 5; d = 16 + s2 - 5 * gg; ph = 4 * gg; }
          da[j] = L_KVT + hh * 1680 + d * 80 + ph * 2;
        }
        lds_wait_ge(misc + M_C5, 16u);
        lds_wait_ge(misc + M_C4, 1u);
        u32x4 v[4];
#pragma nounroll
        for (u32 s = 0; s < SPIN_CAP; ++s) {
          bool ok = true;
#pragma unroll
          for (int r = 0; r < 4; r++) v[r] = __builtin_amdgcn_raw_buffer_load_b128(rws, src, r * 1024, 16);
#pragma unroll
          for (int r = 0; r < 4; r++) ok &= ((v[r].y == tag) & (v[r].w == tag)) | (lane >= (r < 3 ? 53 : 51));
          if (__all(ok)) break;
          asm volatile("" ::: "memory");
        }
#pragma unroll
        for (int r = 0; r < 4; r++) if (lane < (r < 3 ? 53 : 51)) *(LAS u32x2*)(lds + da[r]) = u32x2{v[r].x, v[r].z};
        lds_inc(misc + M_C3, lane);
        STAMP(11);
      }
      __builtin_amdgcn_s_setprio(0);
    } else {
      lds_ptr a0 = LP(pc[3]), a1 = LP(pc[4]); const u32 om = pc[5], ot = pc[6];
      STAMP(12);
      lds_wait_ge(misc + M_C3, 4u);
      f32x16 t0 = {}, t1 = {};
#pragma unroll
      for (int j = 0; j < 5; j++) {
        const bf16x8 bq = __builtin_bit_cast(bf16x8, u32x4{phi[4 * j], phi[4 * j + 1], phi[4 * j + 2], phi[4 * j + 3]});
        t1 = MFMA32(ld128(a1 + 16 * j), bq, t1); t0 = MFMA32(ld128(a0 + 16 * j), bq, t0);
      }
      const float rl = __builtin_amdgcn_rcpf(t1[4] + 1e-6f);
      const u32x4 w0 = {cvtpk(t0[0] * rl, t0[1] * rl), cvtpk(t0[2] * rl, t0[3] * rl), cvtpk(t0[8] * rl, t0[9] * rl), cvtpk(t0[10] * rl, t0[11] * rl)};
      const u32x4 w1 = {cvtpk(t0[4] * rl, t0[5] * rl), cvtpk(t0[6] * rl, t0[7] * rl), cvtpk(t0[12] * rl, t0[13] * rl), cvtpk(t0[14] * rl, t0[15] * rl)};
      const u32x2 w2 = {cvtpk(t1[0] * rl, t1[1] * rl), cvtpk(t1[2] * rl, t1[3] * rl)};
      __builtin_amdgcn_raw_buffer_store_b128(w0, rws, om, 0, 16);
      __builtin_amdgcn_raw_buffer_store_b128(w1, rws, om, 256, 16);
      __builtin_amdgcn_raw_buffer_store_b64(w2, rws, ot, 0, 16);
      STAMP(6);
      published(tg);
    }
  }
  STAMP(7);
  relane();
  if (wid >= 8) {
    if (b == 0 && wid == 15) {
#pragma nounroll
      for (u32 s = 0; s < SPIN_CAP; ++s) {
        const u32x4 v = __builtin_amdgcn_raw_buffer_load_b128(rws, W_STARTED + lane * 16, 0, 16);
        if (__all(v.x == tag && v.y == tag && v.z == tag && v.w == tag)) break;
        __builtin_amdgcn_s_sleep(2);
      }
      if (lane == 0) gstore(ws, W_EPOCH, tag);
    }
  } else {
    const int rg = br ? (0x2013 >> (4 * (wid >> 1))) & 3 : wid >> 1, kh = wid & 1;
    const u32 obm = og + (rt * 8 + rg * 2) * 20480 + kh * 8192 + lane * 16, obt = og + (rt * 8 + rg * 2) * 20480 + kh * 2048 + lane * 8;
    const u32 mo[2] = {(u32)kh * 20480u, (u32)(1 - kh) * 20480u};
    u32x4 of[10][2];
    auto ldo = [&](int s) {
#pragma unroll
      for (int m = 0; m < 2; m++) {
        if (s < 8) of[s][m] = __builtin_amdgcn_raw_buffer_load_b128(rws, obm, mo[m] + s * 1024, 16);
        else {
          const u32x2 a = __builtin_amdgcn_raw_buffer_load_b64(rws, obt, mo[m] + (s + 8) * 1024, 16);
          const u32x2 c = __builtin_amdgcn_raw_buffer_load_b64(rws, obt, mo[m] + (s + 8) * 1024 + 512, 16);
          of[s][m] = u32x4{a.x, a.y, c.x, c.y};
        }
      }
    };
    u32 wm = lds0 + l_wo + (16 * (g >> 1) + 4 * (g & 1) + q4) * 160 + 8 * p4, wt = wm + (16 + 2 * kh) * 5120; wm += kh * 8 * 5120;
    PIN(wm); PIN(wt);
    bf16x8 wf[5];
    auto ldw = [&](int s, int n) { lds_ptr p = (s < 8 ? LP(wm) + s * 5120 : LP(wt) + (s - 8) * 5120) + n * 32; wf[n] = cat8(trrd(p), trrd(p + 1280)); };
    f32x4 ac2[2][5];
    constexpr int PF = 6;
    lds_wait_ge(misc + M_C6, br ? 5u : 9u);
    lds_wait_ge(misc + M_D + rg * 4, br ? 4u : 2u);
    STAMP(8);
#pragma nounroll
    for (int pass = 0;; ++pass) {
      if (pass) poll_tag(ws, ofl + (lane & 7) * 4, tag, false);
#pragma nounroll
      for (int tr = 0; tr < 64; ++tr) {
#pragma unroll
        for (int s = 9; s > 5; s--) ldo(s);
        asm volatile("" ::: "memory");
        u32 mx = 0;
#pragma unroll
        for (int s = 8; s < 10; s++)
#pragma unroll
          for (int m = 0; m < 2; m++) mx = max(max(mx, max(of[s][m].x, of[s][m].y)), max(of[s][m].z, of[s][m].w));
        if (pass || !__any(mx == ~0u)) break;
      }
      STAMP(9);
#pragma unroll
      for (int s = 5; s > 9 - PF; s--) ldo(s);
#pragma unroll
      for (int n = 0; n < 5; n++) { ldw(9, n); ac2[0][n] = f32x4{}; ac2[1][n] = f32x4{}; }
#pragma unroll
      for (int s = 9; s >= 0; s--) {
        SBAR();
#pragma unroll
        for (int n = 0; n < 5; n++) {
          ac2[0][n] = MFMA16(wf[n], __builtin_bit_cast(bf16x8, of[s][0]), ac2[0][n]);
          ac2[1][n] = MFMA16(wf[n], __builtin_bit_cast(bf16x8, of[s][1]), ac2[1][n]);
          if (s > 0) ldw(s - 1, n);
          SBAR();
        }
        if (s - PF >= 0) ldo(s - PF);
      }
      const float chk = ac2[0][0][0] + ac2[1][0][0];
      if (pass || !__any(chk != chk)) break;
    }
    STAMP(13);
    relane();
    if (br) lds_wait_ge(misc + M_AD, 16u);
    lds_ptr red = lds + L_RED + lane * 16;
#pragma unroll
    for (int n = 0; n < 5; n++) *(LAS f32x4*)(red + (wid ^ 1) * 5120 + n * 1024) = ac2[1][n];
    lds_inc(misc + M_X + (wid ^ 1) * 4, lane);
    const int r16 = rt * 8 + rg * 2 + kh;
    lds_wait_ge(misc + M_X + wid * 4, 1u);
    f32x4 acc[5];
#pragma unroll
    for (int n = 0; n < 5; n++) acc[n] = *(LAS f32x4*)(red + wid * 5120 + n * 1024);
    SBAR();
#pragma unroll
    for (int n = 0; n < 5; n++) acc[n] += ac2[0][n];
    const rsrc_t ro = __builtin_amdgcn_make_buffer_rsrc((void*)out, (short)0, S_ * 1280 * 2, 0x00020000);
    const u32 obase = (r16 * 16 + i16) * 2560 + br * 1280 + hg * 160;
#pragma unroll
    for (int n = 0; n < 4; n += 2) {
      const auto x = __builtin_amdgcn_permlane16_swap(cvtpk(acc[n][0], acc[n][1]), cvtpk(acc[n + 1][0], acc[n + 1][1]), false, false);
      const auto y = __builtin_amdgcn_permlane16_swap(cvtpk(acc[n][2], acc[n][3]), cvtpk(acc[n + 1][2], acc[n + 1][3]), false, false);
      __builtin_amdgcn_raw_buffer_store_b128(u32x4{x[0], y[0], x[1], y[1]}, ro, obase + (g & 1) * 32 + (g >> 1) * 16, n * 32, 0);
    }
    __builtin_amdgcn_raw_buffer_store_b64(u32x2{cvtpk(acc[4][0], acc[4][1]), cvtpk(acc[4][2], acc[4][3])}, ro, obase + g * 8, 128, 0);
  }
  asm volatile(".Lcode_end:");
  STAMP(10);
}

extern "C" void kernel_launch(void* const* d_in, const int* in_sizes, int n_in, void* d_out, int out_size, void* d_ws, size_t ws_size, hipStream_t stream) {
  (void)hipFuncSetAttribute((const void*)fused, hipFuncAttributeMaxDynamicSharedMemorySize, LDS_BYTES);
  fused<<<256, 1024, LDS_BYTES, stream>>>((const u16*)d_in[0], (const u16*)d_in[1], (const u16*)d_in[2], (const u16*)d_in[3],
                                          (const u16*)d_in[4], (const u16*)d_in[5], (const u16*)d_in[6], (u16*)d_out, (char*)d_ws);
}
```

```cpp
#include <hip/hip_runtime.h>
#include <hip/hip_bf16.h>
#include <type_traits>

typedef unsigned short u16;
typedef unsigned u32;
typedef short bf16x8 __attribute__((ext_vector_type(8)));
typedef short s16x4 __attribute__((ext_vector_type(4)));
typedef float f32x4 __attribute__((ext_vector_type(4)));
typedef float f32x16 __attribute__((ext_vector_type(16)));
typedef float f32x2 __attribute__((ext_vector_type(2)));
typedef __bf16 bf2_t __attribute__((ext_vector_type(2)));
typedef u32 u32x4 __attribute__((ext_vector_type(4)));
typedef u32 u32x2 __attribute__((ext_vector_type(2)));
#define LAS __attribute__((address_space(3)))
#define GAS __attribute__((address_space(1)))
typedef LAS char* lds_ptr;
typedef __amdgpu_buffer_rsrc_t rsrc_t;
#define DI __device__ __forceinline__
#define RFL(x) __builtin_amdgcn_readfirstlane(x)
#define SBAR() __builtin_amdgcn_sched_barrier(0)
#define LP(x) ((lds_ptr)(size_t)(u32)(x))
#define PIN(x) asm volatile("" : "+v"(x))
#define RLX_AGENT __ATOMIC_RELAXED, __HIP_MEMORY_SCOPE_AGENT
#define RLX_WG __ATOMIC_RELAXED, __HIP_MEMORY_SCOPE_WORKGROUP
#define XMAP 2
#define HB 10
#define RPM ((12 + HB - 2) / (HB - 1))
#define S1DELAY 8
#define P3V 1
#define P3S 12
#define P3L 7
#define P3PF 5
#define WFN 8
#define ROT 4

constexpr int S_ = 2048, D_ = 640, N3 = 1920;
constexpr int XB = 128 * 64 * 2, WB = 64 * 240 * 2, STB = XB + WB;
constexpr int L_RAW = 3 * STB;
constexpr int L_ZERO = L_RAW + 3200;
constexpr int L_WTAB = L_RAW + 4096;
constexpr int L_MASK = 158720;
constexpr int L_ONES = 160768;
constexpr int L_MISC = L_ONES + 64;
constexpr int L_DUMMY = 162816;
constexpr int LDS_BYTES = 163840;
constexpr int L_HALO = 0, L_OWN = 81920, L_Q = 122880, LB_WO = 56320, L_RED = 0;
constexpr int L_PK = 0, L_AV = 47104, L_AK = 71680, L_AQ = L_AK + 22528, L_KVT = L_WTAB, LA_WO = 40960;
constexpr int M_TAG = 0, M_C0 = 16, M_C1 = 32, M_C2 = 36, M_C3 = 40, M_C4 = 44, M_C5 = 48, M_C6 = 52, M_C7 = 56, M_C9 = 60, M_WR = 64, M_C8 = 112, M_WV = 116, M_P0 = 120, M_P1 = 124, M_D = 128, M_X = 144, M_AD = 176, M_C6B = 180;
constexpr u32 W_EPOCH = 0, W_STARTED = 1024, W_KVFLAG = 4096, W_OFLAG = 32768, W_SLICE = 65536;
constexpr u32 W_PART = 1u << 20, W_KVG = 8u << 20, W_OG = 16u << 20, W_TOTAL = 24u << 20;
constexpr u32 PARTB = 3456;
constexpr u32 OOB = 0x80000000u;
constexpr u32 SPIN_CAP = 1u << 18;

DI void dma16(rsrc_t r, u32 voff, u32 soff, u32 ldsaddr) {
  asm volatile("s_mov_b32 m0, %0\n\ts_nop 0\n\tbuffer_load_dwordx4 %1, %2, %3 offen lds" ::"s"(ldsaddr), "v"(voff), "s"(r), "s"(soff) : "m0", "memory");
}
DI void dma16c(rsrc_t r, u32 voff, u32 soff, u32 ldsaddr) {
  asm volatile("s_mov_b32 m0, %0\n\ts_nop 0\n\tbuffer_load_dwordx4 %1, %2, %3 offen sc1 lds" ::"s"(ldsaddr), "v"(voff), "s"(r), "s"(soff) : "m0", "memory");
}
DI s16x4 trrd(lds_ptr p) { return __builtin_bit_cast(s16x4, __builtin_amdgcn_ds_read_tr16_b64_v4i16((LAS s16x4*)p)); }
DI bf16x8 cat8(s16x4 a, s16x4 b) { return __builtin_shufflevector(a, b, 0, 1, 2, 3, 4, 5, 6, 7); }
DI u32 cvtpk2(f32x2 v) { return __builtin_bit_cast(u32, __builtin_convertvector(v, bf2_t)); }
DI u32 cvtpk(float lo, float hi) { return cvtpk2(f32x2{lo, hi}); }
DI float blo(u32 w) { return __uint_as_float(w << 16); }
DI float bhi(u32 w) { return __uint_as_float(w & 0xffff0000u); }
template <int N> DI void waitv() { asm volatile("s_waitcnt vmcnt(%0)" ::"i"(N) : "memory"); }
DI void lbar() { asm volatile("s_waitcnt lgkmcnt(0)\n\ts_barrier" ::: "memory"); }
DI void waitl() { asm volatile("s_waitcnt lgkmcnt(0)" ::: "memory"); }
DI bf16x8 ld128(lds_ptr p) { return *(LAS bf16x8*)p; }
DI s16x4 ld64(lds_ptr p) { return *(LAS s16x4*)p; }
#define MFMA16(a, b, c) __builtin_amdgcn_mfma_f32_16x16x32_bf16(a, b, c, 0, 0, 0)
#define MFMA32(a, b, c) __builtin_amdgcn_mfma_f32_32x32x16_bf16(a, b, c, 0, 0, 0)
#define MFMA32K8(a, b, c) __builtin_amdgcn_mfma_f32_32x32x8bf16_1k(a, b, c, 0, 0, 0)
DI float ex2(float x) { return __builtin_amdgcn_exp2f(x); }
DI u32 gload(char* ws, u32 off) { return __hip_atomic_load((GAS u32*)(ws + off), RLX_AGENT); }
DI void gstore(char* ws, u32 off, u32 v) { __hip_atomic_store((GAS u32*)(ws + off), v, RLX_AGENT); }
DI u32 lds_word(lds_ptr p) { return __hip_atomic_load((LAS u32*)p, RLX_WG); }
DI void lds_inc(lds_ptr p, int) {
  unsigned long long sv;
  asm volatile("s_mov_b64 %0, exec\n\ts_mov_b64 exec, 1\n\tds_add_u32 %1, %2\n\ts_mov_b64 exec, %0" : "=&s"(sv) : "v"((u32)(size_t)p), "v"(1u) : "memory");
}
DI u32 lds_add_ret(lds_ptr p) { return __hip_atomic_fetch_add((LAS u32*)p, 1u, RLX_WG); }
DI void lds_wait_ge(lds_ptr p, u32 want) {
  u32 v, sv, cnt = SPIN_CAP * 8;
  asm volatile("1:\n\tds_read_b32 %0, %3\n\ts_waitcnt lgkmcnt(0)\n\tv_readfirstlane_b32 %1, %0\n\ts_nop 0\n\ts_cmp_ge_u32 %1, %4\n\ts_cbranch_scc1 2f\n\t"
               "s_sub_u32 %2, %2, 1\n\ts_cbranch_scc1 2f\n\ts_sleep 1\n\ts_branch 1b\n2:"
               : "=&v"(v), "=&s"(sv), "+s"(cnt) : "v"((u32)(size_t)p), "s"(want) : "scc", "memory");
}
DI void poll_tag(char* ws, u32 off, u32 tag, bool done) {
#pragma nounroll
  for (u32 s = 0; s < SPIN_CAP; ++s) { const u32 v = gload(ws, off); if (__all(done || v == tag)) break; __builtin_amdgcn_s_sleep(1); }
  asm volatile("" ::: "memory");
}

#define STAMP(i)
#define STAMPV(i)

struct Frags { bf16x8 x[2]; s16x4 wl[5], wh[5]; };

__global__ __launch_bounds__(1024) void fused(const u16* xA, const u16* wA, const u16* wphi, const u16* woA,
                                              const u16* xB, const u16* wB, const u16* woB, u16* out, char* ws) {
  extern __shared__ __attribute__((aligned(16))) char smem[];
  unsigned long long pc0; u32 len;
  asm volatile("s_getpc_b64 %0\n.Lcode_ref:\n\ts_mov_b32 %1, .Lcode_end-.Lcode_ref" : "=s"(pc0), "=s"(len));
  lds_ptr lds = (lds_ptr)smem;
  const u32 lds0 = (u32)(size_t)lds;
  const int b = blockIdx.x, xcd = b & 7, idx = b >> 3;
  const int br = idx >> 4, rt = (xcd & 3) * 4 + ((idx >> 2) & 3), hg = (xcd >> 2) * 4 + (idx & 3);
  int lane = threadIdx.x & 63; const int wid = RFL(threadIdx.x >> 6);
  int g = lane >> 4, i16 = lane & 15, q4 = i16 >> 2, p4 = lane & 3, h = lane >> 5, l32 = lane & 31;
  auto relane = [&]() { PIN(lane); lane &= 63; g = lane >> 4; i16 = lane & 15; q4 = i16 >> 2; p4 = lane & 3; h = lane >> 5; l32 = lane & 31; };
  const rsrc_t rws = __builtin_amdgcn_make_buffer_rsrc((void*)ws, (short)0, W_TOTAL, 0x00020000);
  lds_ptr misc = lds + L_MISC;
  STAMP(0);

  const rsrc_t rx = __builtin_amdgcn_make_buffer_rsrc((void*)((br ? xB : xA) + (size_t)rt * 128 * D_), (short)0, 128 * D_ * 2, 0x00020000);
  const rsrc_t rw = __builtin_amdgcn_make_buffer_rsrc((void*)(br ? wB : wA), (short)0, D_ * N3 * 2, 0x00020000);
  const int cls = ((idx >> (XMAP ? 2 : 3)) + idx) & 3;
  const unsigned long long orda = (cls & 1) ? 0x8796435201ull : 0x9768532410ull, ordb = (cls & 1) ? 0x9865742130ull : 0x9875463102ull, ord = (cls & 2) ? ordb : orda;
  const int rot = (int)(ord & 15);
  const u32 vx0 = (lane >> 3) * 1280 + (((lane & 7) ^ (lane >> 4)) << 4), vx1 = vx0 ^ 64;
  const int wc = lane >= 30 ? lane - 30 : lane;
  const u32 vw = (lane >= 30 ? 3840 : 0) + (wc >= 20 ? 2240 : wc >= 10 ? 1120 : 0) + hg * 160 + wc * 16;
  auto ldx = [&](int i, int kt, u32 buf) { dma16(rx, (i & 1) ? vx1 : vx0, 128 * kt + i * 8 * 1280, buf + i * 1024); };
  auto ldw1 = [&](int j, int kt, u32 buf) { const int p = 2 * j, k = (p & 0x33) | ((p & 4) << 1) | ((p & 8) >> 1); dma16(rw, vw, (64 * kt + k) * 3840, buf + XB + j * 960); };
  const u32 og = W_OG + br * (128 * 20480);
  u32 pc[9] = {};
  auto pre = [&]() {
    auto og_main = [&](int row, int hd, int hf) -> u32 { return og + (row >> 4) * 20480 + ((hg * 2 + (hd >> 1)) << 10) + (((2 * (hd & 1) + hf) * 16 + (row & 15)) << 4); };
    auto og_tail = [&](int row, int hd) -> u32 { return og + (row >> 4) * 20480 + 16384 + hg * 512 + ((hd * 16 + (row & 15)) << 3); };
    if (br) {
      const int hd = wid & 3, qi = wid < 8 ? 1 - (wid >> 2) : wid >> 2, u = 35 - l32 + 4 * h; const bool vones = (g & 1) && p4;
      pc[0] = L_Q + hd * 6144 + (32 * qi + l32) * 48 + 16 * h;
      pc[1] = hd * 2560 + l32 * 80 + 16 * h;
      pc[2] = h ? L_ONES : hd * 2560 + l32 * 80 + 32;
      pc[3] = vones ? L_ONES : hd * 2560 + (4 * h + q4) * 80 + 40 + 32 * (g & 1) + 8 * p4;
      pc[4] = h ? 0 : 10240; pc[5] = vones ? 0 : 10240;
      pc[6] = L_MASK + (u & 3) * 256 + (u & ~3) * 4;
      pc[7] = og_main(rt * 128 + 32 * qi + l32, hd, h);
      pc[8] = h ? OOB : og_tail(rt * 128 + 32 * qi + l32, hd);
      pc[0] += lds0; pc[1] += lds0; pc[2] += lds0; pc[3] += lds0; pc[6] += lds0;
    } else {
      const int tg = wid & 3, pair = (wid >> 2) & 1, isq = wid >> 3;
      pc[0] = (isq ? L_AQ : L_AK) + (32 * tg + l32) * 176 + pair * 80 + 16 * h;
      pc[1] = L_WTAB + pair * 5120 + lane * 16;
      pc[2] = L_WTAB + pair * 5120 + 2048 + lane * 8;
      if (isq) {
        const int hp = (l32 >> 2) & 1, r = (l32 & 3) + 4 * (l32 >> 3);
        pc[3] = (h == hp) ? L_KVT + (2 * pair + h) * 1680 + r * 80 : L_ZERO;
        pc[4] = (h == hp && r <= 4) ? L_KVT + (2 * pair + h) * 1680 + (16 + r) * 80 : L_ZERO;
        pc[5] = og_main(rt * 128 + 32 * tg + l32, 2 * pair + h, 0);
        pc[6] = og_tail(rt * 128 + 32 * tg + l32, 2 * pair + h);
        pc[7] = 0;
      } else {
        const int hd = 2 * pair + (tg >> 1), dt = tg & 1;
        pc[3] = L_AV + hd * 6144 + (8 * g + q4) * 48 + 32 * dt + 8 * p4;
        pc[4] = L_PK + (2 * pair + h) * 10240 + (32 * tg + l32) * 80;
        pc[5] = L_PK + hd * 10240 + (8 * g + q4) * 80 + 8 * p4;
        pc[6] = (!dt || i16 <= 4) ? W_PART + ((hg * 4 + hd) * 16 + rt) * PARTB + (dt * 160 + (dt ? g * 5 + i16 : lane)) * 16 : OOB;
        pc[7] = g < 2 ? pc[6] : OOB;
      }
      pc[0] += lds0; pc[1] += lds0; pc[2] += lds0; pc[3] += lds0; pc[4] += lds0; if (!isq) pc[5] += lds0;
      pc[8] = pc[0] + 64 - 8 * h;
    }
#pragma unroll
    for (int i = 0; i < 9; i++) PIN(pc[i]);
  };
  if (wid < 4) {
    __builtin_amdgcn_s_setprio(3);
    u32 ep = 0;
    if (wid == 3) ep = gload(ws, W_EPOCH);
    if (wid == 2) *(LAS u32*)(misc + lane * 4) = 0;
    int kt = rot, bi = 0;
    if (wid == 1) {
      const rsrc_t rc = __builtin_amdgcn_make_buffer_rsrc((void*)(pc0 & ~15ull), (short)0, len & ~15u, 0x00020000);
      dma16(rc, idx * 1024 + lane * 16, 0, lds0 + L_DUMMY);
    }
    __builtin_amdgcn_s_barrier();
#pragma nounroll
    for (int s = 1; s < 10; s++) {
      bi = bi == 2 ? 0 : bi + 1; kt = (int)(ord >> (4 * s)) & 15;
      const u32 buf = lds0 + bi * STB;
      if (s == 2 && br == 0) {
        const rsrc_t rp = __builtin_amdgcn_make_buffer_rsrc((void*)(wphi + hg * 1600), (short)0, 3200, 0x00020000);
        dma16(rp, wid * 1024 + lane * 16, 0, lds0 + L_RAW + wid * 1024);
      }
#pragma unroll
      for (int i = 0; i < 4; i++) ldx(wid * 4 + i, kt, buf);
      if (lane < 60) {
#pragma unroll
        for (int j = 0; j < 8; j++) ldw1(wid * 8 + j, kt, buf);
      }
      waitv<12>();
      lbar();
      if (s == 9) pre();
      if (s == 9 && br == 0) {
        const int pair = wid >> 1, T = wid & 1, rb = g & 1;
        const int fb = 2 * rb + (p4 >> 1);
        const bool okT = (T == 0) || (fb == 0);
        const int f0 = T ? 16 : 4 * fb;
        s16x4 r[5];
#pragma unroll
        for (int u = 0; u < 5; u++) {
          const int cg = (u < 4) ? 4 * (u >> 1) + 2 * h + (u & 1) : 8 + h;
          const int hc = cg >= 5, d0 = 4 * (cg - 5 * hc);
          const bool ok = okT && (hc == (p4 & 1));
          r[u] = trrd(lds + (ok ? L_RAW + (2 * pair + hc) * 800 + (d0 + q4) * 40 + f0 * 2 : L_ZERO));
        }
        lds_ptr tb = lds + L_WTAB + wid * 2560;
        *(LAS bf16x8*)(tb + lane * 16) = cat8(r[0], r[1]);
        *(LAS bf16x8*)(tb + 1024 + lane * 16) = cat8(r[2], r[3]);
        *(LAS s16x4*)(tb + 2048 + lane * 8) = r[4];
      }
      STAMPV(32 + s - 1);
    }
    {
      const rsrc_t rwo = __builtin_amdgcn_make_buffer_rsrc((void*)(br ? woB : woA), (short)0, D_ * D_ * 2, 0x00020000);
      waitv<0>();
      if (wid == 3 && lane == 0) *(LAS u32*)(misc + M_TAG) = ep * 0x9E3779B1u + 0x7F4A7C15u;
      lbar();
      STAMP(15);
#pragma nounroll
      for (int j = 0; j < 5; j++) {
        const int i = wid * 5 + j;
        const u32 o_ = i < 16 ? og + (rt * 8 + (i >> 1)) * 20480 + hg * 2048 + (i & 1) * 1024 + lane * 16
                              : og + (rt * 8 + 2 * (i - 16) + h) * 20480 + 16384 + hg * 512 + l32 * 16;
        __builtin_amdgcn_raw_buffer_store_b128(u32x4{~0u, ~0u, ~0u, ~0u}, rws, o_, 0, 16);
      }
      { const int ln = (idx & 15) * 200 + wid * 50 + (lane < 50 ? lane : 49); dma16(rwo, (ln / 5) * 1280 + (hg >> 2) * 640 + (ln % 5) * 128, 0, lds0 + L_DUMMY); }
#define NPF 1
    }
    if (br) {
      waitv<NPF>();
      if (lane == 0 && lds_add_ret(misc + M_C4) == 3) gstore(ws, W_STARTED + ((br * 16 + rt) * 8 + hg) * 4, lds_word(misc + M_TAG));
    }
  } else {
    const int cw = wid - 4, mi = cw & 3, nj = cw < 8 ? (cw >> 2) + 1 : 0;
    if (cw < 4) {
#pragma unroll
      for (int i = 0; i < 4; i++) ldx(cw * 4 + i, rot, lds0);
    } else if (lane < 60) {
#pragma unroll
      for (int j = 0; j < 4; j++) ldw1((cw - 4) * 4 + j, rot, lds0);
    }
    __builtin_amdgcn_s_barrier();
    if (cw < 8) {
      const int c = cw & 3, x = lane + c - 35;
      *(LAS float*)(lds + L_MASK + cw * 256 + lane * 4) = ((x > 0) != (cw >= 4)) ? -1e30f : 0.f;
    } else if (cw == 8 && lane < 4) *(LAS u32x2*)(lds + L_ONES + lane * 640) = u32x2{0x3F803F80u, 0x3F803F80u};
    pre();
    const int row = mi * 32 + i16;
    const u32 xa0 = row * 128 + ((g ^ ((row >> 1) & 7)) << 4), xa1 = xa0 ^ 64;
    const u32 wa = XB + (16 * (g >> 1) + 4 * (g & 1) + q4) * 480 + 8 * p4 + nj * 160;
    int base, HS, RS;
    if (br) {
      if (nj == 0) { base = L_Q; HS = 6144; RS = 48; }
      else { base = L_OWN + (nj - 1) * 40 + mi * 7680; HS = 2560; RS = 80; }
    } else {
      if (nj == 2) { base = L_AV; HS = 6144; RS = 48; }
      else { base = nj ? L_AK : L_AQ; HS = 40; RS = 176; }
    }
    int ea[5];
#pragma unroll
    for (int n = 0; n < 5; n++) { const int c4 = 4 * n + g, hd = (c4 * 13) >> 6, d4 = c4 - 5 * hd; ea[n] = lds0 + base + hd * HS + row * RS + d4 * 8; PIN(ea[n]); }
    f32x4 acc[2][5] = {};
    auto rd = [&](Frags& f, lds_ptr bb, int ks, int j) {
      if (j < 2) f.x[j] = ld128(bb + (ks ? xa1 : xa0) + j * 2048);
      else { int n = (j - 2) >> 1; if (j & 1) f.wh[n] = trrd(bb + wa + ks * 15360 + 3840 + n * 32); else f.wl[n] = trrd(bb + wa + ks * 15360 + n * 32); }
    };
#define MM(F, i) acc[(i) & 1][(i) >> 1] = MFMA16(cat8(F.wl[(i) >> 1], F.wh[(i) >> 1]), F.x[(i) & 1], acc[(i) & 1][(i) >> 1])
    Frags F0, F1;
    int boff = 0;
    __builtin_amdgcn_s_setprio(3);
    waitv<0>();
    lbar();
    STAMP(14);
#pragma unroll
    for (int j = 0; j < 12; j++) rd(F0, lds, 0, j);
#pragma unroll
    for (int t = 0; t < 10; t++) {
      SBAR();
#pragma unroll
      for (int i = 0; i < 10; i++) { MM(F0, i); if (i < 6) { rd(F1, lds + boff, 1, 2 * i); rd(F1, lds + boff, 1, 2 * i + 1); } SBAR(); }
#pragma unroll
      for (int i = 0; i < 10 - HB; i++) MM(F1, i);
      SBAR();
      boff = ((t + 1) % 3) * STB; asm volatile("" : "+s"(boff));
      if (t < 9) {
        __builtin_amdgcn_s_waitcnt(0xC07F);
        asm volatile("s_barrier" : "+v"(acc[0][0]), "+v"(acc[1][0]), "+v"(acc[0][1]), "+v"(acc[1][1]), "+v"(acc[0][2]), "+v"(acc[1][2]), "+v"(acc[0][3]), "+v"(acc[1][3]), "+v"(acc[0][4]), "+v"(acc[1][4]) :: "memory");
      } else {
        lds_inc(misc + M_C8, lane);
        asm volatile("" : "+v"(acc[0][0]), "+v"(acc[1][0]), "+v"(acc[0][1]), "+v"(acc[1][1]), "+v"(acc[0][2]), "+v"(acc[1][2]), "+v"(acc[0][3]), "+v"(acc[1][3]), "+v"(acc[0][4]), "+v"(acc[1][4]));
      }
      asm volatile("" ::: "memory");
#pragma unroll
      for (int i = 10 - HB; i < 10; i++) {
#pragma unroll
        for (int j = (i - 10 + HB) * RPM; j < (i - 9 + HB) * RPM && j < 12; j++) if (t < 9) rd(F0, lds + boff, 0, j);
        MM(F1, i); SBAR();
      }
    }
    STAMP(1);
    relane();
    u32x2 pk[2][5];
    if (br && nj == 0) {
#pragma unroll
      for (int i = 0; i < 10; i++) { const f32x4 v = acc[i & 1][i >> 1] * (0.22360679774997896f * 1.4426950408889634f); pk[i & 1][i >> 1] = u32x2{cvtpk(v[0], v[1]), cvtpk(v[2], v[3])}; }
    } else {
#pragma unroll
      for (int i = 0; i < 10; i++) { const f32x4 v = acc[i & 1][i >> 1]; pk[i & 1][i >> 1] = u32x2{cvtpk(v[0], v[1]), cvtpk(v[2], v[3])}; asm volatile("" ::: "memory"); }
    }
    int eb[5];
#pragma unroll
    for (int n = 0; n < 5; n++) { eb[n] = ea[n] + 16 * RS; PIN(eb[n]); }
#pragma unroll
    for (int n = 0; n < 5; n++) { *(LAS u32x2*)LP(ea[n]) = pk[0][n]; *(LAS u32x2*)LP(eb[n]) = pk[1][n]; }
    if (br == 0 && nj == 2) {
#pragma unroll
      for (int m = 0; m < 2; m++) *(LAS u32x2*)(lds + L_AV + g * 6144 + (mi * 32 + i16 + 16 * m) * 48 + 40) = u32x2{0x3F803F80u, 0x3F803F80u};
    }
    lds_inc(misc + M_WR + nj * 16 + mi * 4, lane);
    if (nj == 2) lds_inc(misc + M_WV, lane);
    lds_inc(misc + M_C9, lane);
    if (br && nj == 1) {
      lds_wait_ge(misc + M_WR + 32 + mi * 4, 1u);
      if (mi < 2) __builtin_amdgcn_s_sleep(6);
      const u32 dst = W_KVG + ((rt * 8 + hg) * 4 + mi) * 10240 + lane * 16;
      u32x4 v[10];
#pragma unroll
      for (int i = 0; i < 10; i++) v[i] = *(LAS u32x4*)(lds + L_OWN + mi * 10240 + i * 1024 + lane * 16);
      SBAR();
#pragma unroll
      for (int i = 0; i < 10; i++) __builtin_amdgcn_raw_buffer_store_b128(v[i], rws, dst, i * 1024, 16);
      waitv<0>();
      if (lane == 0) gstore(ws, W_KVFLAG + (rt * 8 + hg) * 128 + mi * 4, lds_word(misc + M_TAG));
      lds_inc(misc + (mi >> 1 ? M_P1 : M_P0), lane);
      STAMP(4);
    } else if (br) __builtin_amdgcn_s_setprio(0);
  }
  relane();
  f32x16 o = {};
  PIN(o);
  if (br) lds_wait_ge(misc + M_C9, 12u);
  STAMP(2);
  const u32 tag = RFL(lds_word(misc + M_TAG));
  const int l_wo = br ? LB_WO : LA_WO;
  auto started = [&]() { if (lane == 0) gstore(ws, W_STARTED + ((br * 16 + rt) * 8 + hg) * 4, tag); };
  auto guard = [&]() { poll_tag(ws, W_STARTED + ((br * 16 + rt) * 8 + (lane & 7)) * 4, tag, false); lds_inc(misc + M_C6, lane); };
  const rsrc_t rwo = __builtin_amdgcn_make_buffer_rsrc((void*)(br ? woB : woA), (short)0, D_ * D_ * 2, 0x00020000);
  u32 wvm[2] = {}, wvt[2] = {};
  const int wj = wid & 3;
  auto wo_init = [&]() {
#pragma unroll
    for (int u = 0; u < 2; u++) {
      int c = (u ? 4 : wj) * 64 + lane; PIN(c);
      const int p = c / 10, ch = c - p * 10;
      const int gg = ((p >> 3) & 2) | ((p >> 2) & 1), tt = (p >> 3) & 1, e = p & 3;
      wvm[u] = ((gg >> 1) * 20 + 4 * (gg & 1) + 8 * tt + e) * 1280 + hg * 160 + ch * 16; wvt[u] = (tt * 80 + gg * 20 + 16 + e) * 1280 + hg * 160 + ch * 16;
    }
  };
  auto wo_dma = [&](int k0, int k1) {
    if (k1 == 20) {
#pragma unroll
      for (int ks = 16; ks < 20; ks++) dma16(rwo, wvt[0], (ks - 16) * 160 * 1280, lds0 + l_wo + ks * 5120 + wj * 1024);
      dma16(rwo, wvt[1], wj * 160 * 1280, lds0 + l_wo + (16 + wj) * 5120 + 4096);
      k1 = 16;
    }
#pragma nounroll
    for (int ks = k0; ks < k1; ks++) {
      dma16(rwo, wvm[0], ks * 40 * 1280, lds0 + l_wo + ks * 5120 + wj * 1024);
      if ((ks & 3) == wj) dma16(rwo, wvm[1], ks * 40 * 1280, lds0 + l_wo + ks * 5120 + 4096);
    }
  };
  const bool wow = wid >= 12;
  const u32 ofl = W_OFLAG + (br * 16 + rt) * 128;
  auto published = [&](int rg) {
    waitv<0>();
    lds_inc(misc + M_D + rg * 4, lane);
    if (lane == 0 && lds_add_ret(misc + M_C2) == (br ? 15 : 7)) gstore(ws, ofl + hg * 4, tag);
  };

  if (br) {
    const int hd = wid & 3, qi = wid < 8 ? 1 - (wid >> 2) : wid >> 2;
    const u32 kvf = W_KVFLAG + (rt * 8 + hg) * 128;
    const bf16x8 qa = ld128(LP(pc[0]));
    s16x4 qb = ld64(LP(pc[0]) + 32);
    if (h) qb = s16x4{0, 0, 0, 0};
    auto ldmask = [&](int pol) -> f32x16 {
      f32x4 t[4];
#pragma unroll
      for (int j = 0; j < 4; j++) t[j] = *(LAS f32x4*)(LP(pc[6]) + pol * 1024 + j * 32);
      return __builtin_shufflevector(__builtin_shufflevector(t[0], t[1], 0, 1, 2, 3, 4, 5, 6, 7), __builtin_shufflevector(t[2], t[3], 0, 1, 2, 3, 4, 5, 6, 7),
                                     0, 1, 2, 3, 4, 5, 6, 7, 8, 9, 10, 11, 12, 13, 14, 15);
    };
    auto pk4 = [&](const f32x16& s, int o_) -> bf16x8 {
      u32x4 p;
#pragma unroll
      for (int j = 0; j < 4; j++) p[j] = cvtpk(ex2(s[o_ + 2 * j]), ex2(s[o_ + 2 * j + 1]));
      return __builtin_bit_cast(bf16x8, p);
    };
    lds_ptr ka_p, kb_p, vp;
    auto seek = [&](int row) { ka_p = LP(pc[1]) + row * 10240; kb_p = LP(pc[2]) + row * pc[4]; vp = LP(pc[3]) + row * pc[5]; };
    f32x16 s, s2; s16x4 c;
    auto step = [&](f32x16& sc, f32x16& sn) {
      ka_p -= 10240; kb_p -= pc[4];
      const bf16x8 ka = ld128(ka_p); const s16x4 kb = ld64(kb_p);
      const bf16x8 v0 = cat8(trrd(vp), trrd(vp + 640)), v1 = cat8(trrd(vp + 1280), trrd(vp + 1920));
      vp -= pc[5];
      SBAR();
      float e[8]; u32x4 p1;
      bf16x8 p0 = pk4(sc, 0); PIN(p0);
      SBAR(); sn = MFMA32(ka, qa, f32x16{}); SBAR();
#pragma unroll
      for (int j = 0; j < 4; j++) e[j] = ex2(sc[8 + j]);
      SBAR(); sn = MFMA32K8(kb, c, sn); SBAR();
#pragma unroll
      for (int j = 4; j < 8; j++) e[j] = ex2(sc[8 + j]);
      SBAR(); o = MFMA32(v0, p0, o); SBAR();
#pragma unroll
      for (int j = 0; j < 4; j++) p1[j] = cvtpk(e[2 * j], e[2 * j + 1]);
      SBAR(); o = MFMA32(v1, __builtin_bit_cast(bf16x8, p1), o); SBAR();
    };
    auto last = [&](f32x16& sc) {
      const bf16x8 v0 = cat8(trrd(vp), trrd(vp + 640)), v1 = cat8(trrd(vp + 1280), trrd(vp + 1920));
      o = MFMA32(v0, pk4(sc, 0), o);
      o = MFMA32(v1, pk4(sc, 8), o);
    };
    seek(L_OWN / 10240 + qi);
    {
      const s16x4 kb = ld64(kb_p);
      s = MFMA32K8(kb, qb, MFMA32(ld128(ka_p), qa, ldmask(0)));
      float mm = fmaxf(s[0], s[1]);
#pragma unroll
      for (int r = 2; r < 16; r += 2) mm = fmaxf(fmaxf(mm, s[r]), s[r + 1]);
      auto rr = __builtin_amdgcn_permlane32_swap(__float_as_uint(mm), __float_as_uint(mm), false, false);
      const u32 nm = cvtpk(-fmaxf(__uint_as_float(rr[0]), __uint_as_float(rr[1])), 0.f);
      const u32x2 qw = __builtin_bit_cast(u32x2, qb);
      c = __builtin_bit_cast(s16x4, u32x2{h ? nm : qw.x, qw.y});
      s = MFMA32K8(kb, __builtin_bit_cast(s16x4, u32x2{h ? nm : 0u, 0u}), s);
    }
    if (qi >= 2) { step(s, s2); step(s2, s); }
    if (qi & 1) { step(s, s2); last(s2); } else last(s);
    lds_inc(misc + M_C1, lane);
    STAMP(3);
    if (wid < 4) {
      const int dd = 2 - (wid >> 1);
#pragma nounroll
      for (int hf = 1; hf >= 0; --hf) {
        const int tl = 2 * hf + (wid & 1);
        lds_wait_ge(misc + (hf ? M_P1 : M_P0), 2u);
        if (rt >= dd) poll_tag(ws, kvf - dd * 1024 + tl * 4, tag, false);
        if (hf) { STAMP(11); } else { STAMP(5); lds_inc(misc + M_C0, lane); }
        if (rt >= dd) {
          const u32 src = W_KVG + (((rt - dd) * 8 + hg) * 4 + tl) * 10240;
#pragma unroll
          for (int i = 0; i < 10; i++) dma16c(rws, lane * 16, src + i * 1024, lds0 + L_HALO + hf * 40960 + wid * 10240 + i * 1024);
        }
      }
    }
    if (wow) wo_init();
    lds_wait_ge(misc + M_C0, 4u);
    int wk = 20;
    if (rt > 0) {
      int n1 = 2, nn = 4, km = 0;
      if (rt >= 2) { n1 = qi < 3 ? 4 : 3; nn = 8 - qi; km = qi >= 2 ? n1 - 1 : nn - 1; }
      const u32 ev1 = (1u << n1) | (wid < 4 ? 4u : 0u) | (wow ? ~0u : 0u);
      const u32 ev2 = (1u << n1) | (1u << km);
      if (wow) __builtin_amdgcn_s_setprio(3); else __builtin_amdgcn_s_setprio(2);
      seek(7);
      s = MFMA32K8(ld64(kb_p), c, MFMA32(ld128(ka_p), qa, f32x16{}));
      auto before = [&](int k) {
        if (k == 2 && wid < 4) { waitv<0>(); lds_inc(misc + M_C7, lane); }
        if (k == n1) {
          ka_p -= (4 - n1) * 10240; kb_p -= (4 - n1) * pc[4];
          lds_wait_ge(misc + M_C7, 4u);
          if (wid == 10) guard();
        }
        if (wow && wk > 0) {
          const u32 c1 = RFL(lds_word(misc + M_C1)), c7 = RFL(lds_word(misc + M_C7)), c3 = RFL(lds_word(misc + M_C3));
          const int lim = c3 >= 16 ? 0 : (c1 >= 16 && c7 >= 4) ? 5 : 20;
          const int k0 = wk - 8 > lim ? wk - 8 : lim;
          if (k0 < wk) { wo_dma(k0, wk); wk = k0; }
        }
      };
      auto after = [&](int k, f32x16& sn) {
        if (k == km) sn += ldmask(1);
        if (k == n1) { vp -= (4 - n1) * pc[5]; lds_inc(misc + M_C3, lane); if (!wow) __builtin_amdgcn_s_setprio(1); }
      };
#pragma nounroll
      for (int k = 1;; k += 2) {
        if (__builtin_expect((ev1 >> k) & 1, 0)) before(k);
        step(s, s2);
        if (__builtin_expect((ev2 >> k) & 1, 0)) after(k, s2);
        if (__builtin_expect(k + 1 == nn, 0)) { s = s2; break; }
        if (__builtin_expect((ev1 >> (k + 1)) & 1, 0)) before(k + 1);
        step(s2, s);
        if (__builtin_expect((ev2 >> (k + 1)) & 1, 0)) after(k + 1, s);
        if (__builtin_expect(k + 2 == nn, 0)) break;
      }
      last(s);
    } else {
      if (wid == 10) guard();
    }
    float l = h ? o[8] : o[12];
    if (__builtin_expect(__any(!(l < 1.8e19f)), 0)) {
      const int dd = lane % 3;
      poll_tag(ws, kvf - (rt - dd < 0 ? 0 : dd) * 1024 + 4 * (lane & 3), tag, false);
      float q[20], a[20], mx = -3e38f; l = 0.f;
      const u32x4 qw = __builtin_bit_cast(u32x4, qa); const u32x2 qv = __builtin_bit_cast(u32x2, qb);
#pragma unroll
      for (int j = 0; j < 10; j++) {
        const u32 w = j < 8 ? __shfl(qw[j & 3], l32 + 32 * (j >> 2)) : __shfl(qv[j & 1], l32);
        q[2 * j] = blo(w); q[2 * j + 1] = bhi(w); a[2 * j] = 0.f; a[2 * j + 1] = 0.f;
      }
      const int row = rt * 128 + 32 * qi + l32;
#pragma nounroll
      for (int t = 0; t < 128; ++t) {
        const int j = row - 2 * t - h;
        if (j >= 0) {
          const u32 src = W_KVG + (((j >> 7) * 8 + hg) * 4 + ((j >> 5) & 3)) * 10240 + hd * 2560 + (j & 31) * 80;
          u32 w[20];
#pragma unroll
          for (int i = 0; i < 5; i++) { const u32x4 v = __builtin_amdgcn_raw_buffer_load_b128(rws, src, i * 16, 16); w[4 * i] = v.x; w[4 * i + 1] = v.y; w[4 * i + 2] = v.z; w[4 * i + 3] = v.w; }
          float sc = 0.f;
#pragma unroll
          for (int i = 0; i < 10; i++) sc += q[2 * i] * blo(w[i]) + q[2 * i + 1] * bhi(w[i]);
          const float mn = fmaxf(mx, sc), f = ex2(mx - mn), p = ex2(sc - mn);
          mx = mn; l = l * f + p;
#pragma unroll
          for (int i = 0; i < 10; i++) { a[2 * i] = a[2 * i] * f + p * blo(w[10 + i]); a[2 * i + 1] = a[2 * i + 1] * f + p * bhi(w[10 + i]); }
        }
      }
      const float mo = __shfl_xor(mx, 32), mn = fmaxf(mx, mo), f = ex2(mx - mn);
      l *= f; l += __shfl_xor(l, 32);
#pragma unroll
      for (int i = 0; i < 20; i++) { a[i] *= f; a[i] += __shfl_xor(a[i], 32); }
#pragma unroll
      for (int r = 0; r < 8; r++) o[r] = h ? a[(r & 3) + 8 * (r >> 2) + 4] : a[(r & 3) + 8 * (r >> 2)];
#pragma unroll
      for (int r = 8; r < 12; r++) o[r] = a[8 + r];
    }
    const float rl = __builtin_amdgcn_rcpf(l);
    const u32x4 w0 = {cvtpk(o[0] * rl, o[1] * rl), cvtpk(o[2] * rl, o[3] * rl), cvtpk(o[4] * rl, o[5] * rl), cvtpk(o[6] * rl, o[7] * rl)};
    const u32x2 w1 = {cvtpk(o[8] * rl, o[9] * rl), cvtpk(o[10] * rl, o[11] * rl)};
    __builtin_amdgcn_raw_buffer_store_b128(w0, rws, pc[7], 0, 16);
    __builtin_amdgcn_raw_buffer_store_b64(w1, rws, pc[8], 0, 16);
    STAMP(6);
    lds_inc(misc + M_AD, lane);
    if (wow && wk > 5) { lds_wait_ge(misc + M_C1, 16u); wo_dma(5, wk); wk = 5; }
    published(qi);
    if (wow) {
      lds_inc(misc + M_C6, lane);
      if (wk > 0) { if (rt) lds_wait_ge(misc + M_C3, 16u); wo_dma(0, wk); waitv<0>(); }
      lds_inc(misc + M_C6B, lane);
    }
    __builtin_amdgcn_s_setprio(0);
  } else {
    const int tg = wid & 3, pair = (wid >> 2) & 1, isq = wid >> 3;
    u32 phi[20];
    {
      lds_wait_ge(misc + M_WR + (isq ? 0 : 16) + tg * 4, 1u);
      const bf16x8 b0 = ld128(LP(pc[0])), a00 = ld128(LP(pc[1])), a10 = ld128(LP(pc[1]) + 2560);
      const bf16x8 b1 = ld128(LP(pc[0]) + 32), a01 = ld128(LP(pc[1]) + 1024), a11 = ld128(LP(pc[1]) + 2560 + 1024);
      const s16x4 b2 = ld64(LP(pc[8])), a02 = ld64(LP(pc[2])), a12 = ld64(LP(pc[2]) + 2560);
      lds_inc(misc + M_C5, lane);
      if (wow) { wo_init(); lds_wait_ge(misc + M_C5, 16u); wo_dma(6, 20); waitv<0>(); lds_inc(misc + M_C6, lane); }
      if (isq) __builtin_amdgcn_s_setprio(0); else __builtin_amdgcn_s_setprio(2);
      f32x16 t0 = MFMA32(a00, b0, f32x16{}), t1 = MFMA32(a10, b0, f32x16{});
      t0 = MFMA32(a01, b1, t0); t1 = MFMA32(a11, b1, t1);
      t0 = MFMA32K8(a02, b2, t0); t1 = MFMA32K8(a12, b2, t1);
      float p[20];
#pragma unroll
      for (int j = 0; j < 10; j++) {
        const u32 w = j < 8 ? cvtpk(t0[2 * j], t0[2 * j + 1]) : cvtpk(t1[2 * j - 16], t1[2 * j - 15]);
        p[2 * j] = blo(w); p[2 * j + 1] = bhi(w);
      }
      float mx = fmaxf(p[0], p[1]), mn = fminf(p[0], p[1]);
#pragma unroll
      for (int j = 2; j < 20; j += 2) { mx = fmaxf(fmaxf(mx, p[j]), p[j + 1]); mn = fminf(fminf(mn, p[j]), p[j + 1]); }
      const float L2E = 1.4426950408889634f;
      const f32x2 sl = {L2E, -L2E}, of = {-mx * L2E, mn * L2E};
      f32x2 e[20], sa = {0.f, 0.f}, sb = {0.f, 0.f};
#pragma unroll
      for (int j = 0; j < 20; j++) {
        const f32x2 a = __builtin_elementwise_fma(f32x2{p[j], p[j]}, sl, of);
        e[j] = f32x2{ex2(a[0]), ex2(a[1])};
        if (j & 1) sb += e[j]; else sa += e[j];
      }
      sa += sb;
      const f32x2 rr = {__builtin_amdgcn_rcpf(sa[0]), __builtin_amdgcn_rcpf(sa[1])};
#pragma unroll
      for (int j = 0; j < 20; j++) phi[j] = cvtpk2(e[j] * rr);
    }
    if (!isq) {
      lds_wait_ge(misc + M_WV, 4u);
      bf16x8 vb[4];
#pragma unroll
      for (int ks = 0; ks < 4; ks++) vb[ks] = cat8(trrd(LP(pc[3]) + ks * 32 * 48), trrd(LP(pc[3]) + ks * 32 * 48 + 4 * 48));
      lds_wait_ge(misc + M_C8, 12u);
#pragma unroll
      for (int j = 0; j < 5; j++) *(LAS u32x4*)(LP(pc[4]) + 16 * j) = u32x4{phi[4 * j], phi[4 * j + 1], phi[4 * j + 2], phi[4 * j + 3]};
      lds_inc(misc + M_C0 + pair * 4, lane);
      STAMP(3);
      lds_wait_ge(misc + M_C0 + pair * 4, 4u);
      f32x4 kv[3] = {};
#pragma unroll
      for (int ks = 0; ks < 4; ks++)
#pragma unroll
        for (int T = 0; T < 3; T++) kv[T] = MFMA16(cat8(trrd(LP(pc[5]) + ks * 2560 + T * 32), trrd(LP(pc[5]) + ks * 2560 + 320 + T * 32)), vb[ks], kv[T]);
#pragma unroll
      for (int T = 0; T < 3; T++)
        __builtin_amdgcn_raw_buffer_store_b128(u32x4{cvtpk(kv[T][0], kv[T][1]), tag, cvtpk(kv[T][2], kv[T][3]), tag}, rws, pc[T < 2 ? 6 : 7], T * ((tg & 1) ? 320 : 1024), 16);
      waitv<3>();
      lds_inc(misc + M_C1, lane);
      STAMP(4);
      if (wid >= 4) { wo_init(); lds_wait_ge(misc + M_C1, 8u); wo_dma(0, 6); waitv<0>(); lds_inc(misc + M_C6, lane); }
      if (wid == 0) {
        const int sl = (rt & 3) * 53 + lane; const bool act = lane < 53 && sl < 210;
        const u32 src = act ? W_PART + (hg * 4 + (rt >> 2)) * 16 * PARTB + sl * 16 : OOB;
        float s0 = 0.f, s1 = 0.f, s2 = 0.f, s3 = 0.f;
        lds_wait_ge(misc + M_C6, 8u);
#pragma nounroll
        for (u32 s = 0; s < SPIN_CAP; ++s) {
          bool ok = true; s0 = 0.f; s1 = 0.f; s2 = 0.f; s3 = 0.f;
          u32x4 v[16];
#pragma unroll
          for (int r = 0; r < 16; r++) v[r] = __builtin_amdgcn_raw_buffer_load_b128(rws, src, r * PARTB, 16);
#pragma unroll
          for (int r = 0; r < 16; r++) { ok &= (v[r].y == tag) & (v[r].w == tag); s0 += blo(v[r].x); s1 += bhi(v[r].x); s2 += blo(v[r].z); s3 += bhi(v[r].z); }
          if (__all(ok || !act)) break;
          asm volatile("" ::: "memory");
        }
        __builtin_amdgcn_raw_buffer_store_b128(u32x4{cvtpk(s0, s1), tag, cvtpk(s2, s3), tag}, rws, act ? W_SLICE + (hg * 16 + rt) * 1024 + lane * 16 : OOB, 0, 16);
        waitv<0>();
        lds_inc(misc + M_C4, lane);
        STAMP(5);
      } else if (wid == 1) { lds_wait_ge(misc + M_C1, 8u); started(); lds_wait_ge(misc + M_C4, 1u); guard(); }
      else if (wid >= 4) {
        const int hh = wid - 4;
        const u32 src = W_SLICE + (hg * 16 + hh * 4) * 1024 + lane * 16;
        int da[4];
#pragma unroll
        for (int j = 0; j < 4; j++) {
          const int sl = j * 53 + lane; int d, ph;
          if (sl < 160) { d = sl & 15; ph = (sl >> 4) * 4; }
          else { const int s2 = sl - 160, gg = s2 / 5; d = 16 + s2 - 5 * gg; ph = 4 * gg; }
          da[j] = L_KVT + hh * 1680 + d * 80 + ph * 2;
        }
        lds_wait_ge(misc + M_C5, 16u);
        lds_wait_ge(misc + M_C4, 1u);
        u32x4 v[4];
#pragma nounroll
        for (u32 s = 0; s < SPIN_CAP; ++s) {
          bool ok = true;
#pragma unroll
          for (int r = 0; r < 4; r++) v[r] = __builtin_amdgcn_raw_buffer_load_b128(rws, src, r * 1024, 16);
#pragma unroll
          for (int r = 0; r < 4; r++) ok &= ((v[r].y == tag) & (v[r].w == tag)) | (lane >= (r < 3 ? 53 : 51));
          if (__all(ok)) break;
          asm volatile("" ::: "memory");
        }
#pragma unroll
        for (int r = 0; r < 4; r++) if (lane < (r < 3 ? 53 : 51)) *(LAS u32x2*)(lds + da[r]) = u32x2{v[r].x, v[r].z};
        lds_inc(misc + M_C3, lane);
        STAMP(11);
      }
      __builtin_amdgcn_s_setprio(0);
    } else {
      lds_ptr a0 = LP(pc[3]), a1 = LP(pc[4]); const u32 om = pc[5], ot = pc[6];
      STAMP(12);
      lds_wait_ge(misc + M_C3, 4u);
      f32x16 t0 = {}, t1 = {};
#pragma unroll
      for (int j = 0; j < 5; j++) {
        const bf16x8 bq = __builtin_bit_cast(bf16x8, u32x4{phi[4 * j], phi[4 * j + 1], phi[4 * j + 2], phi[4 * j + 3]});
        t1 = MFMA32(ld128(a1 + 16 * j), bq, t1); t0 = MFMA32(ld128(a0 + 16 * j), bq, t0);
      }
      const float rl = __builtin_amdgcn_rcpf(t1[4] + 1e-6f);
      const u32x4 w0 = {cvtpk(t0[0] * rl, t0[1] * rl), cvtpk(t0[2] * rl, t0[3] * rl), cvtpk(t0[8] * rl, t0[9] * rl), cvtpk(t0[10] * rl, t0[11] * rl)};
      const u32x4 w1 = {cvtpk(t0[4] * rl, t0[5] * rl), cvtpk(t0[6] * rl, t0[7] * rl), cvtpk(t0[12] * rl, t0[13] * rl), cvtpk(t0[14] * rl, t0[15] * rl)};
      const u32x2 w2 = {cvtpk(t1[0] * rl, t1[1] * rl), cvtpk(t1[2] * rl, t1[3] * rl)};
      __builtin_amdgcn_raw_buffer_store_b128(w0, rws, om, 0, 16);
      __builtin_amdgcn_raw_buffer_store_b128(w1, rws, om, 256, 16);
      __builtin_amdgcn_raw_buffer_store_b64(w2, rws, ot, 0, 16);
      STAMP(6);
      published(tg);
    }
  }
  STAMP(7);
  relane();
  if (wid >= 8) {
    if (b == 0 && wid == 15) {
#pragma nounroll
      for (u32 s = 0; s < SPIN_CAP; ++s) {
        const u32x4 v = __builtin_amdgcn_raw_buffer_load_b128(rws, W_STARTED + lane * 16, 0, 16);
        if (__all(v.x == tag && v.y == tag && v.z == tag && v.w == tag)) break;
        __builtin_amdgcn_s_sleep(2);
      }
      if (lane == 0) gstore(ws, W_EPOCH, tag);
    }
  } else {
    const int rg = br ? (0x2013 >> (4 * (wid >> 1))) & 3 : wid >> 1, kh = wid & 1;
    const u32 obm = og + (rt * 8 + rg * 2) * 20480 + kh * 8192 + lane * 16, obt = og + (rt * 8 + rg * 2) * 20480 + kh * 2048 + lane * 8;
    const u32 mo[2] = {(u32)kh * 20480u, (u32)(1 - kh) * 20480u};
    u32x4 of[10][2];
    auto ldo = [&](int s) {
#pragma unroll
      for (int m = 0; m < 2; m++) {
        if (s < 8) of[s][m] = __builtin_amdgcn_raw_buffer_load_b128(rws, obm, mo[m] + s * 1024, 16);
        else {
          const u32x2 a = __builtin_amdgcn_raw_buffer_load_b64(rws, obt, mo[m] + (s + 8) * 1024, 16);
          const u32x2 c = __builtin_amdgcn_raw_buffer_load_b64(rws, obt, mo[m] + (s + 8) * 1024 + 512, 16);
          of[s][m] = u32x4{a.x, a.y, c.x, c.y};
        }
      }
    };
    u32 wm = lds0 + l_wo + (16 * (g >> 1) + 4 * (g & 1) + q4) * 160 + 8 * p4, wt = wm + (16 + 2 * kh) * 5120; wm += kh * 8 * 5120;
    PIN(wm); PIN(wt);
    bf16x8 wf[WFN];
    auto ldw = [&](int i) { const int s = 9 - i / 5, n = i % 5; lds_ptr p = (s < 8 ? LP(wm) + s * 5120 : LP(wt) + (s - 8) * 5120) + n * 32; wf[i % WFN] = cat8(trrd(p), trrd(p + 1280)); };
    f32x4 ac2[2][5];
    constexpr int PF = P3PF;
    lds_wait_ge(misc + M_C6, br ? 5u : 9u);
    lds_wait_ge(misc + M_D + rg * 4, br ? 4u : 2u);
    STAMP(8);
#pragma nounroll
    for (int pass = 0;; ++pass) {
      if (pass) poll_tag(ws, ofl + (lane & 7) * 4, tag, false);
#pragma unroll
      for (int i = 0; i < WFN; i++) ldw(i);
#pragma nounroll
      for (int tr = 0; tr < 64; ++tr) {
#pragma unroll
        for (int s = 9; s > 5; s--) ldo(s);
        asm volatile("" ::: "memory");
        u32 mx = 0;
#pragma unroll
        for (int s = 8; s < 10; s++)
#pragma unroll
          for (int m = 0; m < 2; m++) mx = max(max(mx, max(of[s][m].x, of[s][m].y)), max(of[s][m].z, of[s][m].w));
        if (pass || !__any(mx == ~0u)) break;
      }
      STAMP(9);
#pragma unroll
      for (int s = 5; s > 9 - PF; s--) ldo(s);
#pragma unroll
      for (int n = 0; n < 5; n++) { ac2[0][n] = f32x4{}; ac2[1][n] = f32x4{}; }
#pragma unroll
      for (int s = 9; s >= 0; s--) {
        if (s == 6) lds_wait_ge(misc + M_C6B, br ? 4u : 0u);
        SBAR();
#pragma unroll
        for (int n = 0; n < 5; n++) {
          const int i = (9 - s) * 5 + n;
          ac2[0][n] = MFMA16(wf[i % WFN], __builtin_bit_cast(bf16x8, of[s][0]), ac2[0][n]);
          ac2[1][n] = MFMA16(wf[i % WFN], __builtin_bit_cast(bf16x8, of[s][1]), ac2[1][n]);
          if (i + WFN < 50) ldw(i + WFN);
          SBAR();
        }
        if (s - PF >= 0) ldo(s - PF);
      }
      const float chk = ac2[0][0][0] + ac2[1][0][0];
      if (pass || !__any(chk != chk)) break;
    }
    STAMP(13);
    relane();
    if (br) lds_wait_ge(misc + M_AD, 16u);
    lds_ptr red = lds + L_RED + lane * 16;
#pragma unroll
    for (int n = 0; n < 5; n++) *(LAS f32x4*)(red + (wid ^ 1) * 5120 + n * 1024) = ac2[1][n];
    lds_inc(misc + M_X + (wid ^ 1) * 4, lane);
    const int r16 = rt * 8 + rg * 2 + kh;
    lds_wait_ge(misc + M_X + wid * 4, 1u);
    f32x4 acc[5];
#pragma unroll
    for (int n = 0; n < 5; n++) acc[n] = *(LAS f32x4*)(red + wid * 5120 + n * 1024);
    SBAR();
#pragma unroll
    for (int n = 0; n < 5; n++) acc[n] += ac2[0][n];
    const rsrc_t ro = __builtin_amdgcn_make_buffer_rsrc((void*)out, (short)0, S_ * 1280 * 2, 0x00020000);
    const u32 obase = (r16 * 16 + i16) * 2560 + br * 1280 + hg * 160;
#pragma unroll
    for (int n = 0; n < 4; n += 2) {
      const auto x = __builtin_amdgcn_permlane16_swap(cvtpk(acc[n][0], acc[n][1]), cvtpk(acc[n + 1][0], acc[n + 1][1]), false, false);
      const auto y = __builtin_amdgcn_permlane16_swap(cvtpk(acc[n][2], acc[n][3]), cvtpk(acc[n + 1][2], acc[n + 1][3]), false, false);
      __builtin_amdgcn_raw_buffer_store_b128(u32x4{x[0], y[0], x[1], y[1]}, ro, obase + (g & 1) * 32 + (g >> 1) * 16, n * 32, 0);
    }
    __builtin_amdgcn_raw_buffer_store_b64(u32x2{cvtpk(acc[4][0], acc[4][1]), cvtpk(acc[4][2], acc[4][3])}, ro, obase + g * 8, 128, 0);
  }
  asm volatile(".Lcode_end:");
  STAMP(10);
}

extern "C" void kernel_launch(void* const* d_in, const int* in_sizes, int n_in, void* d_out, int out_size, void* d_ws, size_t ws_size, hipStream_t stream) {
  (void)hipFuncSetAttribute((const void*)fused, hipFuncAttributeMaxDynamicSharedMemorySize, LDS_BYTES);
  fused<<<256, 1024, LDS_BYTES, stream>>>((const u16*)d_in[0], (const u16*)d_in[1], (const u16*)d_in[2], (const u16*)d_in[3],
                                          (const u16*)d_in[4], (const u16*)d_in[5], (const u16*)d_in[6], (u16*)d_out, (char*)d_ws);
}
```

```cpp
#include <hip/hip_runtime.h>
#include <hip/hip_bf16.h>
#include <type_traits>

typedef unsigned short u16;
typedef unsigned u32;
typedef short bf16x8 __attribute__((ext_vector_type(8)));
typedef short s16x4 __attribute__((ext_vector_type(4)));
typedef float f32x4 __attribute__((ext_vector_type(4)));
typedef float f32x16 __attribute__((ext_vector_type(16)));
typedef float f32x2 __attribute__((ext_vector_type(2)));
typedef __bf16 bf2_t __attribute__((ext_vector_type(2)));
typedef u32 u32x4 __attribute__((ext_vector_type(4)));
typedef u32 u32x2 __attribute__((ext_vector_type(2)));
#define LAS __attribute__((address_space(3)))
#define GAS __attribute__((address_space(1)))
typedef LAS char* lds_ptr;
typedef __amdgpu_buffer_rsrc_t rsrc_t;
#define DI __device__ __forceinline__
#define RFL(x) __builtin_amdgcn_readfirstlane(x)
#define SBAR() __builtin_amdgcn_sched_barrier(0)
#define LP(x) ((lds_ptr)(size_t)(u32)(x))
#define PIN(x) asm volatile("" : "+v"(x))
#define RLX_AGENT __ATOMIC_RELAXED, __HIP_MEMORY_SCOPE_AGENT
#define RLX_WG __ATOMIC_RELAXED, __HIP_MEMORY_SCOPE_WORKGROUP
#define XMAP 2
#define HB 10
#define RPM ((12 + HB - 2) / (HB - 1))
#define S1DELAY 8
#define P3V 1
#define P3S 12
#define P3L 7
#define P3PF 5
#define WFN 8
#define XROT 2
#define ROT 4

constexpr int S_ = 2048, D_ = 640, N3 = 1920;
constexpr int XB = 128 * 64 * 2, WB = 64 * 240 * 2, STB = XB + WB;
constexpr int L_RAW = 3 * STB;
constexpr int L_ZERO = L_RAW + 3200;
constexpr int L_WTAB = L_RAW + 4096;
constexpr int L_MASK = 158720;
constexpr int L_ONES = 160768;
constexpr int L_MISC = L_ONES + 64;
constexpr int L_DUMMY = 162816;
constexpr int LDS_BYTES = 163840;
constexpr int L_HALO = 0, L_OWN = 81920, L_Q = 122880, LB_WO = 56320, L_RED = 0;
constexpr int L_PK = 0, L_AV = 47104, L_AK = 71680, L_AQ = L_AK + 22528, L_KVT = L_WTAB, LA_WO = 40960;
constexpr int M_TAG = 0, M_C0 = 16, M_C1 = 32, M_C2 = 36, M_C3 = 40, M_C4 = 44, M_C5 = 48, M_C6 = 52, M_C7 = 56, M_C9 = 60, M_WR = 64, M_C8 = 112, M_WV = 116, M_P0 = 120, M_P1 = 124, M_D = 128, M_X = 144, M_AD = 176, M_C6B = 180;
constexpr u32 W_EPOCH = 0, W_STARTED = 1024, W_KVFLAG = 4096, W_OFLAG = 32768, W_SLICE = 65536;
constexpr u32 W_PART = 1u << 20, W_KVG = 8u << 20, W_OG = 16u << 20, W_TOTAL = 24u << 20;
constexpr u32 PARTB = 3456;
constexpr u32 OOB = 0x80000000u;
constexpr u32 SPIN_CAP = 1u << 18;

DI void dma16(rsrc_t r, u32 voff, u32 soff, u32 ldsaddr) {
  asm volatile("s_mov_b32 m0, %0\n\ts_nop 0\n\tbuffer_load_dwordx4 %1, %2, %3 offen lds" ::"s"(ldsaddr), "v"(voff), "s"(r), "s"(soff) : "m0", "memory");
}
DI void dma16c(rsrc_t r, u32 voff, u32 soff, u32 ldsaddr) {
  asm volatile("s_mov_b32 m0, %0\n\ts_nop 0\n\tbuffer_load_dwordx4 %1, %2, %3 offen sc1 lds" ::"s"(ldsaddr), "v"(voff), "s"(r), "s"(soff) : "m0", "memory");
}
DI s16x4 trrd(lds_ptr p) { return __builtin_bit_cast(s16x4, __builtin_amdgcn_ds_read_tr16_b64_v4i16((LAS s16x4*)p)); }
DI bf16x8 cat8(s16x4 a, s16x4 b) { return __builtin_shufflevector(a, b, 0, 1, 2, 3, 4, 5, 6, 7); }
DI u32 cvtpk2(f32x2 v) { return __builtin_bit_cast(u32, __builtin_convertvector(v, bf2_t)); }
DI u32 cvtpk(float lo, float hi) { return cvtpk2(f32x2{lo, hi}); }
DI float blo(u32 w) { return __uint_as_float(w << 16); }
DI float bhi(u32 w) { return __uint_as_float(w & 0xffff0000u); }
template <int N> DI void waitv() { asm volatile("s_waitcnt vmcnt(%0)" ::"i"(N) : "memory"); }
DI void lbar() { asm volatile("s_waitcnt lgkmcnt(0)\n\ts_barrier" ::: "memory"); }
DI void waitl() { asm volatile("s_waitcnt lgkmcnt(0)" ::: "memory"); }
DI bf16x8 ld128(lds_ptr p) { return *(LAS bf16x8*)p; }
DI s16x4 ld64(lds_ptr p) { return *(LAS s16x4*)p; }
#define MFMA16(a, b, c) __builtin_amdgcn_mfma_f32_16x16x32_bf16(a, b, c, 0, 0, 0)
#define MFMA32(a, b, c) __builtin_amdgcn_mfma_f32_32x32x16_bf16(a, b, c, 0, 0, 0)
#define MFMA32K8(a, b, c) __builtin_amdgcn_mfma_f32_32x32x8bf16_1k(a, b, c, 0, 0, 0)
DI float ex2(float x) { return __builtin_amdgcn_exp2f(x); }
DI u32 gload(char* ws, u32 off) { return __hip_atomic_load((GAS u32*)(ws + off), RLX_AGENT); }
DI void gstore(char* ws, u32 off, u32 v) { __hip_atomic_store((GAS u32*)(ws + off), v, RLX_AGENT); }
DI u32 lds_word(lds_ptr p) { return __hip_atomic_load((LAS u32*)p, RLX_WG); }
DI void lds_inc(lds_ptr p, int) {
  unsigned long long sv;
  asm volatile("s_mov_b64 %0, exec\n\ts_mov_b64 exec, 1\n\tds_add_u32 %1, %2\n\ts_mov_b64 exec, %0" : "=&s"(sv) : "v"((u32)(size_t)p), "v"(1u) : "memory");
}
DI u32 lds_add_ret(lds_ptr p) { return __hip_atomic_fetch_add((LAS u32*)p, 1u, RLX_WG); }
DI void lds_wait_ge(lds_ptr p, u32 want) {
  u32 v, sv, cnt = SPIN_CAP * 8;
  asm volatile("1:\n\tds_read_b32 %0, %3\n\ts_waitcnt lgkmcnt(0)\n\tv_readfirstlane_b32 %1, %0\n\ts_nop 0\n\ts_cmp_ge_u32 %1, %4\n\ts_cbranch_scc1 2f\n\t"
               "s_sub_u32 %2, %2, 1\n\ts_cbranch_scc1 2f\n\ts_sleep 1\n\ts_branch 1b\n2:"
               : "=&v"(v), "=&s"(sv), "+s"(cnt) : "v"((u32)(size_t)p), "s"(want) : "scc", "memory");
}
DI void poll_tag(char* ws, u32 off, u32 tag, bool done) {
#pragma nounroll
  for (u32 s = 0; s < SPIN_CAP; ++s) { const u32 v = gload(ws, off); if (__all(done || v == tag)) break; __builtin_amdgcn_s_sleep(1); }
  asm volatile("" ::: "memory");
}

#define STAMP(i)
#define STAMPV(i)

struct Frags { bf16x8 x[2]; s16x4 wl[5], wh[5]; };

__global__ __launch_bounds__(1024) void fused(const u16* xA, const u16* wA, const u16* wphi, const u16* woA,
                                              const u16* xB, const u16* wB, const u16* woB, u16* out, char* ws) {
  extern __shared__ __attribute__((aligned(16))) char smem[];
  unsigned long long pc0; u32 len;
  asm volatile("s_getpc_b64 %0\n.Lcode_ref:\n\ts_mov_b32 %1, .Lcode_end-.Lcode_ref" : "=s"(pc0), "=s"(len));
  lds_ptr lds = (lds_ptr)smem;
  const u32 lds0 = (u32)(size_t)lds;
  const int b = blockIdx.x, xcd = b & 7, idx = b >> 3;
  const int br = idx >> 4, rt = (xcd & 3) * 4 + ((idx >> 2) & 3), hg = (xcd >> 2) * 4 + (idx & 3);
  int lane = threadIdx.x & 63; const int wid = RFL(threadIdx.x >> 6);
  int g = lane >> 4, i16 = lane & 15, q4 = i16 >> 2, p4 = lane & 3, h = lane >> 5, l32 = lane & 31;
  auto relane = [&]() { PIN(lane); lane &= 63; g = lane >> 4; i16 = lane & 15; q4 = i16 >> 2; p4 = lane & 3; h = lane >> 5; l32 = lane & 31; };
  const rsrc_t rws = __builtin_amdgcn_make_buffer_rsrc((void*)ws, (short)0, W_TOTAL, 0x00020000);
  lds_ptr misc = lds + L_MISC;
  STAMP(0);

  const rsrc_t rx = __builtin_amdgcn_make_buffer_rsrc((void*)((br ? xB : xA) + (size_t)rt * 128 * D_), (short)0, 128 * D_ * 2, 0x00020000);
  const rsrc_t rw = __builtin_amdgcn_make_buffer_rsrc((void*)(br ? wB : wA), (short)0, D_ * N3 * 2, 0x00020000);
  const int cls = ((idx >> (XMAP ? 2 : 3)) + idx) & 3;
  const unsigned long long orda = (cls & 1) ? 0x8796435201ull : 0x9768532410ull, ordb = (cls & 1) ? 0x9865742130ull : 0x9875463102ull, ord = (cls & 2) ? ordb : orda;
  const int xr = (((xcd & 3) * 5) >> 1) + (xcd >> 2);
  auto slice = [&](int s) { int k = ((int)(ord >> (4 * s)) & 15) + xr; k -= k >= 20 ? 20 : k >= 10 ? 10 : 0; return k; };
  const int rot = slice(0);
  const u32 vx0 = (lane >> 3) * 1280 + (((lane & 7) ^ (lane >> 4)) << 4), vx1 = vx0 ^ 64;
  const int wc = lane >= 30 ? lane - 30 : lane;
  const u32 vw = (lane >= 30 ? 3840 : 0) + (wc >= 20 ? 2240 : wc >= 10 ? 1120 : 0) + hg * 160 + wc * 16;
  auto ldx = [&](int i, int kt, u32 buf) { dma16(rx, (i & 1) ? vx1 : vx0, 128 * kt + i * 8 * 1280, buf + i * 1024); };
  auto ldw1 = [&](int j, int kt, u32 buf) { const int p = 2 * j, k = (p & 0x33) | ((p & 4) << 1) | ((p & 8) >> 1); dma16(rw, vw, (64 * kt + k) * 3840, buf + XB + j * 960); };
  const u32 og = W_OG + br * (128 * 20480);
  u32 pc[9] = {};
  auto pre = [&]() {
    auto og_main = [&](int row, int hd, int hf) -> u32 { return og + (row >> 4) * 20480 + ((hg * 2 + (hd >> 1)) << 10) + (((2 * (hd & 1) + hf) * 16 + (row & 15)) << 4); };
    auto og_tail = [&](int row, int hd) -> u32 { return og + (row >> 4) * 20480 + 16384 + hg * 512 + ((hd * 16 + (row & 15)) << 3); };
    if (br) {
      const int hd = wid & 3, qi = wid < 8 ? 1 - (wid >> 2) : wid >> 2, u = 35 - l32 + 4 * h; const bool vones = (g & 1) && p4;
      pc[0] = L_Q + hd * 6144 + (32 * qi + l32) * 48 + 16 * h;
      pc[1] = hd * 2560 + l32 * 80 + 16 * h;
      pc[2] = h ? L_ONES : hd * 2560 + l32 * 80 + 32;
      pc[3] = vones ? L_ONES : hd * 2560 + (4 * h + q4) * 80 + 40 + 32 * (g & 1) + 8 * p4;
      pc[4] = h ? 0 : 10240; pc[5] = vones ? 0 : 10240;
      pc[6] = L_MASK + (u & 3) * 256 + (u & ~3) * 4;
      pc[7] = og_main(rt * 128 + 32 * qi + l32, hd, h);
      pc[8] = h ? OOB : og_tail(rt * 128 + 32 * qi + l32, hd);
      pc[0] += lds0; pc[1] += lds0; pc[2] += lds0; pc[3] += lds0; pc[6] += lds0;
    } else {
      const int tg = wid & 3, pair = (wid >> 2) & 1, isq = wid >> 3;
      pc[0] = (isq ? L_AQ : L_AK) + (32 * tg + l32) * 176 + pair * 80 + 16 * h;
      pc[1] = L_WTAB + pair * 5120 + lane * 16;
      pc[2] = L_WTAB + pair * 5120 + 2048 + lane * 8;
      if (isq) {
        const int hp = (l32 >> 2) & 1, r = (l32 & 3) + 4 * (l32 >> 3);
        pc[3] = (h == hp) ? L_KVT + (2 * pair + h) * 1680 + r * 80 : L_ZERO;
        pc[4] = (h == hp && r <= 4) ? L_KVT + (2 * pair + h) * 1680 + (16 + r) * 80 : L_ZERO;
        pc[5] = og_main(rt * 128 + 32 * tg + l32, 2 * pair + h, 0);
        pc[6] = og_tail(rt * 128 + 32 * tg + l32, 2 * pair + h);
        pc[7] = 0;
      } else {
        const int hd = 2 * pair + (tg >> 1), dt = tg & 1;
        pc[3] = L_AV + hd * 6144 + (8 * g + q4) * 48 + 32 * dt + 8 * p4;
        pc[4] = L_PK + (2 * pair + h) * 10240 + (32 * tg + l32) * 80;
        pc[5] = L_PK + hd * 10240 + (8 * g + q4) * 80 + 8 * p4;
        pc[6] = (!dt || i16 <= 4) ? W_PART + ((hg * 4 + hd) * 16 + rt) * PARTB + (dt * 160 + (dt ? g * 5 + i16 : lane)) * 16 : OOB;
        pc[7] = g < 2 ? pc[6] : OOB;
      }
      pc[0] += lds0; pc[1] += lds0; pc[2] += lds0; pc[3] += lds0; pc[4] += lds0; if (!isq) pc[5] += lds0;
      pc[8] = pc[0] + 64 - 8 * h;
    }
#pragma unroll
    for (int i = 0; i < 9; i++) PIN(pc[i]);
  };
  if (wid < 4) {
    __builtin_amdgcn_s_setprio(3);
    u32 ep = 0;
    if (wid == 3) ep = gload(ws, W_EPOCH);
    if (wid == 2) *(LAS u32*)(misc + lane * 4) = 0;
    int kt = rot, bi = 0;
    if (wid == 1) {
      const rsrc_t rc = __builtin_amdgcn_make_buffer_rsrc((void*)(pc0 & ~15ull), (short)0, len & ~15u, 0x00020000);
      dma16(rc, idx * 1024 + lane * 16, 0, lds0 + L_DUMMY);
    }
    __builtin_amdgcn_s_barrier();
#pragma nounroll
    for (int s = 1; s < 10; s++) {
      bi = bi == 2 ? 0 : bi + 1; kt = slice(s);
      const u32 buf = lds0 + bi * STB;
      if (s == 2 && br == 0) {
        const rsrc_t rp = __builtin_amdgcn_make_buffer_rsrc((void*)(wphi + hg * 1600), (short)0, 3200, 0x00020000);
        dma16(rp, wid * 1024 + lane * 16, 0, lds0 + L_RAW + wid * 1024);
      }
#pragma unroll
      for (int i = 0; i < 4; i++) ldx(wid * 4 + i, kt, buf);
      if (lane < 60) {
#pragma unroll
        for (int j = 0; j < 8; j++) ldw1(wid * 8 + j, kt, buf);
      }
      waitv<12>();
      lbar();
      if (s == 9) pre();
      if (s == 9 && br == 0) {
        const int pair = wid >> 1, T = wid & 1, rb = g & 1;
        const int fb = 2 * rb + (p4 >> 1);
        const bool okT = (T == 0) || (fb == 0);
        const int f0 = T ? 16 : 4 * fb;
        s16x4 r[5];
#pragma unroll
        for (int u = 0; u < 5; u++) {
          const int cg = (u < 4) ? 4 * (u >> 1) + 2 * h + (u & 1) : 8 + h;
          const int hc = cg >= 5, d0 = 4 * (cg - 5 * hc);
          const bool ok = okT && (hc == (p4 & 1));
          r[u] = trrd(lds + (ok ? L_RAW + (2 * pair + hc) * 800 + (d0 + q4) * 40 + f0 * 2 : L_ZERO));
        }
        lds_ptr tb = lds + L_WTAB + wid * 2560;
        *(LAS bf16x8*)(tb + lane * 16) = cat8(r[0], r[1]);
        *(LAS bf16x8*)(tb + 1024 + lane * 16) = cat8(r[2], r[3]);
        *(LAS s16x4*)(tb + 2048 + lane * 8) = r[4];
      }
      STAMPV(32 + s - 1);
    }
    {
      const rsrc_t rwo = __builtin_amdgcn_make_buffer_rsrc((void*)(br ? woB : woA), (short)0, D_ * D_ * 2, 0x00020000);
      waitv<0>();
      if (wid == 3 && lane == 0) *(LAS u32*)(misc + M_TAG) = ep * 0x9E3779B1u + 0x7F4A7C15u;
      lbar();
      STAMP(15);
#pragma nounroll
      for (int j = 0; j < 5; j++) {
        const int i = wid * 5 + j;
        const u32 o_ = i < 16 ? og + (rt * 8 + (i >> 1)) * 20480 + hg * 2048 + (i & 1) * 1024 + lane * 16
                              : og + (rt * 8 + 2 * (i - 16) + h) * 20480 + 16384 + hg * 512 + l32 * 16;
        __builtin_amdgcn_raw_buffer_store_b128(u32x4{~0u, ~0u, ~0u, ~0u}, rws, o_, 0, 16);
      }
      { const int ln = (idx & 15) * 200 + wid * 50 + (lane < 50 ? lane : 49); dma16(rwo, (ln / 5) * 1280 + (hg >> 2) * 640 + (ln % 5) * 128, 0, lds0 + L_DUMMY); }
#define NPF 1
    }
    if (br) {
      waitv<NPF>();
      if (lane == 0 && lds_add_ret(misc + M_C4) == 3) gstore(ws, W_STARTED + ((br * 16 + rt) * 8 + hg) * 4, lds_word(misc + M_TAG));
    }
  } else {
    const int cw = wid - 4, mi = cw & 3, nj = cw < 8 ? (cw >> 2) + 1 : 0;
    if (cw < 4) {
#pragma unroll
      for (int i = 0; i < 4; i++) ldx(cw * 4 + i, rot, lds0);
    } else if (lane < 60) {
#pragma unroll
      for (int j = 0; j < 4; j++) ldw1((cw - 4) * 4 + j, rot, lds0);
    }
    __builtin_amdgcn_s_barrier();
    if (cw < 8) {
      const int c = cw & 3, x = lane + c - 35;
      *(LAS float*)(lds + L_MASK + cw * 256 + lane * 4) = ((x > 0) != (cw >= 4)) ? -1e30f : 0.f;
    } else if (cw == 8 && lane < 4) *(LAS u32x2*)(lds + L_ONES + lane * 640) = u32x2{0x3F803F80u, 0x3F803F80u};
    pre();
    const int row = mi * 32 + i16;
    const u32 xa0 = row * 128 + ((g ^ ((row >> 1) & 7)) << 4), xa1 = xa0 ^ 64;
    const u32 wa = XB + (16 * (g >> 1) + 4 * (g & 1) + q4) * 480 + 8 * p4 + nj * 160;
    int base, HS, RS;
    if (br) {
      if (nj == 0) { base = L_Q; HS = 6144; RS = 48; }
      else { base = L_OWN + (nj - 1) * 40 + mi * 7680; HS = 2560; RS = 80; }
    } else {
      if (nj == 2) { base = L_AV; HS = 6144; RS = 48; }
      else { base = nj ? L_AK : L_AQ; HS = 40; RS = 176; }
    }
    int ea[5];
#pragma unroll
    for (int n = 0; n < 5; n++) { const int c4 = 4 * n + g, hd = (c4 * 13) >> 6, d4 = c4 - 5 * hd; ea[n] = lds0 + base + hd * HS + row * RS + d4 * 8; PIN(ea[n]); }
    f32x4 acc[2][5] = {};
    auto rd = [&](Frags& f, lds_ptr bb, int ks, int j) {
      if (j < 2) f.x[j] = ld128(bb + (ks ? xa1 : xa0) + j * 2048);
      else { int n = (j - 2) >> 1; if (j & 1) f.wh[n] = trrd(bb + wa + ks * 15360 + 3840 + n * 32); else f.wl[n] = trrd(bb + wa + ks * 15360 + n * 32); }
    };
#define MM(F, i) acc[(i) & 1][(i) >> 1] = MFMA16(cat8(F.wl[(i) >> 1], F.wh[(i) >> 1]), F.x[(i) & 1], acc[(i) & 1][(i) >> 1])
    Frags F0, F1;
    int boff = 0;
    __builtin_amdgcn_s_setprio(3);
    waitv<0>();
    lbar();
    STAMP(14);
#pragma unroll
    for (int j = 0; j < 12; j++) rd(F0, lds, 0, j);
#pragma unroll
    for (int t = 0; t < 10; t++) {
      SBAR();
#pragma unroll
      for (int i = 0; i < 10; i++) { MM(F0, i); if (i < 6) { rd(F1, lds + boff, 1, 2 * i); rd(F1, lds + boff, 1, 2 * i + 1); } SBAR(); }
#pragma unroll
      for (int i = 0; i < 10 - HB; i++) MM(F1, i);
      SBAR();
      boff = ((t + 1) % 3) * STB; asm volatile("" : "+s"(boff));
      if (t < 9) {
        __builtin_amdgcn_s_waitcnt(0xC07F);
        asm volatile("s_barrier" : "+v"(acc[0][0]), "+v"(acc[1][0]), "+v"(acc[0][1]), "+v"(acc[1][1]), "+v"(acc[0][2]), "+v"(acc[1][2]), "+v"(acc[0][3]), "+v"(acc[1][3]), "+v"(acc[0][4]), "+v"(acc[1][4]) :: "memory");
      } else {
        lds_inc(misc + M_C8, lane);
        asm volatile("" : "+v"(acc[0][0]), "+v"(acc[1][0]), "+v"(acc[0][1]), "+v"(acc[1][1]), "+v"(acc[0][2]), "+v"(acc[1][2]), "+v"(acc[0][3]), "+v"(acc[1][3]), "+v"(acc[0][4]), "+v"(acc[1][4]));
      }
      asm volatile("" ::: "memory");
#pragma unroll
      for (int i = 10 - HB; i < 10; i++) {
#pragma unroll
        for (int j = (i - 10 + HB) * RPM; j < (i - 9 + HB) * RPM && j < 12; j++) if (t < 9) rd(F0, lds + boff, 0, j);
        MM(F1, i); SBAR();
      }
    }
    STAMP(1);
    relane();
    u32x2 pk[2][5];
    if (br && nj == 0) {
#pragma unroll
      for (int i = 0; i < 10; i++) { const f32x4 v = acc[i & 1][i >> 1] * (0.22360679774997896f * 1.4426950408889634f); pk[i & 1][i >> 1] = u32x2{cvtpk(v[0], v[1]), cvtpk(v[2], v[3])}; }
    } else {
#pragma unroll
      for (int i = 0; i < 10; i++) { const f32x4 v = acc[i & 1][i >> 1]; pk[i & 1][i >> 1] = u32x2{cvtpk(v[0], v[1]), cvtpk(v[2], v[3])}; asm volatile("" ::: "memory"); }
    }
    int eb[5];
#pragma unroll
    for (int n = 0; n < 5; n++) { eb[n] = ea[n] + 16 * RS; PIN(eb[n]); }
#pragma unroll
    for (int n = 0; n < 5; n++) { *(LAS u32x2*)LP(ea[n]) = pk[0][n]; *(LAS u32x2*)LP(eb[n]) = pk[1][n]; }
    if (br == 0 && nj == 2) {
#pragma unroll
      for (int m = 0; m < 2; m++) *(LAS u32x2*)(lds + L_AV + g * 6144 + (mi * 32 + i16 + 16 * m) * 48 + 40) = u32x2{0x3F803F80u, 0x3F803F80u};
    }
    lds_inc(misc + M_WR + nj * 16 + mi * 4, lane);
    if (nj == 2) lds_inc(misc + M_WV, lane);
    lds_inc(misc + M_C9, lane);
    if (br && nj == 1) {
      lds_wait_ge(misc + M_WR + 32 + mi * 4, 1u);
      if (mi < 2) __builtin_amdgcn_s_sleep(6);
      const u32 dst = W_KVG + ((rt * 8 + hg) * 4 + mi) * 10240 + lane * 16;
      u32x4 v[10];
#pragma unroll
      for (int i = 0; i < 10; i++) v[i] = *(LAS u32x4*)(lds + L_OWN + mi * 10240 + i * 1024 + lane * 16);
      SBAR();
#pragma unroll
      for (int i = 0; i < 10; i++) __builtin_amdgcn_raw_buffer_store_b128(v[i], rws, dst, i * 1024, 16);
      waitv<0>();
      if (lane == 0) gstore(ws, W_KVFLAG + (rt * 8 + hg) * 128 + mi * 4, lds_word(misc + M_TAG));
      lds_inc(misc + (mi >> 1 ? M_P1 : M_P0), lane);
      STAMP(4);
    } else if (br) __builtin_amdgcn_s_setprio(0);
  }
  relane();
  f32x16 o = {};
  PIN(o);
  if (br) lds_wait_ge(misc + M_C9, 12u);
  STAMP(2);
  const u32 tag = RFL(lds_word(misc + M_TAG));
  const int l_wo = br ? LB_WO : LA_WO;
  auto started = [&]() { if (lane == 0) gstore(ws, W_STARTED + ((br * 16 + rt) * 8 + hg) * 4, tag); };
  auto guard = [&]() { poll_tag(ws, W_STARTED + ((br * 16 + rt) * 8 + (lane & 7)) * 4, tag, false); lds_inc(misc + M_C6, lane); };
  const rsrc_t rwo = __builtin_amdgcn_make_buffer_rsrc((void*)(br ? woB : woA), (short)0, D_ * D_ * 2, 0x00020000);
  u32 wvm[2] = {}, wvt[2] = {};
  const int wj = wid & 3;
  auto wo_init = [&]() {
#pragma unroll
    for (int u = 0; u < 2; u++) {
      int c = (u ? 4 : wj) * 64 + lane; PIN(c);
      const int p = c / 10, ch = c - p * 10;
      const int gg = ((p >> 3) & 2) | ((p >> 2) & 1), tt = (p >> 3) & 1, e = p & 3;
      wvm[u] = ((gg >> 1) * 20 + 4 * (gg & 1) + 8 * tt + e) * 1280 + hg * 160 + ch * 16; wvt[u] = (tt * 80 + gg * 20 + 16 + e) * 1280 + hg * 160 + ch * 16;
    }
  };
  auto wo_dma = [&](int k0, int k1) {
    if (k1 == 20) {
#pragma unroll
      for (int ks = 16; ks < 20; ks++) dma16(rwo, wvt[0], (ks - 16) * 160 * 1280, lds0 + l_wo + ks * 5120 + wj * 1024);
      dma16(rwo, wvt[1], wj * 160 * 1280, lds0 + l_wo + (16 + wj) * 5120 + 4096);
      k1 = 16;
    }
#pragma nounroll
    for (int ks = k0; ks < k1; ks++) {
      dma16(rwo, wvm[0], ks * 40 * 1280, lds0 + l_wo + ks * 5120 + wj * 1024);
      if ((ks & 3) == wj) dma16(rwo, wvm[1], ks * 40 * 1280, lds0 + l_wo + ks * 5120 + 4096);
    }
  };
  const bool wow = wid >= 12;
  const u32 ofl = W_OFLAG + (br * 16 + rt) * 128;
  auto published = [&](int rg) {
    waitv<0>();
    lds_inc(misc + M_D + rg * 4, lane);
    if (lane == 0 && lds_add_ret(misc + M_C2) == (br ? 15 : 7)) gstore(ws, ofl + hg * 4, tag);
  };

  if (br) {
    const int hd = wid & 3, qi = wid < 8 ? 1 - (wid >> 2) : wid >> 2;
    const u32 kvf = W_KVFLAG + (rt * 8 + hg) * 128;
    const bf16x8 qa = ld128(LP(pc[0]));
    s16x4 qb = ld64(LP(pc[0]) + 32);
    if (h) qb = s16x4{0, 0, 0, 0};
    auto ldmask = [&](int pol) -> f32x16 {
      f32x4 t[4];
#pragma unroll
      for (int j = 0; j < 4; j++) t[j] = *(LAS f32x4*)(LP(pc[6]) + pol * 1024 + j * 32);
      return __builtin_shufflevector(__builtin_shufflevector(t[0], t[1], 0, 1, 2, 3, 4, 5, 6, 7), __builtin_shufflevector(t[2], t[3], 0, 1, 2, 3, 4, 5, 6, 7),
                                     0, 1, 2, 3, 4, 5, 6, 7, 8, 9, 10, 11, 12, 13, 14, 15);
    };
    auto pk4 = [&](const f32x16& s, int o_) -> bf16x8 {
      u32x4 p;
#pragma unroll
      for (int j = 0; j < 4; j++) p[j] = cvtpk(ex2(s[o_ + 2 * j]), ex2(s[o_ + 2 * j + 1]));
      return __builtin_bit_cast(bf16x8, p);
    };
    lds_ptr ka_p, kb_p, vp;
    auto seek = [&](int row) { ka_p = LP(pc[1]) + row * 10240; kb_p = LP(pc[2]) + row * pc[4]; vp = LP(pc[3]) + row * pc[5]; };
    f32x16 s, s2; s16x4 c;
    auto step = [&](f32x16& sc, f32x16& sn) {
      ka_p -= 10240; kb_p -= pc[4];
      const bf16x8 ka = ld128(ka_p); const s16x4 kb = ld64(kb_p);
      const bf16x8 v0 = cat8(trrd(vp), trrd(vp + 640)), v1 = cat8(trrd(vp + 1280), trrd(vp + 1920));
      vp -= pc[5];
      SBAR();
      float e[8]; u32x4 p1;
      bf16x8 p0 = pk4(sc, 0); PIN(p0);
      SBAR(); sn = MFMA32(ka, qa, f32x16{}); SBAR();
#pragma unroll
      for (int j = 0; j < 4; j++) e[j] = ex2(sc[8 + j]);
      SBAR(); sn = MFMA32K8(kb, c, sn); SBAR();
#pragma unroll
      for (int j = 4; j < 8; j++) e[j] = ex2(sc[8 + j]);
      SBAR(); o = MFMA32(v0, p0, o); SBAR();
#pragma unroll
      for (int j = 0; j < 4; j++) p1[j] = cvtpk(e[2 * j], e[2 * j + 1]);
      SBAR(); o = MFMA32(v1, __builtin_bit_cast(bf16x8, p1), o); SBAR();
    };
    auto last = [&](f32x16& sc) {
      const bf16x8 v0 = cat8(trrd(vp), trrd(vp + 640)), v1 = cat8(trrd(vp + 1280), trrd(vp + 1920));
      o = MFMA32(v0, pk4(sc, 0), o);
      o = MFMA32(v1, pk4(sc, 8), o);
    };
    seek(L_OWN / 10240 + qi);
    {
      const s16x4 kb = ld64(kb_p);
      s = MFMA32K8(kb, qb, MFMA32(ld128(ka_p), qa, ldmask(0)));
      float mm = fmaxf(s[0], s[1]);
#pragma unroll
      for (int r = 2; r < 16; r += 2) mm = fmaxf(fmaxf(mm, s[r]), s[r + 1]);
      auto rr = __builtin_amdgcn_permlane32_swap(__float_as_uint(mm), __float_as_uint(mm), false, false);
      const u32 nm = cvtpk(-fmaxf(__uint_as_float(rr[0]), __uint_as_float(rr[1])), 0.f);
      const u32x2 qw = __builtin_bit_cast(u32x2, qb);
      c = __builtin_bit_cast(s16x4, u32x2{h ? nm : qw.x, qw.y});
      s = MFMA32K8(kb, __builtin_bit_cast(s16x4, u32x2{h ? nm : 0u, 0u}), s);
    }
    if (qi >= 2) { step(s, s2); step(s2, s); }
    if (qi & 1) { step(s, s2); last(s2); } else last(s);
    lds_inc(misc + M_C1, lane);
    STAMP(3);
    if (wid < 4) {
      const int dd = 2 - (wid >> 1);
#pragma nounroll
      for (int hf = 1; hf >= 0; --hf) {
        const int tl = 2 * hf + (wid & 1);
        lds_wait_ge(misc + (hf ? M_P1 : M_P0), 2u);
        if (rt >= dd) poll_tag(ws, kvf - dd * 1024 + tl * 4, tag, false);
        if (hf) { STAMP(11); } else { STAMP(5); lds_inc(misc + M_C0, lane); }
        if (rt >= dd) {
          const u32 src = W_KVG + (((rt - dd) * 8 + hg) * 4 + tl) * 10240;
#pragma unroll
          for (int i = 0; i < 10; i++) dma16c(rws, lane * 16, src + i * 1024, lds0 + L_HALO + hf * 40960 + wid * 10240 + i * 1024);
        }
      }
    }
    if (wow) wo_init();
    lds_wait_ge(misc + M_C0, 4u);
    int wk = 20;
    if (rt > 0) {
      int n1 = 2, nn = 4, km = 0;
      if (rt >= 2) { n1 = qi < 3 ? 4 : 3; nn = 8 - qi; km = qi >= 2 ? n1 - 1 : nn - 1; }
      const u32 ev1 = (1u << n1) | (wid < 4 ? 4u : 0u) | (wow ? ~0u : 0u);
      const u32 ev2 = (1u << n1) | (1u << km);
      if (wow) __builtin_amdgcn_s_setprio(3); else __builtin_amdgcn_s_setprio(2);
      seek(7);
      s = MFMA32K8(ld64(kb_p), c, MFMA32(ld128(ka_p), qa, f32x16{}));
      auto before = [&](int k) {
        if (k == 2 && wid < 4) { waitv<0>(); lds_inc(misc + M_C7, lane); }
        if (k == n1) {
          ka_p -= (4 - n1) * 10240; kb_p -= (4 - n1) * pc[4];
          lds_wait_ge(misc + M_C7, 4u);
          if (wid == 10) guard();
        }
        if (wow && wk > 0) {
          const u32 c1 = RFL(lds_word(misc + M_C1)), c7 = RFL(lds_word(misc + M_C7)), c3 = RFL(lds_word(misc + M_C3));
          const int lim = c3 >= 16 ? 0 : (c1 >= 16 && c7 >= 4) ? 5 : 20;
          const int k0 = wk - 8 > lim ? wk - 8 : lim;
          if (k0 < wk) { wo_dma(k0, wk); wk = k0; }
        }
      };
      auto after = [&](int k, f32x16& sn) {
        if (k == km) sn += ldmask(1);
        if (k == n1) { vp -= (4 - n1) * pc[5]; lds_inc(misc + M_C3, lane); if (!wow) __builtin_amdgcn_s_setprio(1); }
      };
#pragma nounroll
      for (int k = 1;; k += 2) {
        if (__builtin_expect((ev1 >> k) & 1, 0)) before(k);
        step(s, s2);
        if (__builtin_expect((ev2 >> k) & 1, 0)) after(k, s2);
        if (__builtin_expect(k + 1 == nn, 0)) { s = s2; break; }
        if (__builtin_expect((ev1 >> (k + 1)) & 1, 0)) before(k + 1);
        step(s2, s);
        if (__builtin_expect((ev2 >> (k + 1)) & 1, 0)) after(k + 1, s);
        if (__builtin_expect(k + 2 == nn, 0)) break;
      }
      last(s);
    } else {
      if (wid == 10) guard();
    }
    float l = h ? o[8] : o[12];
    if (__builtin_expect(__any(!(l < 1.8e19f)), 0)) {
      const int dd = lane % 3;
      poll_tag(ws, kvf - (rt - dd < 0 ? 0 : dd) * 1024 + 4 * (lane & 3), tag, false);
      float q[20], a[20], mx = -3e38f; l = 0.f;
      const u32x4 qw = __builtin_bit_cast(u32x4, qa); const u32x2 qv = __builtin_bit_cast(u32x2, qb);
#pragma unroll
      for (int j = 0; j < 10; j++) {
        const u32 w = j < 8 ? __shfl(qw[j & 3], l32 + 32 * (j >> 2)) : __shfl(qv[j & 1], l32);
        q[2 * j] = blo(w); q[2 * j + 1] = bhi(w); a[2 * j] = 0.f; a[2 * j + 1] = 0.f;
      }
      const int row = rt * 128 + 32 * qi + l32;
#pragma nounroll
      for (int t = 0; t < 128; ++t) {
        const int j = row - 2 * t - h;
        if (j >= 0) {
          const u32 src = W_KVG + (((j >> 7) * 8 + hg) * 4 + ((j >> 5) & 3)) * 10240 + hd * 2560 + (j & 31) * 80;
          u32 w[20];
#pragma unroll
          for (int i = 0; i < 5; i++) { const u32x4 v = __builtin_amdgcn_raw_buffer_load_b128(rws, src, i * 16, 16); w[4 * i] = v.x; w[4 * i + 1] = v.y; w[4 * i + 2] = v.z; w[4 * i + 3] = v.w; }
          float sc = 0.f;
#pragma unroll
          for (int i = 0; i < 10; i++) sc += q[2 * i] * blo(w[i]) + q[2 * i + 1] * bhi(w[i]);
          const float mn = fmaxf(mx, sc), f = ex2(mx - mn), p = ex2(sc - mn);
          mx = mn; l = l * f + p;
#pragma unroll
          for (int i = 0; i < 10; i++) { a[2 * i] = a[2 * i] * f + p * blo(w[10 + i]); a[2 * i + 1] = a[2 * i + 1] * f + p * bhi(w[10 + i]); }
        }
      }
      const float mo = __shfl_xor(mx, 32), mn = fmaxf(mx, mo), f = ex2(mx - mn);
      l *= f; l += __shfl_xor(l, 32);
#pragma unroll
      for (int i = 0; i < 20; i++) { a[i] *= f; a[i] += __shfl_xor(a[i], 32); }
#pragma unroll
      for (int r = 0; r < 8; r++) o[r] = h ? a[(r & 3) + 8 * (r >> 2) + 4] : a[(r & 3) + 8 * (r >> 2)];
#pragma unroll
      for (int r = 8; r < 12; r++) o[r] = a[8 + r];
    }
    const float rl = __builtin_amdgcn_rcpf(l);
    const u32x4 w0 = {cvtpk(o[0] * rl, o[1] * rl), cvtpk(o[2] * rl, o[3] * rl), cvtpk(o[4] * rl, o[5] * rl), cvtpk(o[6] * rl, o[7] * rl)};
    const u32x2 w1 = {cvtpk(o[8] * rl, o[9] * rl), cvtpk(o[10] * rl, o[11] * rl)};
    __builtin_amdgcn_raw_buffer_store_b128(w0, rws, pc[7], 0, 16);
    __builtin_amdgcn_raw_buffer_store_b64(w1, rws, pc[8], 0, 16);
    STAMP(6);
    lds_inc(misc + M_AD, lane);
    if (wow && wk > 5) { lds_wait_ge(misc + M_C1, 16u); wo_dma(5, wk); wk = 5; }
    published(qi);
    if (wow) {
      lds_inc(misc + M_C6, lane);
      if (wk > 0) { if (rt) lds_wait_ge(misc + M_C3, 16u); wo_dma(0, wk); waitv<0>(); }
      lds_inc(misc + M_C6B, lane);
    }
    __builtin_amdgcn_s_setprio(0);
  } else {
    const int tg = wid & 3, pair = (wid >> 2) & 1, isq = wid >> 3;
    u32 phi[20];
    {
      lds_wait_ge(misc + M_WR + (isq ? 0 : 16) + tg * 4, 1u);
      const bf16x8 b0 = ld128(LP(pc[0])), a00 = ld128(LP(pc[1])), a10 = ld128(LP(pc[1]) + 2560);
      const bf16x8 b1 = ld128(LP(pc[0]) + 32), a01 = ld128(LP(pc[1]) + 1024), a11 = ld128(LP(pc[1]) + 2560 + 1024);
      const s16x4 b2 = ld64(LP(pc[8])), a02 = ld64(LP(pc[2])), a12 = ld64(LP(pc[2]) + 2560);
      lds_inc(misc + M_C5, lane);
      if (wow) { wo_init(); lds_wait_ge(misc + M_C5, 16u); wo_dma(6, 20); waitv<0>(); lds_inc(misc + M_C6, lane); }
      if (isq) __builtin_amdgcn_s_setprio(0); else __builtin_amdgcn_s_setprio(2);
      f32x16 t0 = MFMA32(a00, b0, f32x16{}), t1 = MFMA32(a10, b0, f32x16{});
      t0 = MFMA32(a01, b1, t0); t1 = MFMA32(a11, b1, t1);
      t0 = MFMA32K8(a02, b2, t0); t1 = MFMA32K8(a12, b2, t1);
      float p[20];
#pragma unroll
      for (int j = 0; j < 10; j++) {
        const u32 w = j < 8 ? cvtpk(t0[2 * j], t0[2 * j + 1]) : cvtpk(t1[2 * j - 16], t1[2 * j - 15]);
        p[2 * j] = blo(w); p[2 * j + 1] = bhi(w);
      }
      float mx = fmaxf(p[0], p[1]), mn = fminf(p[0], p[1]);
#pragma unroll
      for (int j = 2; j < 20; j += 2) { mx = fmaxf(fmaxf(mx, p[j]), p[j + 1]); mn = fminf(fminf(mn, p[j]), p[j + 1]); }
      const float L2E = 1.4426950408889634f;
      const f32x2 sl = {L2E, -L2E}, of = {-mx * L2E, mn * L2E};
      f32x2 e[20], sa = {0.f, 0.f}, sb = {0.f, 0.f};
#pragma unroll
      for (int j = 0; j < 20; j++) {
        const f32x2 a = __builtin_elementwise_fma(f32x2{p[j], p[j]}, sl, of);
        e[j] = f32x2{ex2(a[0]), ex2(a[1])};
        if (j & 1) sb += e[j]; else sa += e[j];
      }
      sa += sb;
      const f32x2 rr = {__builtin_amdgcn_rcpf(sa[0]), __builtin_amdgcn_rcpf(sa[1])};
#pragma unroll
      for (int j = 0; j < 20; j++) phi[j] = cvtpk2(e[j] * rr);
    }
    if (!isq) {
      lds_wait_ge(misc + M_WV, 4u);
      bf16x8 vb[4];
#pragma unroll
      for (int ks = 0; ks < 4; ks++) vb[ks] = cat8(trrd(LP(pc[3]) + ks * 32 * 48), trrd(LP(pc[3]) + ks * 32 * 48 + 4 * 48));
      lds_wait_ge(misc + M_C8, 12u);
#pragma unroll
      for (int j = 0; j < 5; j++) *(LAS u32x4*)(LP(pc[4]) + 16 * j) = u32x4{phi[4 * j], phi[4 * j + 1], phi[4 * j + 2], phi[4 * j + 3]};
      lds_inc(misc + M_C0 + pair * 4, lane);
      STAMP(3);
      lds_wait_ge(misc + M_C0 + pair * 4, 4u);
      f32x4 kv[3] = {};
#pragma unroll
      for (int ks = 0; ks < 4; ks++)
#pragma unroll
        for (int T = 0; T < 3; T++) kv[T] = MFMA16(cat8(trrd(LP(pc[5]) + ks * 2560 + T * 32), trrd(LP(pc[5]) + ks * 2560 + 320 + T * 32)), vb[ks], kv[T]);
#pragma unroll
      for (int T = 0; T < 3; T++)
        __builtin_amdgcn_raw_buffer_store_b128(u32x4{cvtpk(kv[T][0], kv[T][1]), tag, cvtpk(kv[T][2], kv[T][3]), tag}, rws, pc[T < 2 ? 6 : 7], T * ((tg & 1) ? 320 : 1024), 16);
      waitv<3>();
      lds_inc(misc + M_C1, lane);
      STAMP(4);
      if (wid >= 4) { wo_init(); lds_wait_ge(misc + M_C1, 8u); wo_dma(0, 6); waitv<0>(); lds_inc(misc + M_C6, lane); }
      if (wid == 0) {
        const int sl = (rt & 3) * 53 + lane; const bool act = lane < 53 && sl < 210;
        const u32 src = act ? W_PART + (hg * 4 + (rt >> 2)) * 16 * PARTB + sl * 16 : OOB;
        float s0 = 0.f, s1 = 0.f, s2 = 0.f, s3 = 0.f;
        lds_wait_ge(misc + M_C6, 8u);
#pragma nounroll
        for (u32 s = 0; s < SPIN_CAP; ++s) {
          bool ok = true; s0 = 0.f; s1 = 0.f; s2 = 0.f; s3 = 0.f;
          u32x4 v[16];
#pragma unroll
          for (int r = 0; r < 16; r++) v[r] = __builtin_amdgcn_raw_buffer_load_b128(rws, src, r * PARTB, 16);
#pragma unroll
          for (int r = 0; r < 16; r++) { ok &= (v[r].y == tag) & (v[r].w == tag); s0 += blo(v[r].x); s1 += bhi(v[r].x); s2 += blo(v[r].z); s3 += bhi(v[r].z); }
          if (__all(ok || !act)) break;
          asm volatile("" ::: "memory");
        }
        __builtin_amdgcn_raw_buffer_store_b128(u32x4{cvtpk(s0, s1), tag, cvtpk(s2, s3), tag}, rws, act ? W_SLICE + (hg * 16 + rt) * 1024 + lane * 16 : OOB, 0, 16);
        waitv<0>();
        lds_inc(misc + M_C4, lane);
        STAMP(5);
      } else if (wid == 1) { lds_wait_ge(misc + M_C1, 8u); started(); lds_wait_ge(misc + M_C4, 1u); guard(); }
      else if (wid >= 4) {
        const int hh = wid - 4;
        const u32 src = W_SLICE + (hg * 16 + hh * 4) * 1024 + lane * 16;
        int da[4];
#pragma unroll
        for (int j = 0; j < 4; j++) {
          const int sl = j * 53 + lane; int d, ph;
          if (sl < 160) { d = sl & 15; ph = (sl >> 4) * 4; }
          else { const int s2 = sl - 160, gg = s2 / 5; d = 16 + s2 - 5 * gg; ph = 4 * gg; }
          da[j] = L_KVT + hh * 1680 + d * 80 + ph * 2;
        }
        lds_wait_ge(misc + M_C5, 16u);
        lds_wait_ge(misc + M_C4, 1u);
        u32x4 v[4];
#pragma nounroll
        for (u32 s = 0; s < SPIN_CAP; ++s) {
          bool ok = true;
#pragma unroll
          for (int r = 0; r < 4; r++) v[r] = __builtin_amdgcn_raw_buffer_load_b128(rws, src, r * 1024, 16);
#pragma unroll
          for (int r = 0; r < 4; r++) ok &= ((v[r].y == tag) & (v[r].w == tag)) | (lane >= (r < 3 ? 53 : 51));
          if (__all(ok)) break;
          asm volatile("" ::: "memory");
        }
#pragma unroll
        for (int r = 0; r < 4; r++) if (lane < (r < 3 ? 53 : 51)) *(LAS u32x2*)(lds + da[r]) = u32x2{v[r].x, v[r].z};
        lds_inc(misc + M_C3, lane);
        STAMP(11);
      }
      __builtin_amdgcn_s_setprio(0);
    } else {
      lds_ptr a0 = LP(pc[3]), a1 = LP(pc[4]); const u32 om = pc[5], ot = pc[6];
      STAMP(12);
      lds_wait_ge(misc + M_C3, 4u);
      f32x16 t0 = {}, t1 = {};
#pragma unroll
      for (int j = 0; j < 5; j++) {
        const bf16x8 bq = __builtin_bit_cast(bf16x8, u32x4{phi[4 * j], phi[4 * j + 1], phi[4 * j + 2], phi[4 * j + 3]});
        t1 = MFMA32(ld128(a1 + 16 * j), bq, t1); t0 = MFMA32(ld128(a0 + 16 * j), bq, t0);
      }
      const float rl = __builtin_amdgcn_rcpf(t1[4] + 1e-6f);
      const u32x4 w0 = {cvtpk(t0[0] * rl, t0[1] * rl), cvtpk(t0[2] * rl, t0[3] * rl), cvtpk(t0[8] * rl, t0[9] * rl), cvtpk(t0[10] * rl, t0[11] * rl)};
      const u32x4 w1 = {cvtpk(t0[4] * rl, t0[5] * rl), cvtpk(t0[6] * rl, t0[7] * rl), cvtpk(t0[12] * rl, t0[13] * rl), cvtpk(t0[14] * rl, t0[15] * rl)};
      const u32x2 w2 = {cvtpk(t1[0] * rl, t1[1] * rl), cvtpk(t1[2] * rl, t1[3] * rl)};
      __builtin_amdgcn_raw_buffer_store_b128(w0, rws, om, 0, 16);
      __builtin_amdgcn_raw_buffer_store_b128(w1, rws, om, 256, 16);
      __builtin_amdgcn_raw_buffer_store_b64(w2, rws, ot, 0, 16);
      STAMP(6);
      published(tg);
    }
  }
  STAMP(7);
  relane();
  if (wid >= 8) {
    if (b == 0 && wid == 15) {
#pragma nounroll
      for (u32 s = 0; s < SPIN_CAP; ++s) {
        const u32x4 v = __builtin_amdgcn_raw_buffer_load_b128(rws, W_STARTED + lane * 16, 0, 16);
        if (__all(v.x == tag && v.y == tag && v.z == tag && v.w == tag)) break;
        __builtin_amdgcn_s_sleep(2);
      }
      if (lane == 0) gstore(ws, W_EPOCH, tag);
    }
  } else {
    const int rg = br ? (0x2013 >> (4 * (wid >> 1))) & 3 : wid >> 1, kh = wid & 1;
    const u32 obm = og + (rt * 8 + rg * 2) * 20480 + kh * 8192 + lane * 16, obt = og + (rt * 8 + rg * 2) * 20480 + kh * 2048 + lane * 8;
    const u32 mo[2] = {(u32)kh * 20480u, (u32)(1 - kh) * 20480u};
    u32x4 of[10][2];
    auto ldo = [&](int s) {
#pragma unroll
      for (int m = 0; m < 2; m++) {
        if (s < 8) of[s][m] = __builtin_amdgcn_raw_buffer_load_b128(rws, obm, mo[m] + s * 1024, 16);
        else {
          const u32x2 a = __builtin_amdgcn_raw_buffer_load_b64(rws, obt, mo[m] + (s + 8) * 1024, 16);
          const u32x2 c = __builtin_amdgcn_raw_buffer_load_b64(rws, obt, mo[m] + (s + 8) * 1024 + 512, 16);
          of[s][m] = u32x4{a.x, a.y, c.x, c.y};
        }
      }
    };
    u32 wm = lds0 + l_wo + (16 * (g >> 1) + 4 * (g & 1) + q4) * 160 + 8 * p4, wt = wm + (16 + 2 * kh) * 5120; wm += kh * 8 * 5120;
    PIN(wm); PIN(wt);
    bf16x8 wf[WFN];
    auto ldw = [&](int i) { const int s = 9 - i / 5, n = i % 5; lds_ptr p = (s < 8 ? LP(wm) + s * 5120 : LP(wt) + (s - 8) * 5120) + n * 32; wf[i % WFN] = cat8(trrd(p), trrd(p + 1280)); };
    f32x4 ac2[2][5];
    constexpr int PF = P3PF;
    lds_wait_ge(misc + M_C6, br ? 5u : 9u);
    lds_wait_ge(misc + M_D + rg * 4, br ? 4u : 2u);
    STAMP(8);
#pragma nounroll
    for (int pass = 0;; ++pass) {
      if (pass) poll_tag(ws, ofl + (lane & 7) * 4, tag, false);
#pragma unroll
      for (int i = 0; i < WFN; i++) ldw(i);
#pragma nounroll
      for (int tr = 0; tr < 64; ++tr) {
#pragma unroll
        for (int s = 9; s > 5; s--) ldo(s);
        asm volatile("" ::: "memory");
        u32 mx = 0;
#pragma unroll
        for (int s = 8; s < 10; s++)
#pragma unroll
          for (int m = 0; m < 2; m++) mx = max(max(mx, max(of[s][m].x, of[s][m].y)), max(of[s][m].z, of[s][m].w));
        if (pass || !__any(mx == ~0u)) break;
      }
      STAMP(9);
#pragma unroll
      for (int s = 5; s > 9 - PF; s--) ldo(s);
#pragma unroll
      for (int n = 0; n < 5; n++) { ac2[0][n] = f32x4{}; ac2[1][n] = f32x4{}; }
#pragma unroll
      for (int s = 9; s >= 0; s--) {
        if (s == 6) lds_wait_ge(misc + M_C6B, br ? 4u : 0u);
        SBAR();
#pragma unroll
        for (int n = 0; n < 5; n++) {
          const int i = (9 - s) * 5 + n;
          ac2[0][n] = MFMA16(wf[i % WFN], __builtin_bit_cast(bf16x8, of[s][0]), ac2[0][n]);
          ac2[1][n] = MFMA16(wf[i % WFN], __builtin_bit_cast(bf16x8, of[s][1]), ac2[1][n]);
          if (i + WFN < 50) ldw(i + WFN);
          SBAR();
        }
        if (s - PF >= 0) ldo(s - PF);
      }
      const float chk = ac2[0][0][0] + ac2[1][0][0];
      if (pass || !__any(chk != chk)) break;
    }
    STAMP(13);
    relane();
    if (br) lds_wait_ge(misc + M_AD, 16u);
    lds_ptr red = lds + L_RED + lane * 16;
#pragma unroll
    for (int n = 0; n < 5; n++) *(LAS f32x4*)(red + (wid ^ 1) * 5120 + n * 1024) = ac2[1][n];
    lds_inc(misc + M_X + (wid ^ 1) * 4, lane);
    const int r16 = rt * 8 + rg * 2 + kh;
    lds_wait_ge(misc + M_X + wid * 4, 1u);
    f32x4 acc[5];
#pragma unroll
    for (int n = 0; n < 5; n++) acc[n] = *(LAS f32x4*)(red + wid * 5120 + n * 1024);
    SBAR();
#pragma unroll
    for (int n = 0; n < 5; n++) acc[n] += ac2[0][n];
    const rsrc_t ro = __builtin_amdgcn_make_buffer_rsrc((void*)out, (short)0, S_ * 1280 * 2, 0x00020000);
    const u32 obase = (r16 * 16 + i16) * 2560 + br * 1280 + hg * 160;
#pragma unroll
    for (int n = 0; n < 4; n += 2) {
      const auto x = __builtin_amdgcn_permlane16_swap(cvtpk(acc[n][0], acc[n][1]), cvtpk(acc[n + 1][0], acc[n + 1][1]), false, false);
      const auto y = __builtin_amdgcn_permlane16_swap(cvtpk(acc[n][2], acc[n][3]), cvtpk(acc[n + 1][2], acc[n + 1][3]), false, false);
      __builtin_amdgcn_raw_buffer_store_b128(u32x4{x[0], y[0], x[1], y[1]}, ro, obase + (g & 1) * 32 + (g >> 1) * 16, n * 32, 0);
    }
    __builtin_amdgcn_raw_buffer_store_b64(u32x2{cvtpk(acc[4][0], acc[4][1]), cvtpk(acc[4][2], acc[4][3])}, ro, obase + g * 8, 128, 0);
  }
  asm volatile(".Lcode_end:");
  STAMP(10);
}

extern "C" void kernel_launch(void* const* d_in, const int* in_sizes, int n_in, void* d_out, int out_size, void* d_ws, size_t ws_size, hipStream_t stream) {
  (void)hipFuncSetAttribute((const void*)fused, hipFuncAttributeMaxDynamicSharedMemorySize, LDS_BYTES);
  fused<<<256, 1024, LDS_BYTES, stream>>>((const u16*)d_in[0], (const u16*)d_in[1], (const u16*)d_in[2], (const u16*)d_in[3],
                                          (const u16*)d_in[4], (const u16*)d_in[5], (const u16*)d_in[6], (u16*)d_out, (char*)d_ws);
}
```

```cpp
#include <hip/hip_runtime.h>
#include <hip/hip_bf16.h>
#include <type_traits>

typedef unsigned short u16;
typedef unsigned u32;
typedef short bf16x8 __attribute__((ext_vector_type(8)));
typedef short s16x4 __attribute__((ext_vector_type(4)));
typedef float f32x4 __attribute__((ext_vector_type(4)));
typedef float f32x16 __attribute__((ext_vector_type(16)));
typedef float f32x2 __attribute__((ext_vector_type(2)));
typedef __bf16 bf2_t __attribute__((ext_vector_type(2)));
typedef u32 u32x4 __attribute__((ext_vector_type(4)));
typedef u32 u32x2 __attribute__((ext_vector_type(2)));
#define LAS __attribute__((address_space(3)))
#define GAS __attribute__((address_space(1)))
typedef LAS char* lds_ptr;
typedef __amdgpu_buffer_rsrc_t rsrc_t;
#define DI __device__ __forceinline__
#define RFL(x) __builtin_amdgcn_readfirstlane(x)
#define SBAR() __builtin_amdgcn_sched_barrier(0)
#define LP(x) ((lds_ptr)(size_t)(u32)(x))
#define PIN(x) asm volatile("" : "+v"(x))
#define RLX_AGENT __ATOMIC_RELAXED, __HIP_MEMORY_SCOPE_AGENT
#define RLX_WG __ATOMIC_RELAXED, __HIP_MEMORY_SCOPE_WORKGROUP
#define XMAP 2
#define HB 10
#define RPM ((12 + HB - 2) / (HB - 1))
#define S1DELAY 8
#define P3V 1
#define P3S 12
#define P3L 7
#define P3PF 5
#define WFN 8
#define XROT 1
#define ROT 4

constexpr int S_ = 2048, D_ = 640, N3 = 1920;
constexpr int XB = 128 * 64 * 2, WB = 64 * 240 * 2, STB = XB + WB;
constexpr int L_RAW = 3 * STB;
constexpr int L_ZERO = L_RAW + 3200;
constexpr int L_WTAB = L_RAW + 4096;
constexpr int L_MASK = 158720;
constexpr int L_ONES = 160768;
constexpr int L_MISC = L_ONES + 64;
constexpr int L_DUMMY = 162816;
constexpr int LDS_BYTES = 163840;
constexpr int L_HALO = 0, L_OWN = 81920, L_Q = 122880, LB_WO = 56320, L_RED = 0;
constexpr int L_PK = 0, L_AV = 47104, L_AK = 71680, L_AQ = L_AK + 22528, L_KVT = L_WTAB, LA_WO = 40960;
constexpr int M_TAG = 0, M_C0 = 16, M_C1 = 32, M_C2 = 36, M_C3 = 40, M_C4 = 44, M_C5 = 48, M_C6 = 52, M_C7 = 56, M_C9 = 60, M_WR = 64, M_C8 = 112, M_WV = 116, M_P0 = 120, M_P1 = 124, M_D = 128, M_X = 144, M_AD = 176, M_C6B = 180;
constexpr u32 W_EPOCH = 0, W_STARTED = 1024, W_KVFLAG = 4096, W_OFLAG = 32768, W_SLICE = 65536;
constexpr u32 W_PART = 1u << 20, W_KVG = 8u << 20, W_OG = 16u << 20, W_TOTAL = 24u << 20;
constexpr u32 PARTB = 3456;
constexpr u32 OOB = 0x80000000u;
constexpr u32 SPIN_CAP = 1u << 18;

DI void dma16(rsrc_t r, u32 voff, u32 soff, u32 ldsaddr) {
  asm volatile("s_mov_b32 m0, %0\n\ts_nop 0\n\tbuffer_load_dwordx4 %1, %2, %3 offen lds" ::"s"(ldsaddr), "v"(voff), "s"(r), "s"(soff) : "m0", "memory");
}
DI void dma16c(rsrc_t r, u32 voff, u32 soff, u32 ldsaddr) {
  asm volatile("s_mov_b32 m0, %0\n\ts_nop 0\n\tbuffer_load_dwordx4 %1, %2, %3 offen sc1 lds" ::"s"(ldsaddr), "v"(voff), "s"(r), "s"(soff) : "m0", "memory");
}
DI s16x4 trrd(lds_ptr p) { return __builtin_bit_cast(s16x4, __builtin_amdgcn_ds_read_tr16_b64_v4i16((LAS s16x4*)p)); }
DI bf16x8 cat8(s16x4 a, s16x4 b) { return __builtin_shufflevector(a, b, 0, 1, 2, 3, 4, 5, 6, 7); }
DI u32 cvtpk2(f32x2 v) { return __builtin_bit_cast(u32, __builtin_convertvector(v, bf2_t)); }
DI u32 cvtpk(float lo, float hi) { return cvtpk2(f32x2{lo, hi}); }
DI float blo(u32 w) { return __uint_as_float(w << 16); }
DI float bhi(u32 w) { return __uint_as_float(w & 0xffff0000u); }
template <int N> DI void waitv() { asm volatile("s_waitcnt vmcnt(%0)" ::"i"(N) : "memory"); }
DI void lbar() { asm volatile("s_waitcnt lgkmcnt(0)\n\ts_barrier" ::: "memory"); }
DI void waitl() { asm volatile("s_waitcnt lgkmcnt(0)" ::: "memory"); }
DI bf16x8 ld128(lds_ptr p) { return *(LAS bf16x8*)p; }
DI s16x4 ld64(lds_ptr p) { return *(LAS s16x4*)p; }
#define MFMA16(a, b, c) __builtin_amdgcn_mfma_f32_16x16x32_bf16(a, b, c, 0, 0, 0)
#define MFMA32(a, b, c) __builtin_amdgcn_mfma_f32_32x32x16_bf16(a, b, c, 0, 0, 0)
#define MFMA32K8(a, b, c) __builtin_amdgcn_mfma_f32_32x32x8bf16_1k(a, b, c, 0, 0, 0)
DI float ex2(float x) { return __builtin_amdgcn_exp2f(x); }
DI u32 gload(char* ws, u32 off) { return __hip_atomic_load((GAS u32*)(ws + off), RLX_AGENT); }
DI void gstore(char* ws, u32 off, u32 v) { __hip_atomic_store((GAS u32*)(ws + off), v, RLX_AGENT); }
DI u32 lds_word(lds_ptr p) { return __hip_atomic_load((LAS u32*)p, RLX_WG); }
DI void lds_inc(lds_ptr p, int) {
  unsigned long long sv;
  asm volatile("s_mov_b64 %0, exec\n\ts_mov_b64 exec, 1\n\tds_add_u32 %1, %2\n\ts_mov_b64 exec, %0" : "=&s"(sv) : "v"((u32)(size_t)p), "v"(1u) : "memory");
}
DI u32 lds_add_ret(lds_ptr p) { return __hip_atomic_fetch_add((LAS u32*)p, 1u, RLX_WG); }
DI void lds_wait_ge(lds_ptr p, u32 want) {
  u32 v, sv, cnt = SPIN_CAP * 8;
  asm volatile("1:\n\tds_read_b32 %0, %3\n\ts_waitcnt lgkmcnt(0)\n\tv_readfirstlane_b32 %1, %0\n\ts_nop 0\n\ts_cmp_ge_u32 %1, %4\n\ts_cbranch_scc1 2f\n\t"
               "s_sub_u32 %2, %2, 1\n\ts_cbranch_scc1 2f\n\ts_sleep 1\n\ts_branch 1b\n2:"
               : "=&v"(v), "=&s"(sv), "+s"(cnt) : "v"((u32)(size_t)p), "s"(want) : "scc", "memory");
}
DI void poll_tag(char* ws, u32 off, u32 tag, bool done) {
#pragma nounroll
  for (u32 s = 0; s < SPIN_CAP; ++s) { const u32 v = gload(ws, off); if (__all(done || v == tag)) break; __builtin_amdgcn_s_sleep(1); }
  asm volatile("" ::: "memory");
}

#define STAMP(i)
#define STAMPV(i)

struct Frags { bf16x8 x[2]; s16x4 wl[5], wh[5]; };

__global__ __launch_bounds__(1024) void fused(const u16* xA, const u16* wA, const u16* wphi, const u16* woA,
                                              const u16* xB, const u16* wB, const u16* woB, u16* out, char* ws) {
  extern __shared__ __attribute__((aligned(16))) char smem[];
  unsigned long long pc0; u32 len;
  asm volatile("s_getpc_b64 %0\n.Lcode_ref:\n\ts_mov_b32 %1, .Lcode_end-.Lcode_ref" : "=s"(pc0), "=s"(len));
  lds_ptr lds = (lds_ptr)smem;
  const u32 lds0 = (u32)(size_t)lds;
  const int b = blockIdx.x, xcd = b & 7, idx = b >> 3;
  const int br = idx >> 4, rt = (xcd & 3) * 4 + ((idx >> 2) & 3), hg = (xcd >> 2) * 4 + (idx & 3);
  int lane = threadIdx.x & 63; const int wid = RFL(threadIdx.x >> 6);
  int g = lane >> 4, i16 = lane & 15, q4 = i16 >> 2, p4 = lane & 3, h = lane >> 5, l32 = lane & 31;
  auto relane = [&]() { PIN(lane); lane &= 63; g = lane >> 4; i16 = lane & 15; q4 = i16 >> 2; p4 = lane & 3; h = lane >> 5; l32 = lane & 31; };
  const rsrc_t rws = __builtin_amdgcn_make_buffer_rsrc((void*)ws, (short)0, W_TOTAL, 0x00020000);
  lds_ptr misc = lds + L_MISC;
  STAMP(0);

  const rsrc_t rx = __builtin_amdgcn_make_buffer_rsrc((void*)((br ? xB : xA) + (size_t)rt * 128 * D_), (short)0, 128 * D_ * 2, 0x00020000);
  const rsrc_t rw = __builtin_amdgcn_make_buffer_rsrc((void*)(br ? wB : wA), (short)0, D_ * N3 * 2, 0x00020000);
  const int cls = ((idx >> (XMAP ? 2 : 3)) + idx) & 3;
  const unsigned long long orda = (cls & 1) ? 0x8796435201ull : 0x9768532410ull, ordb = (cls & 1) ? 0x9865742130ull : 0x9875463102ull, ord = (cls & 2) ? ordb : orda;
  const int xr = (((xcd & 3) * 5) >> 1) + (xcd >> 2) * 5;
  auto slice = [&](int s) { int k = ((int)(ord >> (4 * s)) & 15) + xr; k -= k >= 20 ? 20 : k >= 10 ? 10 : 0; return k; };
  const int rot = slice(0);
  const u32 vx0 = (lane >> 3) * 1280 + (((lane & 7) ^ (lane >> 4)) << 4), vx1 = vx0 ^ 64;
  const int wc = lane >= 30 ? lane - 30 : lane;
  const u32 vw = (lane >= 30 ? 3840 : 0) + (wc >= 20 ? 2240 : wc >= 10 ? 1120 : 0) + hg * 160 + wc * 16;
  auto ldx = [&](int i, int kt, u32 buf) { dma16(rx, (i & 1) ? vx1 : vx0, 128 * kt + i * 8 * 1280, buf + i * 1024); };
  auto ldw1 = [&](int j, int kt, u32 buf) { const int p = 2 * j, k = (p & 0x33) | ((p & 4) << 1) | ((p & 8) >> 1); dma16(rw, vw, (64 * kt + k) * 3840, buf + XB + j * 960); };
  const u32 og = W_OG + br * (128 * 20480);
  u32 pc[9] = {};
  auto pre = [&]() {
    auto og_main = [&](int row, int hd, int hf) -> u32 { return og + (row >> 4) * 20480 + ((hg * 2 + (hd >> 1)) << 10) + (((2 * (hd & 1) + hf) * 16 + (row & 15)) << 4); };
    auto og_tail = [&](int row, int hd) -> u32 { return og + (row >> 4) * 20480 + 16384 + hg * 512 + ((hd * 16 + (row & 15)) << 3); };
    if (br) {
      const int hd = wid & 3, qi = wid < 8 ? 1 - (wid >> 2) : wid >> 2, u = 35 - l32 + 4 * h; const bool vones = (g & 1) && p4;
      pc[0] = L_Q + hd * 6144 + (32 * qi + l32) * 48 + 16 * h;
      pc[1] = hd * 2560 + l32 * 80 + 16 * h;
      pc[2] = h ? L_ONES : hd * 2560 + l32 * 80 + 32;
      pc[3] = vones ? L_ONES : hd * 2560 + (4 * h + q4) * 80 + 40 + 32 * (g & 1) + 8 * p4;
      pc[4] = h ? 0 : 10240; pc[5] = vones ? 0 : 10240;
      pc[6] = L_MASK + (u & 3) * 256 + (u & ~3) * 4;
      pc[7] = og_main(rt * 128 + 32 * qi + l32, hd, h);
      pc[8] = h ? OOB : og_tail(rt * 128 + 32 * qi + l32, hd);
      pc[0] += lds0; pc[1] += lds0; pc[2] += lds0; pc[3] += lds0; pc[6] += lds0;
    } else {
      const int tg = wid & 3, pair = (wid >> 2) & 1, isq = wid >> 3;
      pc[0] = (isq ? L_AQ : L_AK) + (32 * tg + l32) * 176 + pair * 80 + 16 * h;
      pc[1] = L_WTAB + pair * 5120 + lane * 16;
      pc[2] = L_WTAB + pair * 5120 + 2048 + lane * 8;
      if (isq) {
        const int hp = (l32 >> 2) & 1, r = (l32 & 3) + 4 * (l32 >> 3);
        pc[3] = (h == hp) ? L_KVT + (2 * pair + h) * 1680 + r * 80 : L_ZERO;
        pc[4] = (h == hp && r <= 4) ? L_KVT + (2 * pair + h) * 1680 + (16 + r) * 80 : L_ZERO;
        pc[5] = og_main(rt * 128 + 32 * tg + l32, 2 * pair + h, 0);
        pc[6] = og_tail(rt * 128 + 32 * tg + l32, 2 * pair + h);
        pc[7] = 0;
      } else {
        const int hd = 2 * pair + (tg >> 1), dt = tg & 1;
        pc[3] = L_AV + hd * 6144 + (8 * g + q4) * 48 + 32 * dt + 8 * p4;
        pc[4] = L_PK + (2 * pair + h) * 10240 + (32 * tg + l32) * 80;
        pc[5] = L_PK + hd * 10240 + (8 * g + q4) * 80 + 8 * p4;
        pc[6] = (!dt || i16 <= 4) ? W_PART + ((hg * 4 + hd) * 16 + rt) * PARTB + (dt * 160 + (dt ? g * 5 + i16 : lane)) * 16 : OOB;
        pc[7] = g < 2 ? pc[6] : OOB;
      }
      pc[0] += lds0; pc[1] += lds0; pc[2] += lds0; pc[3] += lds0; pc[4] += lds0; if (!isq) pc[5] += lds0;
      pc[8] = pc[0] + 64 - 8 * h;
    }
#pragma unroll
    for (int i = 0; i < 9; i++) PIN(pc[i]);
  };
  if (wid < 4) {
    __builtin_amdgcn_s_setprio(3);
    u32 ep = 0;
    if (wid == 3) ep = gload(ws, W_EPOCH);
    if (wid == 2) *(LAS u32*)(misc + lane * 4) = 0;
    int kt = rot, bi = 0;
    if (wid == 1) {
      const rsrc_t rc = __builtin_amdgcn_make_buffer_rsrc((void*)(pc0 & ~15ull), (short)0, len & ~15u, 0x00020000);
      dma16(rc, idx * 1024 + lane * 16, 0, lds0 + L_DUMMY);
    }
    __builtin_amdgcn_s_barrier();
#pragma nounroll
    for (int s = 1; s < 10; s++) {
      bi = bi == 2 ? 0 : bi + 1; kt = slice(s);
      const u32 buf = lds0 + bi * STB;
      if (s == 2 && br == 0) {
        const rsrc_t rp = __builtin_amdgcn_make_buffer_rsrc((void*)(wphi + hg * 1600), (short)0, 3200, 0x00020000);
        dma16(rp, wid * 1024 + lane * 16, 0, lds0 + L_RAW + wid * 1024);
      }
#pragma unroll
      for (int i = 0; i < 4; i++) ldx(wid * 4 + i, kt, buf);
      if (lane < 60) {
#pragma unroll
        for (int j = 0; j < 8; j++) ldw1(wid * 8 + j, kt, buf);
      }
      waitv<12>();
      lbar();
      if (s == 9) pre();
      if (s == 9 && br == 0) {
        const int pair = wid >> 1, T = wid & 1, rb = g & 1;
        const int fb = 2 * rb + (p4 >> 1);
        const bool okT = (T == 0) || (fb == 0);
        const int f0 = T ? 16 : 4 * fb;
        s16x4 r[5];
#pragma unroll
        for (int u = 0; u < 5; u++) {
          const int cg = (u < 4) ? 4 * (u >> 1) + 2 * h + (u & 1) : 8 + h;
          const int hc = cg >= 5, d0 = 4 * (cg - 5 * hc);
          const bool ok = okT && (hc == (p4 & 1));
          r[u] = trrd(lds + (ok ? L_RAW + (2 * pair + hc) * 800 + (d0 + q4) * 40 + f0 * 2 : L_ZERO));
        }
        lds_ptr tb = lds + L_WTAB + wid * 2560;
        *(LAS bf16x8*)(tb + lane * 16) = cat8(r[0], r[1]);
        *(LAS bf16x8*)(tb + 1024 + lane * 16) = cat8(r[2], r[3]);
        *(LAS s16x4*)(tb + 2048 + lane * 8) = r[4];
      }
      STAMPV(32 + s - 1);
    }
    {
      const rsrc_t rwo = __builtin_amdgcn_make_buffer_rsrc((void*)(br ? woB : woA), (short)0, D_ * D_ * 2, 0x00020000);
      waitv<0>();
      if (wid == 3 && lane == 0) *(LAS u32*)(misc + M_TAG) = ep * 0x9E3779B1u + 0x7F4A7C15u;
      lbar();
      STAMP(15);
#pragma nounroll
      for (int j = 0; j < 5; j++) {
        const int i = wid * 5 + j;
        const u32 o_ = i < 16 ? og + (rt * 8 + (i >> 1)) * 20480 + hg * 2048 + (i & 1) * 1024 + lane * 16
                              : og + (rt * 8 + 2 * (i - 16) + h) * 20480 + 16384 + hg * 512 + l32 * 16;
        __builtin_amdgcn_raw_buffer_store_b128(u32x4{~0u, ~0u, ~0u, ~0u}, rws, o_, 0, 16);
      }
      { const int ln = (idx & 15) * 200 + wid * 50 + (lane < 50 ? lane : 49); dma16(rwo, (ln / 5) * 1280 + (hg >> 2) * 640 + (ln % 5) * 128, 0, lds0 + L_DUMMY); }
#define NPF 1
    }
    if (br) {
      waitv<NPF>();
      if (lane == 0 && lds_add_ret(misc + M_C4) == 3) gstore(ws, W_STARTED + ((br * 16 + rt) * 8 + hg) * 4, lds_word(misc + M_TAG));
    }
  } else {
    const int cw = wid - 4, mi = cw & 3, nj = cw < 8 ? (cw >> 2) + 1 : 0;
    if (cw < 4) {
#pragma unroll
      for (int i = 0; i < 4; i++) ldx(cw * 4 + i, rot, lds0);
    } else if (lane < 60) {
#pragma unroll
      for (int j = 0; j < 4; j++) ldw1((cw - 4) * 4 + j, rot, lds0);
    }
    __builtin_amdgcn_s_barrier();
    if (cw < 8) {
      const int c = cw & 3, x = lane + c - 35;
      *(LAS float*)(lds + L_MASK + cw * 256 + lane * 4) = ((x > 0) != (cw >= 4)) ? -1e30f : 0.f;
    } else if (cw == 8 && lane < 4) *(LAS u32x2*)(lds + L_ONES + lane * 640) = u32x2{0x3F803F80u, 0x3F803F80u};
    pre();
    const int row = mi * 32 + i16;
    const u32 xa0 = row * 128 + ((g ^ ((row >> 1) & 7)) << 4), xa1 = xa0 ^ 64;
    const u32 wa = XB + (16 * (g >> 1) + 4 * (g & 1) + q4) * 480 + 8 * p4 + nj * 160;
    int base, HS, RS;
    if (br) {
      if (nj == 0) { base = L_Q; HS = 6144; RS = 48; }
      else { base = L_OWN + (nj - 1) * 40 + mi * 7680; HS = 2560; RS = 80; }
    } else {
      if (nj == 2) { base = L_AV; HS = 6144; RS = 48; }
      else { base = nj ? L_AK : L_AQ; HS = 40; RS = 176; }
    }
    int ea[5];
#pragma unroll
    for (int n = 0; n < 5; n++) { const int c4 = 4 * n + g, hd = (c4 * 13) >> 6, d4 = c4 - 5 * hd; ea[n] = lds0 + base + hd * HS + row * RS + d4 * 8; PIN(ea[n]); }
    f32x4 acc[2][5] = {};
    auto rd = [&](Frags& f, lds_ptr bb, int ks, int j) {
      if (j < 2) f.x[j] = ld128(bb + (ks ? xa1 : xa0) + j * 2048);
      else { int n = (j - 2) >> 1; if (j & 1) f.wh[n] = trrd(bb + wa + ks * 15360 + 3840 + n * 32); else f.wl[n] = trrd(bb + wa + ks * 15360 + n * 32); }
    };
#define MM(F, i) acc[(i) & 1][(i) >> 1] = MFMA16(cat8(F.wl[(i) >> 1], F.wh[(i) >> 1]), F.x[(i) & 1], acc[(i) & 1][(i) >> 1])
    Frags F0, F1;
    int boff = 0;
    __builtin_amdgcn_s_setprio(3);
    waitv<0>();
    lbar();
    STAMP(14);
#pragma unroll
    for (int j = 0; j < 12; j++) rd(F0, lds, 0, j);
#pragma unroll
    for (int t = 0; t < 10; t++) {
      SBAR();
#pragma unroll
      for (int i = 0; i < 10; i++) { MM(F0, i); if (i < 6) { rd(F1, lds + boff, 1, 2 * i); rd(F1, lds + boff, 1, 2 * i + 1); } SBAR(); }
#pragma unroll
      for (int i = 0; i < 10 - HB; i++) MM(F1, i);
      SBAR();
      boff = ((t + 1) % 3) * STB; asm volatile("" : "+s"(boff));
      if (t < 9) {
        __builtin_amdgcn_s_waitcnt(0xC07F);
        asm volatile("s_barrier" : "+v"(acc[0][0]), "+v"(acc[1][0]), "+v"(acc[0][1]), "+v"(acc[1][1]), "+v"(acc[0][2]), "+v"(acc[1][2]), "+v"(acc[0][3]), "+v"(acc[1][3]), "+v"(acc[0][4]), "+v"(acc[1][4]) :: "memory");
      } else {
        lds_inc(misc + M_C8, lane);
        asm volatile("" : "+v"(acc[0][0]), "+v"(acc[1][0]), "+v"(acc[0][1]), "+v"(acc[1][1]), "+v"(acc[0][2]), "+v"(acc[1][2]), "+v"(acc[0][3]), "+v"(acc[1][3]), "+v"(acc[0][4]), "+v"(acc[1][4]));
      }
      asm volatile("" ::: "memory");
#pragma unroll
      for (int i = 10 - HB; i < 10; i++) {
#pragma unroll
        for (int j = (i - 10 + HB) * RPM; j < (i - 9 + HB) * RPM && j < 12; j++) if (t < 9) rd(F0, lds + boff, 0, j);
        MM(F1, i); SBAR();
      }
    }
    STAMP(1);
    relane();
    u32x2 pk[2][5];
    if (br && nj == 0) {
#pragma unroll
      for (int i = 0; i < 10; i++) { const f32x4 v = acc[i & 1][i >> 1] * (0.22360679774997896f * 1.4426950408889634f); pk[i & 1][i >> 1] = u32x2{cvtpk(v[0], v[1]), cvtpk(v[2], v[3])}; }
    } else {
#pragma unroll
      for (int i = 0; i < 10; i++) { const f32x4 v = acc[i & 1][i >> 1]; pk[i & 1][i >> 1] = u32x2{cvtpk(v[0], v[1]), cvtpk(v[2], v[3])}; asm volatile("" ::: "memory"); }
    }
    int eb[5];
#pragma unroll
    for (int n = 0; n < 5; n++) { eb[n] = ea[n] + 16 * RS; PIN(eb[n]); }
#pragma unroll
    for (int n = 0; n < 5; n++) { *(LAS u32x2*)LP(ea[n]) = pk[0][n]; *(LAS u32x2*)LP(eb[n]) = pk[1][n]; }
    if (br == 0 && nj == 2) {
#pragma unroll
      for (int m = 0; m < 2; m++) *(LAS u32x2*)(lds + L_AV + g * 6144 + (mi * 32 + i16 + 16 * m) * 48 + 40) = u32x2{0x3F803F80u, 0x3F803F80u};
    }
    lds_inc(misc + M_WR + nj * 16 + mi * 4, lane);
    if (nj == 2) lds_inc(misc + M_WV, lane);
    lds_inc(misc + M_C9, lane);
    if (br && nj == 1) {
      lds_wait_ge(misc + M_WR + 32 + mi * 4, 1u);
      if (mi < 2) __builtin_amdgcn_s_sleep(6);
      const u32 dst = W_KVG + ((rt * 8 + hg) * 4 + mi) * 10240 + lane * 16;
      u32x4 v[10];
#pragma unroll
      for (int i = 0; i < 10; i++) v[i] = *(LAS u32x4*)(lds + L_OWN + mi * 10240 + i * 1024 + lane * 16);
      SBAR();
#pragma unroll
      for (int i = 0; i < 10; i++) __builtin_amdgcn_raw_buffer_store_b128(v[i], rws, dst, i * 1024, 16);
      waitv<0>();
      if (lane == 0) gstore(ws, W_KVFLAG + (rt * 8 + hg) * 128 + mi * 4, lds_word(misc + M_TAG));
      if (mi >> 1) lds_inc(misc + M_P1, lane);
      STAMP(4);
    } else if (br) __builtin_amdgcn_s_setprio(0);
  }
  relane();
  f32x16 o = {};
  PIN(o);
  if (br) lds_wait_ge(misc + M_C9, 12u);
  STAMP(2);
  const u32 tag = RFL(lds_word(misc + M_TAG));
  const int l_wo = br ? LB_WO : LA_WO;
  auto started = [&]() { if (lane == 0) gstore(ws, W_STARTED + ((br * 16 + rt) * 8 + hg) * 4, tag); };
  auto guard = [&]() { poll_tag(ws, W_STARTED + ((br * 16 + rt) * 8 + (lane & 7)) * 4, tag, false); lds_inc(misc + M_C6, lane); };
  const rsrc_t rwo = __builtin_amdgcn_make_buffer_rsrc((void*)(br ? woB : woA), (short)0, D_ * D_ * 2, 0x00020000);
  u32 wvm[2] = {}, wvt[2] = {};
  const int wj = wid & 3;
  auto wo_init = [&]() {
#pragma unroll
    for (int u = 0; u < 2; u++) {
      int c = (u ? 4 : wj) * 64 + lane; PIN(c);
      const int p = c / 10, ch = c - p * 10;
      const int gg = ((p >> 3) & 2) | ((p >> 2) & 1), tt = (p >> 3) & 1, e = p & 3;
      wvm[u] = ((gg >> 1) * 20 + 4 * (gg & 1) + 8 * tt + e) * 1280 + hg * 160 + ch * 16; wvt[u] = (tt * 80 + gg * 20 + 16 + e) * 1280 + hg * 160 + ch * 16;
    }
  };
  auto wo_dma = [&](int k0, int k1) {
    if (k1 == 20) {
#pragma unroll
      for (int ks = 16; ks < 20; ks++) dma16(rwo, wvt[0], (ks - 16) * 160 * 1280, lds0 + l_wo + ks * 5120 + wj * 1024);
      dma16(rwo, wvt[1], wj * 160 * 1280, lds0 + l_wo + (16 + wj) * 5120 + 4096);
      k1 = 16;
    }
#pragma nounroll
    for (int ks = k0; ks < k1; ks++) {
      dma16(rwo, wvm[0], ks * 40 * 1280, lds0 + l_wo + ks * 5120 + wj * 1024);
      if ((ks & 3) == wj) dma16(rwo, wvm[1], ks * 40 * 1280, lds0 + l_wo + ks * 5120 + 4096);
    }
  };
  const bool wow = wid >= 12;
  const u32 ofl = W_OFLAG + (br * 16 + rt) * 128;
  auto published = [&](int rg) {
    waitv<0>();
    lds_inc(misc + M_D + rg * 4, lane);
    if (lane == 0 && lds_add_ret(misc + M_C2) == (br ? 15 : 7)) gstore(ws, ofl + hg * 4, tag);
  };

  if (br) {
    const int hd = wid & 3, qi = wid < 8 ? 1 - (wid >> 2) : wid >> 2;
    const u32 kvf = W_KVFLAG + (rt * 8 + hg) * 128;
    const bf16x8 qa = ld128(LP(pc[0]));
    s16x4 qb = ld64(LP(pc[0]) + 32);
    if (h) qb = s16x4{0, 0, 0, 0};
    auto ldmask = [&](int pol) -> f32x16 {
      f32x4 t[4];
#pragma unroll
      for (int j = 0; j < 4; j++) t[j] = *(LAS f32x4*)(LP(pc[6]) + pol * 1024 + j * 32);
      return __builtin_shufflevector(__builtin_shufflevector(t[0], t[1], 0, 1, 2, 3, 4, 5, 6, 7), __builtin_shufflevector(t[2], t[3], 0, 1, 2, 3, 4, 5, 6, 7),
                                     0, 1, 2, 3, 4, 5, 6, 7, 8, 9, 10, 11, 12, 13, 14, 15);
    };
    auto pk4 = [&](const f32x16& s, int o_) -> bf16x8 {
      u32x4 p;
#pragma unroll
      for (int j = 0; j < 4; j++) p[j] = cvtpk(ex2(s[o_ + 2 * j]), ex2(s[o_ + 2 * j + 1]));
      return __builtin_bit_cast(bf16x8, p);
    };
    lds_ptr ka_p, kb_p, vp;
    auto seek = [&](int row) { ka_p = LP(pc[1]) + row * 10240; kb_p = LP(pc[2]) + row * pc[4]; vp = LP(pc[3]) + row * pc[5]; };
    f32x16 s, s2; s16x4 c;
    auto step = [&](f32x16& sc, f32x16& sn) {
      ka_p -= 10240; kb_p -= pc[4];
      const bf16x8 ka = ld128(ka_p); const s16x4 kb = ld64(kb_p);
      const bf16x8 v0 = cat8(trrd(vp), trrd(vp + 640)), v1 = cat8(trrd(vp + 1280), trrd(vp + 1920));
      vp -= pc[5];
      SBAR();
      float e[8]; u32x4 p1;
      bf16x8 p0 = pk4(sc, 0); PIN(p0);
      SBAR(); sn = MFMA32(ka, qa, f32x16{}); SBAR();
#pragma unroll
      for (int j = 0; j < 4; j++) e[j] = ex2(sc[8 + j]);
      SBAR(); sn = MFMA32K8(kb, c, sn); SBAR();
#pragma unroll
      for (int j = 4; j < 8; j++) e[j] = ex2(sc[8 + j]);
      SBAR(); o = MFMA32(v0, p0, o); SBAR();
#pragma unroll
      for (int j = 0; j < 4; j++) p1[j] = cvtpk(e[2 * j], e[2 * j + 1]);
      SBAR(); o = MFMA32(v1, __builtin_bit_cast(bf16x8, p1), o); SBAR();
    };
    auto last = [&](f32x16& sc) {
      const bf16x8 v0 = cat8(trrd(vp), trrd(vp + 640)), v1 = cat8(trrd(vp + 1280), trrd(vp + 1920));
      o = MFMA32(v0, pk4(sc, 0), o);
      o = MFMA32(v1, pk4(sc, 8), o);
    };
    lds_inc(misc + M_C5, lane);
    seek(L_OWN / 10240 + qi);
    {
      const s16x4 kb = ld64(kb_p);
      s = MFMA32K8(kb, qb, MFMA32(ld128(ka_p), qa, ldmask(0)));
      float mm = fmaxf(s[0], s[1]);
#pragma unroll
      for (int r = 2; r < 16; r += 2) mm = fmaxf(fmaxf(mm, s[r]), s[r + 1]);
      auto rr = __builtin_amdgcn_permlane32_swap(__float_as_uint(mm), __float_as_uint(mm), false, false);
      const u32 nm = cvtpk(-fmaxf(__uint_as_float(rr[0]), __uint_as_float(rr[1])), 0.f);
      const u32x2 qw = __builtin_bit_cast(u32x2, qb);
      c = __builtin_bit_cast(s16x4, u32x2{h ? nm : qw.x, qw.y});
      s = MFMA32K8(kb, __builtin_bit_cast(s16x4, u32x2{h ? nm : 0u, 0u}), s);
    }
    if (qi >= 2) { step(s, s2); step(s2, s); }
    if (qi & 1) { step(s, s2); last(s2); } else last(s);
    lds_inc(misc + M_C1, lane);
    STAMP(3);
    if (wid < 4) {
      const int dd = 2 - (wid >> 1);
      bool seen = false;
#pragma nounroll
      for (int hf = 1; hf >= 0; --hf) {
        const int tl = 2 * hf + (wid & 1);
        if (hf) lds_wait_ge(misc + M_P1, 2u);
        else {
          const u32 v = gload(ws, kvf - (rt >= dd ? dd : 0) * 1024 + tl * 4);
          lds_inc(misc + M_P0, lane);
          waitv<0>();
          seen = RFL(v) == tag;
          STAMP(5); lds_inc(misc + M_C0, lane);
        }
        if (rt >= dd && !seen) poll_tag(ws, kvf - dd * 1024 + tl * 4, tag, false);
        if (hf) { STAMP(11); }
        if (rt >= dd) {
          const u32 src = W_KVG + (((rt - dd) * 8 + hg) * 4 + tl) * 10240;
#pragma unroll
          for (int i = 0; i < 10; i++) dma16c(rws, lane * 16, src + i * 1024, lds0 + L_HALO + hf * 40960 + wid * 10240 + i * 1024);
        }
      }
    }
    if (wid >= 8) wo_init();
    if (qi == 2) { lds_wait_ge(misc + M_C5, 16u); lds_wait_ge(misc + M_P0, 4u); wo_dma(13, 20); }
    lds_wait_ge(misc + M_C0, 4u);
    int wk = 13;
    if (rt > 0) {
      int n1 = 2, nn = 4, km = 0;
      if (rt >= 2) { n1 = qi < 3 ? 4 : 3; nn = 8 - qi; km = qi >= 2 ? n1 - 1 : nn - 1; }
      const u32 ev1 = (1u << n1) | (wid < 4 ? 4u : 0u) | (wow ? ~0u : 0u);
      const u32 ev2 = (1u << n1) | (1u << km);
      if (wow) __builtin_amdgcn_s_setprio(3); else __builtin_amdgcn_s_setprio(2);
      seek(7);
      s = MFMA32K8(ld64(kb_p), c, MFMA32(ld128(ka_p), qa, f32x16{}));
      auto before = [&](int k) {
        if (k == 2 && wid < 4) { waitv<0>(); lds_inc(misc + M_C7, lane); }
        if (k == n1) {
          ka_p -= (4 - n1) * 10240; kb_p -= (4 - n1) * pc[4];
          lds_wait_ge(misc + M_C7, 4u);
          if (qi == 2) { waitv<0>(); lds_inc(misc + M_C6, lane); }
          if (wid == 10) guard();
        }
        if (wow && wk > 0) {
          const u32 c1 = RFL(lds_word(misc + M_C1)), c7 = RFL(lds_word(misc + M_C7)), c3 = RFL(lds_word(misc + M_C3));
          const int lim = c3 >= 16 ? 0 : (c1 >= 16 && c7 >= 4) ? 5 : 20;
          const int k0 = wk - 8 > lim ? wk - 8 : lim;
          if (k0 < wk) { wo_dma(k0, wk); wk = k0; }
        }
      };
      auto after = [&](int k, f32x16& sn) {
        if (k == km) sn += ldmask(1);
        if (k == n1) { vp -= (4 - n1) * pc[5]; lds_inc(misc + M_C3, lane); if (!wow) __builtin_amdgcn_s_setprio(1); }
      };
#pragma nounroll
      for (int k = 1;; k += 2) {
        if (__builtin_expect((ev1 >> k) & 1, 0)) before(k);
        step(s, s2);
        if (__builtin_expect((ev2 >> k) & 1, 0)) after(k, s2);
        if (__builtin_expect(k + 1 == nn, 0)) { s = s2; break; }
        if (__builtin_expect((ev1 >> (k + 1)) & 1, 0)) before(k + 1);
        step(s2, s);
        if (__builtin_expect((ev2 >> (k + 1)) & 1, 0)) after(k + 1, s);
        if (__builtin_expect(k + 2 == nn, 0)) break;
      }
      last(s);
    } else {
      if (qi == 2) { waitv<0>(); lds_inc(misc + M_C6, lane); }
      if (wid == 10) guard();
    }
    float l = h ? o[8] : o[12];
    if (__builtin_expect(__any(!(l < 1.8e19f)), 0)) {
      const int dd = lane % 3;
      poll_tag(ws, kvf - (rt - dd < 0 ? 0 : dd) * 1024 + 4 * (lane & 3), tag, false);
      float q[20], a[20], mx = -3e38f; l = 0.f;
      const u32x4 qw = __builtin_bit_cast(u32x4, qa); const u32x2 qv = __builtin_bit_cast(u32x2, qb);
#pragma unroll
      for (int j = 0; j < 10; j++) {
        const u32 w = j < 8 ? __shfl(qw[j & 3], l32 + 32 * (j >> 2)) : __shfl(qv[j & 1], l32);
        q[2 * j] = blo(w); q[2 * j + 1] = bhi(w); a[2 * j] = 0.f; a[2 * j + 1] = 0.f;
      }
      const int row = rt * 128 + 32 * qi + l32;
#pragma nounroll
      for (int t = 0; t < 128; ++t) {
        const int j = row - 2 * t - h;
        if (j >= 0) {
          const u32 src = W_KVG + (((j >> 7) * 8 + hg) * 4 + ((j >> 5) & 3)) * 10240 + hd * 2560 + (j & 31) * 80;
          u32 w[20];
#pragma unroll
          for (int i = 0; i < 5; i++) { const u32x4 v = __builtin_amdgcn_raw_buffer_load_b128(rws, src, i * 16, 16); w[4 * i] = v.x; w[4 * i + 1] = v.y; w[4 * i + 2] = v.z; w[4 * i + 3] = v.w; }
          float sc = 0.f;
#pragma unroll
          for (int i = 0; i < 10; i++) sc += q[2 * i] * blo(w[i]) + q[2 * i + 1] * bhi(w[i]);
          const float mn = fmaxf(mx, sc), f = ex2(mx - mn), p = ex2(sc - mn);
          mx = mn; l = l * f + p;
#pragma unroll
          for (int i = 0; i < 10; i++) { a[2 * i] = a[2 * i] * f + p * blo(w[10 + i]); a[2 * i + 1] = a[2 * i + 1] * f + p * bhi(w[10 + i]); }
        }
      }
      const float mo = __shfl_xor(mx, 32), mn = fmaxf(mx, mo), f = ex2(mx - mn);
      l *= f; l += __shfl_xor(l, 32);
#pragma unroll
      for (int i = 0; i < 20; i++) { a[i] *= f; a[i] += __shfl_xor(a[i], 32); }
#pragma unroll
      for (int r = 0; r < 8; r++) o[r] = h ? a[(r & 3) + 8 * (r >> 2) + 4] : a[(r & 3) + 8 * (r >> 2)];
#pragma unroll
      for (int r = 8; r < 12; r++) o[r] = a[8 + r];
    }
    const float rl = __builtin_amdgcn_rcpf(l);
    const u32x4 w0 = {cvtpk(o[0] * rl, o[1] * rl), cvtpk(o[2] * rl, o[3] * rl), cvtpk(o[4] * rl, o[5] * rl), cvtpk(o[6] * rl, o[7] * rl)};
    const u32x2 w1 = {cvtpk(o[8] * rl, o[9] * rl), cvtpk(o[10] * rl, o[11] * rl)};
    __builtin_amdgcn_raw_buffer_store_b128(w0, rws, pc[7], 0, 16);
    __builtin_amdgcn_raw_buffer_store_b64(w1, rws, pc[8], 0, 16);
    STAMP(6);
    lds_inc(misc + M_AD, lane);
    if (wow && wk > 5) { lds_wait_ge(misc + M_C1, 16u); wo_dma(5, wk); wk = 5; }
    published(qi);
    if (wow) {
      lds_inc(misc + M_C6, lane);
      if (wk > 0) { if (rt) lds_wait_ge(misc + M_C3, 16u); wo_dma(0, wk); waitv<0>(); }
      lds_inc(misc + M_C6B, lane);
    }
    __builtin_amdgcn_s_setprio(0);
  } else {
    const int tg = wid & 3, pair = (wid >> 2) & 1, isq = wid >> 3;
    u32 phi[20];
    {
      lds_wait_ge(misc + M_WR + (isq ? 0 : 16) + tg * 4, 1u);
      const bf16x8 b0 = ld128(LP(pc[0])), a00 = ld128(LP(pc[1])), a10 = ld128(LP(pc[1]) + 2560);
      const bf16x8 b1 = ld128(LP(pc[0]) + 32), a01 = ld128(LP(pc[1]) + 1024), a11 = ld128(LP(pc[1]) + 2560 + 1024);
      const s16x4 b2 = ld64(LP(pc[8])), a02 = ld64(LP(pc[2])), a12 = ld64(LP(pc[2]) + 2560);
      lds_inc(misc + M_C5, lane);
      if (wow) { wo_init(); lds_wait_ge(misc + M_C5, 16u); wo_dma(6, 20); waitv<0>(); lds_inc(misc + M_C6, lane); }
      if (isq) { __builtin_amdgcn_s_setprio(0); lds_wait_ge(misc + M_C0 + 4, 4u); } else __builtin_amdgcn_s_setprio(2);
      f32x16 t0 = MFMA32(a00, b0, f32x16{}), t1 = MFMA32(a10, b0, f32x16{});
      t0 = MFMA32(a01, b1, t0); t1 = MFMA32(a11, b1, t1);
      t0 = MFMA32K8(a02, b2, t0); t1 = MFMA32K8(a12, b2, t1);
      float p[20];
#pragma unroll
      for (int j = 0; j < 10; j++) {
        const u32 w = j < 8 ? cvtpk(t0[2 * j], t0[2 * j + 1]) : cvtpk(t1[2 * j - 16], t1[2 * j - 15]);
        p[2 * j] = blo(w); p[2 * j + 1] = bhi(w);
      }
      float mx = fmaxf(p[0], p[1]), mn = fminf(p[0], p[1]);
#pragma unroll
      for (int j = 2; j < 20; j += 2) { mx = fmaxf(fmaxf(mx, p[j]), p[j + 1]); mn = fminf(fminf(mn, p[j]), p[j + 1]); }
      const float L2E = 1.4426950408889634f;
      const f32x2 sl = {L2E, -L2E}, of = {-mx * L2E, mn * L2E};
      f32x2 e[20], sa = {0.f, 0.f}, sb = {0.f, 0.f};
#pragma unroll
      for (int j = 0; j < 20; j++) {
        const f32x2 a = __builtin_elementwise_fma(f32x2{p[j], p[j]}, sl, of);
        e[j] = f32x2{ex2(a[0]), ex2(a[1])};
        if (j & 1) sb += e[j]; else sa += e[j];
      }
      sa += sb;
      const f32x2 rr = {__builtin_amdgcn_rcpf(sa[0]), __builtin_amdgcn_rcpf(sa[1])};
#pragma unroll
      for (int j = 0; j < 20; j++) phi[j] = cvtpk2(e[j] * rr);
    }
    if (!isq) {
      lds_wait_ge(misc + M_WV, 4u);
      bf16x8 vb[4];
#pragma unroll
      for (int ks = 0; ks < 4; ks++) vb[ks] = cat8(trrd(LP(pc[3]) + ks * 32 * 48), trrd(LP(pc[3]) + ks * 32 * 48 + 4 * 48));
      lds_wait_ge(misc + M_C8, 12u);
#pragma unroll
      for (int j = 0; j < 5; j++) *(LAS u32x4*)(LP(pc[4]) + 16 * j) = u32x4{phi[4 * j], phi[4 * j + 1], phi[4 * j + 2], phi[4 * j + 3]};
      lds_inc(misc + M_C0 + pair * 4, lane);
      STAMP(3);
      lds_wait_ge(misc + M_C0 + pair * 4, 4u);
      f32x4 kv[3] = {};
#pragma unroll
      for (int ks = 0; ks < 4; ks++)
#pragma unroll
        for (int T = 0; T < 3; T++) kv[T] = MFMA16(cat8(trrd(LP(pc[5]) + ks * 2560 + T * 32), trrd(LP(pc[5]) + ks * 2560 + 320 + T * 32)), vb[ks], kv[T]);
#pragma unroll
      for (int T = 0; T < 3; T++)
        __builtin_amdgcn_raw_buffer_store_b128(u32x4{cvtpk(kv[T][0], kv[T][1]), tag, cvtpk(kv[T][2], kv[T][3]), tag}, rws, pc[T < 2 ? 6 : 7], T * ((tg & 1) ? 320 : 1024), 16);
      waitv<3>();
      lds_inc(misc + M_C1, lane);
      STAMP(4);
      if (wid == 0) {
        const int sl = (rt & 3) * 53 + lane; const bool act = lane < 53 && sl < 210;
        const u32 src = act ? W_PART + (hg * 4 + (rt >> 2)) * 16 * PARTB + sl * 16 : OOB;
        float s0 = 0.f, s1 = 0.f, s2 = 0.f, s3 = 0.f;
        lds_wait_ge(misc + M_C6, 4u);
        lds_wait_ge(misc + M_C7, 4u);
#pragma nounroll
        for (u32 s = 0; s < SPIN_CAP; ++s) {
          bool ok = true; s0 = 0.f; s1 = 0.f; s2 = 0.f; s3 = 0.f;
          u32x4 v[16];
#pragma unroll
          for (int r = 0; r < 16; r++) v[r] = __builtin_amdgcn_raw_buffer_load_b128(rws, src, r * PARTB, 16);
#pragma unroll
          for (int r = 0; r < 16; r++) { ok &= (v[r].y == tag) & (v[r].w == tag); s0 += blo(v[r].x); s1 += bhi(v[r].x); s2 += blo(v[r].z); s3 += bhi(v[r].z); }
          if (__all(ok || !act)) break;
          asm volatile("" ::: "memory");
        }
        __builtin_amdgcn_raw_buffer_store_b128(u32x4{cvtpk(s0, s1), tag, cvtpk(s2, s3), tag}, rws, act ? W_SLICE + (hg * 16 + rt) * 1024 + lane * 16 : OOB, 0, 16);
        waitv<0>();
        lds_inc(misc + M_C4, lane);
        STAMP(5);
      } else if (wid == 1) { lds_wait_ge(misc + M_C1, 8u); started(); lds_wait_ge(misc + M_C4, 1u); guard(); }
      else if (wid >= 4) {
        const int hh = wid - 4;
        const u32 src = W_SLICE + (hg * 16 + hh * 4) * 1024 + lane * 16;
        int da[4];
#pragma unroll
        for (int j = 0; j < 4; j++) {
          const int sl = j * 53 + lane; int d, ph;
          if (sl < 160) { d = sl & 15; ph = (sl >> 4) * 4; }
          else { const int s2 = sl - 160, gg = s2 / 5; d = 16 + s2 - 5 * gg; ph = 4 * gg; }
          da[j] = L_KVT + hh * 1680 + d * 80 + ph * 2;
        }
        waitv<0>(); lds_inc(misc + M_C7, lane);
        wo_init();
        lds_wait_ge(misc + M_C1, 8u);
        lds_wait_ge(misc + M_C5, 16u);
        lds_wait_ge(misc + M_C4, 1u);
        u32x4 v[4];
#pragma nounroll
        for (u32 s = 0; s < SPIN_CAP; ++s) {
          bool ok = true;
#pragma unroll
          for (int r = 0; r < 4; r++) v[r] = __builtin_amdgcn_raw_buffer_load_b128(rws, src, r * 1024, 16);
#pragma unroll
          for (int r = 0; r < 4; r++) ok &= ((v[r].y == tag) & (v[r].w == tag)) | (lane >= (r < 3 ? 53 : 51));
          if (__all(ok)) break;
          asm volatile("" ::: "memory");
        }
#pragma unroll
        for (int r = 0; r < 4; r++) if (lane < (r < 3 ? 53 : 51)) *(LAS u32x2*)(lds + da[r]) = u32x2{v[r].x, v[r].z};
        lds_inc(misc + M_C3, lane);
        STAMP(11);
        wo_dma(0, 6); waitv<0>(); lds_inc(misc + M_C6, lane);
      }
      __builtin_amdgcn_s_setprio(0);
    } else {
      lds_ptr a0 = LP(pc[3]), a1 = LP(pc[4]); const u32 om = pc[5], ot = pc[6];
      STAMP(12);
      lds_wait_ge(misc + M_C3, 4u);
      f32x16 t0 = {}, t1 = {};
#pragma unroll
      for (int j = 0; j < 5; j++) {
        const bf16x8 bq = __builtin_bit_cast(bf16x8, u32x4{phi[4 * j], phi[4 * j + 1], phi[4 * j + 2], phi[4 * j + 3]});
        t1 = MFMA32(ld128(a1 + 16 * j), bq, t1); t0 = MFMA32(ld128(a0 + 16 * j), bq, t0);
      }
      const float rl = __builtin_amdgcn_rcpf(t1[4] + 1e-6f);
      const u32x4 w0 = {cvtpk(t0[0] * rl, t0[1] * rl), cvtpk(t0[2] * rl, t0[3] * rl), cvtpk(t0[8] * rl, t0[9] * rl), cvtpk(t0[10] * rl, t0[11] * rl)};
      const u32x4 w1 = {cvtpk(t0[4] * rl, t0[5] * rl), cvtpk(t0[6] * rl, t0[7] * rl), cvtpk(t0[12] * rl, t0[13] * rl), cvtpk(t0[14] * rl, t0[15] * rl)};
      const u32x2 w2 = {cvtpk(t1[0] * rl, t1[1] * rl), cvtpk(t1[2] * rl, t1[3] * rl)};
      __builtin_amdgcn_raw_buffer_store_b128(w0, rws, om, 0, 16);
      __builtin_amdgcn_raw_buffer_store_b128(w1, rws, om, 256, 16);
      __builtin_amdgcn_raw_buffer_store_b64(w2, rws, ot, 0, 16);
      STAMP(6);
      published(tg);
    }
  }
  STAMP(7);
  relane();
  if (wid >= 8) {
    if (b == 0 && wid == 15) {
#pragma nounroll
      for (u32 s = 0; s < SPIN_CAP; ++s) {
        const u32x4 v = __builtin_amdgcn_raw_buffer_load_b128(rws, W_STARTED + lane * 16, 0, 16);
        if (__all(v.x == tag && v.y == tag && v.z == tag && v.w == tag)) break;
        __builtin_amdgcn_s_sleep(2);
      }
      if (lane == 0) gstore(ws, W_EPOCH, tag);
    }
  } else {
    const int rg = br ? (0x2013 >> (4 * (wid >> 1))) & 3 : wid >> 1, kh = wid & 1;
    const u32 obm = og + (rt * 8 + rg * 2) * 20480 + kh * 8192 + lane * 16, obt = og + (rt * 8 + rg * 2) * 20480 + kh * 2048 + lane * 8;
    const u32 mo[2] = {(u32)kh * 20480u, (u32)(1 - kh) * 20480u};
    u32x4 of[10][2];
    auto ldo = [&](int s) {
#pragma unroll
      for (int m = 0; m < 2; m++) {
        if (s < 8) of[s][m] = __builtin_amdgcn_raw_buffer_load_b128(rws, obm, mo[m] + s * 1024, 16);
        else {
          const u32x2 a = __builtin_amdgcn_raw_buffer_load_b64(rws, obt, mo[m] + (s + 8) * 1024, 16);
          const u32x2 c = __builtin_amdgcn_raw_buffer_load_b64(rws, obt, mo[m] + (s + 8) * 1024 + 512, 16);
          of[s][m] = u32x4{a.x, a.y, c.x, c.y};
        }
      }
    };
    u32 wm = lds0 + l_wo + (16 * (g >> 1) + 4 * (g & 1) + q4) * 160 + 8 * p4, wt = wm + (16 + 2 * kh) * 5120; wm += kh * 8 * 5120;
    PIN(wm); PIN(wt);
    bf16x8 wf[WFN];
    auto ldw = [&](int i) { const int s = 9 - i / 5, n = i % 5; lds_ptr p = (s < 8 ? LP(wm) + s * 5120 : LP(wt) + (s - 8) * 5120) + n * 32; wf[i % WFN] = cat8(trrd(p), trrd(p + 1280)); };
    f32x4 ac2[2][5];
    constexpr int PF = P3PF;
    lds_wait_ge(misc + M_C6, 9u);
    lds_wait_ge(misc + M_D + rg * 4, br ? 4u : 2u);
    STAMP(8);
#pragma nounroll
    for (int pass = 0;; ++pass) {
      if (pass) poll_tag(ws, ofl + (lane & 7) * 4, tag, false);
#pragma unroll
      for (int i = 0; i < WFN; i++) ldw(i);
#pragma nounroll
      for (int tr = 0; tr < 64; ++tr) {
#pragma unroll
        for (int s = 9; s > 5; s--) ldo(s);
        asm volatile("" ::: "memory");
        u32 mx = 0;
#pragma unroll
        for (int s = 8; s < 10; s++)
#pragma unroll
          for (int m = 0; m < 2; m++) mx = max(max(mx, max(of[s][m].x, of[s][m].y)), max(of[s][m].z, of[s][m].w));
        if (pass || !__any(mx == ~0u)) break;
      }
      STAMP(9);
#pragma unroll
      for (int s = 5; s > 9 - PF; s--) ldo(s);
#pragma unroll
      for (int n = 0; n < 5; n++) { ac2[0][n] = f32x4{}; ac2[1][n] = f32x4{}; }
#pragma unroll
      for (int s = 9; s >= 0; s--) {
        if (s == 6) lds_wait_ge(misc + M_C6B, br ? 4u : 0u);
        SBAR();
#pragma unroll
        for (int n = 0; n < 5; n++) {
          const int i = (9 - s) * 5 + n;
          ac2[0][n] = MFMA16(wf[i % WFN], __builtin_bit_cast(bf16x8, of[s][0]), ac2[0][n]);
          ac2[1][n] = MFMA16(wf[i % WFN], __builtin_bit_cast(bf16x8, of[s][1]), ac2[1][n]);
          if (i + WFN < 50) ldw(i + WFN);
          SBAR();
        }
        if (s - PF >= 0) ldo(s - PF);
      }
      const float chk = ac2[0][0][0] + ac2[1][0][0];
      if (pass || !__any(chk != chk)) break;
    }
    STAMP(13);
    relane();
    if (br) lds_wait_ge(misc + M_AD, 16u);
    lds_ptr red = lds + L_RED + lane * 16;
#pragma unroll
    for (int n = 0; n < 5; n++) *(LAS f32x4*)(red + (wid ^ 1) * 5120 + n * 1024) = ac2[1][n];
    lds_inc(misc + M_X + (wid ^ 1) * 4, lane);
    const int r16 = rt * 8 + rg * 2 + kh;
    lds_wait_ge(misc + M_X + wid * 4, 1u);
    f32x4 acc[5];
#pragma unroll
    for (int n = 0; n < 5; n++) acc[n] = *(LAS f32x4*)(red + wid * 5120 + n * 1024);
    SBAR();
#pragma unroll
    for (int n = 0; n < 5; n++) acc[n] += ac2[0][n];
    const rsrc_t ro = __builtin_amdgcn_make_buffer_rsrc((void*)out, (short)0, S_ * 1280 * 2, 0x00020000);
    const u32 obase = (r16 * 16 + i16) * 2560 + br * 1280 + hg * 160;
#pragma unroll
    for (int n = 0; n < 4; n += 2) {
      const auto x = __builtin_amdgcn_permlane16_swap(cvtpk(acc[n][0], acc[n][1]), cvtpk(acc[n + 1][0], acc[n + 1][1]), false, false);
      const auto y = __builtin_amdgcn_permlane16_swap(cvtpk(acc[n][2], acc[n][3]), cvtpk(acc[n + 1][2], acc[n + 1][3]), false, false);
      __builtin_amdgcn_raw_buffer_store_b128(u32x4{x[0], y[0], x[1], y[1]}, ro, obase + (g & 1) * 32 + (g >> 1) * 16, n * 32, 0);
    }
    __builtin_amdgcn_raw_buffer_store_b64(u32x2{cvtpk(acc[4][0], acc[4][1]), cvtpk(acc[4][2], acc[4][3])}, ro, obase + g * 8, 128, 0);
  }
  asm volatile(".Lcode_end:");
  STAMP(10);
}

extern "C" void kernel_launch(void* const* d_in, const int* in_sizes, int n_in, void* d_out, int out_size, void* d_ws, size_t ws_size, hipStream_t stream) {
  (void)hipFuncSetAttribute((const void*)fused, hipFuncAttributeMaxDynamicSharedMemorySize, LDS_BYTES);
  fused<<<256, 1024, LDS_BYTES, stream>>>((const u16*)d_in[0], (const u16*)d_in[1], (const u16*)d_in[2], (const u16*)d_in[3],
                                          (const u16*)d_in[4], (const u16*)d_in[5], (const u16*)d_in[6], (u16*)d_out, (char*)d_ws);
}
```

```cpp
#include <hip/hip_runtime.h>
#include <hip/hip_bf16.h>

typedef unsigned short u16;
typedef unsigned u32;
typedef short bf16x8 __attribute__((ext_vector_type(8)));
typedef short s16x4 __attribute__((ext_vector_type(4)));
typedef float f32x4 __attribute__((ext_vector_type(4)));
typedef float f32x16 __attribute__((ext_vector_type(16)));
typedef float f32x2 __attribute__((ext_vector_type(2)));
typedef __bf16 bf2_t __attribute__((ext_vector_type(2)));
typedef u32 u32x4 __attribute__((ext_vector_type(4)));
typedef u32 u32x2 __attribute__((ext_vector_type(2)));
#define LAS __attribute__((address_space(3)))
#define GAS __attribute__((address_space(1)))
typedef LAS char* lds_ptr;
typedef __amdgpu_buffer_rsrc_t rsrc_t;
#define DI __device__ __forceinline__
#define RFL(x) __builtin_amdgcn_readfirstlane(x)
#define SBAR() __builtin_amdgcn_sched_barrier(0)
#define LP(x) ((lds_ptr)(size_t)(u32)(x))
#define PIN(x) asm volatile("" : "+v"(x))
#define RLX_AGENT __ATOMIC_RELAXED, __HIP_MEMORY_SCOPE_AGENT
#define RLX_WG __ATOMIC_RELAXED, __HIP_MEMORY_SCOPE_WORKGROUP

constexpr int S_ = 2048, D_ = 640, N3 = 1920;
constexpr int XB = 128 * 64 * 2, WB = 64 * 240 * 2, STB = XB + WB;
constexpr int L_RAW = 3 * STB;
constexpr int L_ZERO = L_RAW + 3200;
constexpr int L_WTAB = L_RAW + 4096;
constexpr int L_MASK = 158720;
constexpr int L_ONES = 160768;
constexpr int L_MISC = L_ONES + 64;
constexpr int L_DUMMY = 162816;
constexpr int LDS_BYTES = 163840;
constexpr int L_HALO = 0, L_OWN = 81920, L_Q = 122880, LB_WO = 56320, L_RED = 0;
constexpr int L_PK = 0, L_AV = 47104, L_AK = 71680, L_AQ = L_AK + 22528, L_KVT = L_WTAB, LA_WO = 40960;
constexpr int M_TAG = 0, M_C0 = 16, M_C1 = 32, M_C2 = 36, M_C3 = 40, M_C4 = 44, M_C5 = 48, M_C6 = 52, M_C7 = 56, M_C9 = 60, M_WR = 64, M_C8 = 112, M_WV = 116, M_P0 = 120, M_P1 = 124, M_D = 128, M_X = 144, M_AD = 176, M_C6B = 180;
constexpr u32 W_EPOCH = 0, W_STARTED = 1024, W_KVFLAG = 4096, W_OFLAG = 32768, W_SLICE = 65536;
constexpr u32 W_PART = 1u << 20, W_KVG = 8u << 20, W_OG = 16u << 20, W_TOTAL = 24u << 20;
constexpr u32 PARTB = 3456;
constexpr u32 OOB = 0x80000000u;
constexpr u32 SPIN_CAP = 1u << 18;

DI void dma16(rsrc_t r, u32 voff, u32 soff, u32 ldsaddr) {
  asm volatile("s_mov_b32 m0, %0\n\ts_nop 0\n\tbuffer_load_dwordx4 %1, %2, %3 offen lds" ::"s"(ldsaddr), "v"(voff), "s"(r), "s"(soff) : "m0", "memory");
}
DI void dma16c(rsrc_t r, u32 voff, u32 soff, u32 ldsaddr) {
  asm volatile("s_mov_b32 m0, %0\n\ts_nop 0\n\tbuffer_load_dwordx4 %1, %2, %3 offen sc1 lds" ::"s"(ldsaddr), "v"(voff), "s"(r), "s"(soff) : "m0", "memory");
}
DI s16x4 trrd(lds_ptr p) { return __builtin_bit_cast(s16x4, __builtin_amdgcn_ds_read_tr16_b64_v4i16((LAS s16x4*)p)); }
DI bf16x8 cat8(s16x4 a, s16x4 b) { return __builtin_shufflevector(a, b, 0, 1, 2, 3, 4, 5, 6, 7); }
DI u32 cvtpk2(f32x2 v) { return __builtin_bit_cast(u32, __builtin_convertvector(v, bf2_t)); }
DI u32 cvtpk(float lo, float hi) { return cvtpk2(f32x2{lo, hi}); }
DI float blo(u32 w) { return __uint_as_float(w << 16); }
DI float bhi(u32 w) { return __uint_as_float(w & 0xffff0000u); }
template <int N> DI void waitv() { asm volatile("s_waitcnt vmcnt(%0)" ::"i"(N) : "memory"); }
DI void lbar() { asm volatile("s_waitcnt lgkmcnt(0)\n\ts_barrier" ::: "memory"); }
DI void waitl() { asm volatile("s_waitcnt lgkmcnt(0)" ::: "memory"); }
DI bf16x8 ld128(lds_ptr p) { return *(LAS bf16x8*)p; }
DI s16x4 ld64(lds_ptr p) { return *(LAS s16x4*)p; }
#define MFMA16(a, b, c) __builtin_amdgcn_mfma_f32_16x16x32_bf16(a, b, c, 0, 0, 0)
#define MFMA32(a, b, c) __builtin_amdgcn_mfma_f32_32x32x16_bf16(a, b, c, 0, 0, 0)
#define MFMA32K8(a, b, c) __builtin_amdgcn_mfma_f32_32x32x8bf16_1k(a, b, c, 0, 0, 0)
DI float ex2(float x) { return __builtin_amdgcn_exp2f(x); }
DI u32 gload(char* ws, u32 off) { return __hip_atomic_load((GAS u32*)(ws + off), RLX_AGENT); }
DI void gstore(char* ws, u32 off, u32 v) { __hip_atomic_store((GAS u32*)(ws + off), v, RLX_AGENT); }
DI u32 lds_word(lds_ptr p) { return __hip_atomic_load((LAS u32*)p, RLX_WG); }
DI void lds_inc(lds_ptr p, int) {
  unsigned long long sv;
  asm volatile("s_mov_b64 %0, exec\n\ts_mov_b64 exec, 1\n\tds_add_u32 %1, %2\n\ts_mov_b64 exec, %0" : "=&s"(sv) : "v"((u32)(size_t)p), "v"(1u) : "memory");
}
DI u32 lds_add_ret(lds_ptr p) { return __hip_atomic_fetch_add((LAS u32*)p, 1u, RLX_WG); }
DI void lds_wait_ge(lds_ptr p, u32 want) {
  u32 v, sv, cnt = SPIN_CAP * 8;
  asm volatile("1:\n\tds_read_b32 %0, %3\n\ts_waitcnt lgkmcnt(0)\n\tv_readfirstlane_b32 %1, %0\n\ts_nop 0\n\ts_cmp_ge_u32 %1, %4\n\ts_cbranch_scc1 2f\n\t"
               "s_sub_u32 %2, %2, 1\n\ts_cbranch_scc1 2f\n\ts_sleep 1\n\ts_branch 1b\n2:"
               : "=&v"(v), "=&s"(sv), "+s"(cnt) : "v"((u32)(size_t)p), "s"(want) : "scc", "memory");
}
DI void poll_tag(char* ws, u32 off, u32 tag, bool done) {
#pragma nounroll
  for (u32 s = 0; s < SPIN_CAP; ++s) { const u32 v = gload(ws, off); if (__all(done || v == tag)) break; __builtin_amdgcn_s_sleep(1); }
  asm volatile("" ::: "memory");
}

struct Frags { bf16x8 x[2]; s16x4 wl[5], wh[5]; };

__global__ __launch_bounds__(1024, 4) void fused(const u16* xA, const u16* wA, const u16* wphi, const u16* woA,
                                              const u16* xB, const u16* wB, const u16* woB, u16* out, char* ws) {
  extern __shared__ __attribute__((aligned(16))) char smem[];
  unsigned long long pc0; u32 len;
  asm volatile("s_getpc_b64 %0\n.Lcode_ref:\n\ts_mov_b32 %1, .Lcode_end-.Lcode_ref" : "=s"(pc0), "=s"(len));
  lds_ptr lds = (lds_ptr)smem;
  const u32 lds0 = (u32)(size_t)lds;
  const int b = blockIdx.x, xcd = b & 7, idx = b >> 3;
  const int br = idx >> 4, rt = (xcd & 3) * 4 + ((idx >> 2) & 3), hg = (xcd >> 2) * 4 + (idx & 3);
  int lane = threadIdx.x & 63; const int wid = RFL(threadIdx.x >> 6);
  int g = lane >> 4, i16 = lane & 15, q4 = i16 >> 2, p4 = lane & 3, h = lane >> 5, l32 = lane & 31;
  auto relane = [&]() { PIN(lane); lane &= 63; g = lane >> 4; i16 = lane & 15; q4 = i16 >> 2; p4 = lane & 3; h = lane >> 5; l32 = lane & 31; };
  const rsrc_t rws = __builtin_amdgcn_make_buffer_rsrc((void*)ws, (short)0, W_TOTAL, 0x00020000);
  lds_ptr misc = lds + L_MISC;
  if (wid < 2) {
    const rsrc_t rc = __builtin_amdgcn_make_buffer_rsrc((void*)(pc0 & ~15ull), (short)0, len & ~15u, 0x00020000);
    dma16(rc, (wid * 32 + idx) * 1024 + lane * 16, 0, lds0 + L_DUMMY);
  }

  const rsrc_t rx = __builtin_amdgcn_make_buffer_rsrc((void*)((br ? xB : xA) + (size_t)rt * 128 * D_), (short)0, 128 * D_ * 2, 0x00020000);
  const rsrc_t rw = __builtin_amdgcn_make_buffer_rsrc((void*)(br ? wB : wA), (short)0, D_ * N3 * 2, 0x00020000);
  const int cls = ((idx >> 2) + idx) & 3;
  const unsigned long long orda = (cls & 1) ? 0x8796435201ull : 0x9768532410ull, ordb = (cls & 1) ? 0x9865742130ull : 0x9875463102ull, ord = (cls & 2) ? ordb : orda;
  auto slice = [&](int s) { return (int)(ord >> (4 * s)) & 15; };
  const int rot = slice(0);
  const u32 vx0 = (lane >> 3) * 1280 + (((lane & 7) ^ (lane >> 4)) << 4), vx1 = vx0 ^ 64;
  const int wc = lane >= 30 ? lane - 30 : lane;
  const u32 vw = (lane >= 30 ? 3840 : 0) + (wc >= 20 ? 2240 : wc >= 10 ? 1120 : 0) + hg * 160 + wc * 16;
  auto ldx = [&](int i, int kt, u32 buf) { dma16(rx, (i & 1) ? vx1 : vx0, 128 * kt + i * 8 * 1280, buf + i * 1024); };
  auto ldw1 = [&](int j, int kt, u32 buf) { const int p = 2 * j, k = (p & 0x33) | ((p & 4) << 1) | ((p & 8) >> 1); dma16(rw, vw, (64 * kt + k) * 3840, buf + XB + j * 960); };
  const u32 og = W_OG + br * (128 * 20480);
  auto nanfill = [&](int i) {
    const u32 o_ = i < 16 ? og + (rt * 8 + (i >> 1)) * 20480 + hg * 2048 + (i & 1) * 1024 + lane * 16
                          : og + (rt * 8 + 2 * (i - 16) + h) * 20480 + 16384 + hg * 512 + l32 * 16;
    __builtin_amdgcn_raw_buffer_store_b128(u32x4{~0u, ~0u, ~0u, ~0u}, rws, o_, 0, 16);
  };
  u32 pc[9] = {};
  auto pre = [&]() {
    auto og_main = [&](int row, int hd, int hf) -> u32 { return og + (row >> 4) * 20480 + ((hg * 2 + (hd >> 1)) << 10) + (((2 * (hd & 1) + hf) * 16 + (row & 15)) << 4); };
    auto og_tail = [&](int row, int hd) -> u32 { return og + (row >> 4) * 20480 + 16384 + hg * 512 + ((hd * 16 + (row & 15)) << 3); };
    if (br) {
      const int hd = wid & 3, qi = wid < 8 ? 1 - (wid >> 2) : wid >> 2, u = 35 - l32 + 4 * h; const bool vones = (g & 1) && p4;
      pc[0] = L_Q + hd * 6144 + (32 * qi + l32) * 48 + 16 * h;
      pc[1] = hd * 2560 + l32 * 80 + 16 * h;
      pc[2] = h ? L_ONES : hd * 2560 + l32 * 80 + 32;
      pc[3] = vones ? L_ONES : hd * 2560 + (4 * h + q4) * 80 + 40 + 32 * (g & 1) + 8 * p4;
      pc[4] = h ? 0 : 10240; pc[5] = vones ? 0 : 10240;
      pc[6] = L_MASK + (u & 3) * 256 + (u & ~3) * 4;
      pc[7] = og_main(rt * 128 + 32 * qi + l32, hd, h);
      pc[8] = h ? OOB : og_tail(rt * 128 + 32 * qi + l32, hd);
      pc[0] += lds0; pc[1] += lds0; pc[2] += lds0; pc[3] += lds0; pc[6] += lds0;
    } else {
      const int tg = wid & 3, pair = (wid >> 2) & 1, isq = wid >> 3;
      pc[0] = (isq ? L_AQ : L_AK) + (32 * tg + l32) * 176 + pair * 80 + 16 * h;
      pc[1] = L_WTAB + pair * 5120 + lane * 16;
      pc[2] = L_WTAB + pair * 5120 + 2048 + lane * 8;
      if (isq) {
        const int hp = (l32 >> 2) & 1, r = (l32 & 3) + 4 * (l32 >> 3);
        pc[3] = (h == hp) ? L_KVT + (2 * pair + h) * 1680 + r * 80 : L_ZERO;
        pc[4] = (h == hp && r <= 4) ? L_KVT + (2 * pair + h) * 1680 + (16 + r) * 80 : L_ZERO;
        pc[5] = og_main(rt * 128 + 32 * tg + l32, 2 * pair + h, 0);
        pc[6] = og_tail(rt * 128 + 32 * tg + l32, 2 * pair + h);
        pc[7] = 0;
      } else {
        const int hd = 2 * pair + (tg >> 1), dt = tg & 1;
        pc[3] = L_AV + hd * 6144 + (8 * g + q4) * 48 + 32 * dt + 8 * p4;
        pc[4] = L_PK + (2 * pair + h) * 10240 + (32 * tg + l32) * 80;
        pc[5] = L_PK + hd * 10240 + (8 * g + q4) * 80 + 8 * p4;
        pc[6] = (!dt || i16 <= 4) ? W_PART + ((hg * 4 + hd) * 16 + rt) * PARTB + (dt * 160 + (dt ? g * 5 + i16 : lane)) * 16 : OOB;
        pc[7] = g < 2 ? pc[6] : OOB;
      }
      pc[0] += lds0; pc[1] += lds0; pc[2] += lds0; pc[3] += lds0; pc[4] += lds0; if (!isq) pc[5] += lds0;
      pc[8] = pc[0] + 64 - 8 * h;
    }
#pragma unroll
    for (int i = 0; i < 9; i++) PIN(pc[i]);
  };
  if (wid < 4) {
    __builtin_amdgcn_s_setprio(3);
    u32 ep = 0;
    if (wid == 3) ep = gload(ws, W_EPOCH);
    if (wid == 2) *(LAS u32*)(misc + lane * 4) = 0;
    int kt = rot, bi = 0;
    __builtin_amdgcn_s_barrier();
#pragma nounroll
    for (int s = 1; s < 10; s++) {
      bi = bi == 2 ? 0 : bi + 1; kt = slice(s);
      const u32 buf = lds0 + bi * STB;
      if (s == 2 && br == 0) {
        const rsrc_t rp = __builtin_amdgcn_make_buffer_rsrc((void*)(wphi + hg * 1600), (short)0, 3200, 0x00020000);
        dma16(rp, wid * 1024 + lane * 16, 0, lds0 + L_RAW + wid * 1024);
      }
#pragma unroll
      for (int i = 0; i < 4; i++) ldx(wid * 4 + i, kt, buf);
      if (lane < 60) {
#pragma unroll
        for (int j = 0; j < 8; j++) ldw1(wid * 8 + j, kt, buf);
      }
      waitv<12>();
      lbar();
      if (s == 6 && wid == 3 && lane == 0) *(LAS u32*)(misc + M_TAG) = ep * 0x9E3779B1u + 0x7F4A7C15u;
      if (s == 9) pre();
      if (s == 9 && br == 0) {
        const int pair = wid >> 1, T = wid & 1, rb = g & 1;
        const int fb = 2 * rb + (p4 >> 1);
        const bool okT = (T == 0) || (fb == 0);
        const int f0 = T ? 16 : 4 * fb;
        s16x4 r[5];
#pragma unroll
        for (int u = 0; u < 5; u++) {
          const int cg = (u < 4) ? 4 * (u >> 1) + 2 * h + (u & 1) : 8 + h;
          const int hc = cg >= 5, d0 = 4 * (cg - 5 * hc);
          const bool ok = okT && (hc == (p4 & 1));
          r[u] = trrd(lds + (ok ? L_RAW + (2 * pair + hc) * 800 + (d0 + q4) * 40 + f0 * 2 : L_ZERO));
        }
        lds_ptr tb = lds + L_WTAB + wid * 2560;
        *(LAS bf16x8*)(tb + lane * 16) = cat8(r[0], r[1]);
        *(LAS bf16x8*)(tb + 1024 + lane * 16) = cat8(r[2], r[3]);
        *(LAS s16x4*)(tb + 2048 + lane * 8) = r[4];
      }
    }
    {
      const rsrc_t rwo = __builtin_amdgcn_make_buffer_rsrc((void*)(br ? woB : woA), (short)0, D_ * D_ * 2, 0x00020000);
      waitv<0>();
      lbar();
      if (br) {
#pragma nounroll
        for (int j = 0; j < 5; j++) nanfill(wid * 5 + j);
      }
      const int ln = (idx & 15) * 200 + wid * 50 + (lane < 50 ? lane : 49);
      dma16(rwo, (ln / 5) * 1280 + (hg >> 2) * 640 + (ln % 5) * 128, 0, lds0 + L_DUMMY);
    }
    if (br) {
      waitv<1>();
      if (lane == 0 && lds_add_ret(misc + M_C4) == 3) gstore(ws, W_STARTED + ((br * 16 + rt) * 8 + hg) * 4, lds_word(misc + M_TAG));
    }
  } else {
    const int cw = wid - 4, mi = cw & 3, nj = cw < 8 ? (cw >> 2) + 1 : 0;
    if (cw < 4) {
#pragma unroll
      for (int i = 0; i < 4; i++) ldx(cw * 4 + i, rot, lds0);
    } else if (lane < 60) {
#pragma unroll
      for (int j = 0; j < 4; j++) ldw1((cw - 4) * 4 + j, rot, lds0);
    }
    __builtin_amdgcn_s_barrier();
    if (cw < 8) {
      const int c = cw & 3, x = lane + c - 35;
      *(LAS float*)(lds + L_MASK + cw * 256 + lane * 4) = ((x > 0) != (cw >= 4)) ? -1e30f : 0.f;
    } else if (cw == 8 && lane < 4) *(LAS u32x2*)(lds + L_ONES + lane * 640) = u32x2{0x3F803F80u, 0x3F803F80u};
    pre();
    const int row = mi * 32 + i16;
    const u32 xa0 = row * 128 + ((g ^ ((row >> 1) & 7)) << 4), xa1 = xa0 ^ 64;
    const u32 wa = XB + (16 * (g >> 1) + 4 * (g & 1) + q4) * 480 + 8 * p4 + nj * 160;
    int base, HS, RS;
    if (br) {
      if (nj == 0) { base = L_Q; HS = 6144; RS = 48; }
      else { base = L_OWN + (nj - 1) * 40 + mi * 7680; HS = 2560; RS = 80; }
    } else {
      if (nj == 2) { base = L_AV; HS = 6144; RS = 48; }
      else { base = nj ? L_AK : L_AQ; HS = 40; RS = 176; }
    }
    int ea[5];
#pragma unroll
    for (int n = 0; n < 5; n++) { const int c4 = 4 * n + g, hd = (c4 * 13) >> 6, d4 = c4 - 5 * hd; ea[n] = lds0 + base + hd * HS + row * RS + d4 * 8; PIN(ea[n]); }
    f32x4 acc[2][5] = {};
    auto rd = [&](Frags& f, lds_ptr bb, int ks, int j) {
      if (j < 2) f.x[j] = ld128(bb + (ks ? xa1 : xa0) + j * 2048);
      else { int n = (j - 2) >> 1; if (j & 1) f.wh[n] = trrd(bb + wa + ks * 15360 + 3840 + n * 32); else f.wl[n] = trrd(bb + wa + ks * 15360 + n * 32); }
    };
#define MM(F, i) acc[(i) & 1][(i) >> 1] = MFMA16(cat8(F.wl[(i) >> 1], F.wh[(i) >> 1]), F.x[(i) & 1], acc[(i) & 1][(i) >> 1])
    Frags F0, F1;
    int boff = 0;
    __builtin_amdgcn_s_setprio(3);
    waitv<0>();
    lbar();
#pragma unroll
    for (int j = 0; j < 12; j++) rd(F0, lds, 0, j);
#pragma unroll
    for (int t = 0; t < 10; t++) {
      SBAR();
#pragma unroll
      for (int i = 0; i < 10; i++) { MM(F0, i); if (i < 6) { rd(F1, lds + boff, 1, 2 * i); rd(F1, lds + boff, 1, 2 * i + 1); } SBAR(); }
      boff = ((t + 1) % 3) * STB; asm volatile("" : "+s"(boff));
      if (t < 9) {
        __builtin_amdgcn_s_waitcnt(0xC07F);
        asm volatile("s_barrier" : "+v"(acc[0][0]), "+v"(acc[1][0]), "+v"(acc[0][1]), "+v"(acc[1][1]), "+v"(acc[0][2]), "+v"(acc[1][2]), "+v"(acc[0][3]), "+v"(acc[1][3]), "+v"(acc[0][4]), "+v"(acc[1][4]) :: "memory");
      } else {
        lds_inc(misc + M_C8, lane);
        asm volatile("" : "+v"(acc[0][0]), "+v"(acc[1][0]), "+v"(acc[0][1]), "+v"(acc[1][1]), "+v"(acc[0][2]), "+v"(acc[1][2]), "+v"(acc[0][3]), "+v"(acc[1][3]), "+v"(acc[0][4]), "+v"(acc[1][4]));
      }
      asm volatile("" ::: "memory");
#pragma unroll
      for (int i = 0; i < 10; i++) {
        if (t < 9 && i < 6) { rd(F0, lds + boff, 0, 2 * i); rd(F0, lds + boff, 0, 2 * i + 1); }
        MM(F1, i); SBAR();
      }
    }
    relane();
    u32x2 pk[2][5];
    if (br && nj == 0) {
#pragma unroll
      for (int i = 0; i < 10; i++) { const f32x4 v = acc[i & 1][i >> 1] * (0.22360679774997896f * 1.4426950408889634f); pk[i & 1][i >> 1] = u32x2{cvtpk(v[0], v[1]), cvtpk(v[2], v[3])}; }
    } else {
#pragma unroll
      for (int i = 0; i < 10; i++) { const f32x4 v = acc[i & 1][i >> 1]; pk[i & 1][i >> 1] = u32x2{cvtpk(v[0], v[1]), cvtpk(v[2], v[3])}; asm volatile("" ::: "memory"); }
    }
    int eb[5];
#pragma unroll
    for (int n = 0; n < 5; n++) { eb[n] = ea[n] + 16 * RS; PIN(eb[n]); }
#pragma unroll
    for (int n = 0; n < 5; n++) { *(LAS u32x2*)LP(ea[n]) = pk[0][n]; *(LAS u32x2*)LP(eb[n]) = pk[1][n]; }
    if (br == 0 && nj == 2) {
#pragma unroll
      for (int m = 0; m < 2; m++) *(LAS u32x2*)(lds + L_AV + g * 6144 + (mi * 32 + i16 + 16 * m) * 48 + 40) = u32x2{0x3F803F80u, 0x3F803F80u};
    }
    lds_inc(misc + M_WR + nj * 16 + mi * 4, lane);
    if (nj == 2) lds_inc(misc + M_WV, lane);
    lds_inc(misc + M_C9, lane);
    if (br && nj == 1) {
      lds_wait_ge(misc + M_WR + 32 + mi * 4, 1u);
      if (mi < 2) __builtin_amdgcn_s_sleep(6);
      const u32 dst = W_KVG + ((rt * 8 + hg) * 4 + mi) * 10240 + lane * 16;
      u32x4 v[10];
#pragma unroll
      for (int i = 0; i < 10; i++) v[i] = *(LAS u32x4*)(lds + L_OWN + mi * 10240 + i * 1024 + lane * 16);
      SBAR();
#pragma unroll
      for (int i = 0; i < 10; i++) __builtin_amdgcn_raw_buffer_store_b128(v[i], rws, dst, i * 1024, 16);
      waitv<0>();
      if (lane == 0) gstore(ws, W_KVFLAG + (rt * 8 + hg) * 128 + mi * 4, lds_word(misc + M_TAG));
      if (mi >> 1) lds_inc(misc + M_P1, lane);
    } else if (br) __builtin_amdgcn_s_setprio(0);
  }
  relane();
  f32x16 o = {};
  PIN(o);
  if (br) lds_wait_ge(misc + M_C9, 12u);
  const u32 tag = RFL(lds_word(misc + M_TAG));
  const int l_wo = br ? LB_WO : LA_WO;
  const rsrc_t rwo = __builtin_amdgcn_make_buffer_rsrc((void*)(br ? woB : woA), (short)0, D_ * D_ * 2, 0x00020000);
  u32 wvm[2] = {}, wvt[2] = {};
  const int wj = wid & 3;
  auto wo_init = [&]() {
#pragma unroll
    for (int u = 0; u < 2; u++) {
      int c = (u ? 4 : wj) * 64 + lane; PIN(c);
      const int p = c / 10, ch = c - p * 10;
      const int gg = ((p >> 3) & 2) | ((p >> 2) & 1), tt = (p >> 3) & 1, e = p & 3;
      wvm[u] = ((gg >> 1) * 20 + 4 * (gg & 1) + 8 * tt + e) * 1280 + hg * 160 + ch * 16; wvt[u] = (tt * 80 + gg * 20 + 16 + e) * 1280 + hg * 160 + ch * 16;
    }
  };
  auto wo_dma = [&](int k0, int k1) {
    if (k1 == 20) {
#pragma unroll
      for (int ks = 16; ks < 20; ks++) dma16(rwo, wvt[0], (ks - 16) * 160 * 1280, lds0 + l_wo + ks * 5120 + wj * 1024);
      dma16(rwo, wvt[1], wj * 160 * 1280, lds0 + l_wo + (16 + wj) * 5120 + 4096);
      k1 = 16;
    }
#pragma nounroll
    for (int ks = k0; ks < k1; ks++) {
      dma16(rwo, wvm[0], ks * 40 * 1280, lds0 + l_wo + ks * 5120 + wj * 1024);
      if ((ks & 3) == wj) dma16(rwo, wvm[1], ks * 40 * 1280, lds0 + l_wo + ks * 5120 + 4096);
    }
  };
  const bool wow = wid >= 12;
  auto guard = [&]() { poll_tag(ws, W_STARTED + ((br * 16 + rt) * 8 + (lane & 7)) * 4, tag, false); lds_inc(misc + M_C6, lane); };
  const u32 ofl = W_OFLAG + (br * 16 + rt) * 128;
  auto published = [&](int rg) {
    waitv<0>();
    lds_inc(misc + M_D + rg * 4, lane);
    if (lane == 0 && lds_add_ret(misc + M_C2) == (br ? 15 : 7)) gstore(ws, ofl + hg * 4, tag);
  };

  if (br) {
    const int hd = wid & 3, qi = wid < 8 ? 1 - (wid >> 2) : wid >> 2;
    const u32 kvf = W_KVFLAG + (rt * 8 + hg) * 128;
    const bf16x8 qa = ld128(LP(pc[0]));
    s16x4 qb = ld64(LP(pc[0]) + 32);
    if (h) qb = s16x4{0, 0, 0, 0};
    auto ldmask = [&](int pol) -> f32x16 {
      f32x4 t[4];
#pragma unroll
      for (int j = 0; j < 4; j++) t[j] = *(LAS f32x4*)(LP(pc[6]) + pol * 1024 + j * 32);
      return __builtin_shufflevector(__builtin_shufflevector(t[0], t[1], 0, 1, 2, 3, 4, 5, 6, 7), __builtin_shufflevector(t[2], t[3], 0, 1, 2, 3, 4, 5, 6, 7),
                                     0, 1, 2, 3, 4, 5, 6, 7, 8, 9, 10, 11, 12, 13, 14, 15);
    };
    auto pk4 = [&](const f32x16& s, int o_) -> bf16x8 {
      u32x4 p;
#pragma unroll
      for (int j = 0; j < 4; j++) p[j] = cvtpk(ex2(s[o_ + 2 * j]), ex2(s[o_ + 2 * j + 1]));
      return __builtin_bit_cast(bf16x8, p);
    };
    lds_ptr ka_p, kb_p, vp;
    auto seek = [&](int row) { ka_p = LP(pc[1]) + row * 10240; kb_p = LP(pc[2]) + row * pc[4]; vp = LP(pc[3]) + row * pc[5]; };
    f32x16 s, s2; s16x4 c;
    auto step = [&](f32x16& sc, f32x16& sn) {
      ka_p -= 10240; kb_p -= pc[4];
      const bf16x8 ka = ld128(ka_p); const s16x4 kb = ld64(kb_p);
      const bf16x8 v0 = cat8(trrd(vp), trrd(vp + 640)), v1 = cat8(trrd(vp + 1280), trrd(vp + 1920));
      vp -= pc[5];
      SBAR();
      float e[8]; u32x4 p1;
      bf16x8 p0 = pk4(sc, 0); PIN(p0);
      SBAR(); sn = MFMA32(ka, qa, f32x16{}); SBAR();
#pragma unroll
      for (int j = 0; j < 4; j++) e[j] = ex2(sc[8 + j]);
      SBAR(); sn = MFMA32K8(kb, c, sn); SBAR();
#pragma unroll
      for (int j = 4; j < 8; j++) e[j] = ex2(sc[8 + j]);
      SBAR(); o = MFMA32(v0, p0, o); SBAR();
#pragma unroll
      for (int j = 0; j < 4; j++) p1[j] = cvtpk(e[2 * j], e[2 * j + 1]);
      SBAR(); o = MFMA32(v1, __builtin_bit_cast(bf16x8, p1), o); SBAR();
    };
    auto last = [&](f32x16& sc) {
      const bf16x8 v0 = cat8(trrd(vp), trrd(vp + 640)), v1 = cat8(trrd(vp + 1280), trrd(vp + 1920));
      o = MFMA32(v0, pk4(sc, 0), o);
      o = MFMA32(v1, pk4(sc, 8), o);
    };
    lds_inc(misc + M_C5, lane);
    seek(L_OWN / 10240 + qi);
    {
      const s16x4 kb = ld64(kb_p);
      s = MFMA32K8(kb, qb, MFMA32(ld128(ka_p), qa, ldmask(0)));
      float mm = fmaxf(s[0], s[1]);
#pragma unroll
      for (int r = 2; r < 16; r += 2) mm = fmaxf(fmaxf(mm, s[r]), s[r + 1]);
      auto rr = __builtin_amdgcn_permlane32_swap(__float_as_uint(mm), __float_as_uint(mm), false, false);
      const u32 nm = cvtpk(-fmaxf(__uint_as_float(rr[0]), __uint_as_float(rr[1])), 0.f);
      const u32x2 qw = __builtin_bit_cast(u32x2, qb);
      c = __builtin_bit_cast(s16x4, u32x2{h ? nm : qw.x, qw.y});
      s = MFMA32K8(kb, __builtin_bit_cast(s16x4, u32x2{h ? nm : 0u, 0u}), s);
    }
    if (qi >= 2) { step(s, s2); step(s2, s); }
    if (qi & 1) { step(s, s2); last(s2); } else last(s);
    lds_inc(misc + M_C1, lane);
    if (wid < 4) {
      const int dd = 2 - (wid >> 1);
      bool seen = false;
#pragma nounroll
      for (int hf = 1; hf >= 0; --hf) {
        const int tl = 2 * hf + (wid & 1);
        if (hf) lds_wait_ge(misc + M_P1, 2u);
        else {
          const u32 v = gload(ws, kvf - (rt >= dd ? dd : 0) * 1024 + tl * 4);
          lds_inc(misc + M_P0, lane);
          waitv<0>();
          seen = RFL(v) == tag;
          lds_inc(misc + M_C0, lane);
        }
        if (rt >= dd && !seen) poll_tag(ws, kvf - dd * 1024 + tl * 4, tag, false);
        if (rt >= dd) {
          const u32 src = W_KVG + (((rt - dd) * 8 + hg) * 4 + tl) * 10240;
#pragma unroll
          for (int i = 0; i < 10; i++) dma16c(rws, lane * 16, src + i * 1024, lds0 + L_HALO + hf * 40960 + wid * 10240 + i * 1024);
        }
      }
    }
    if (wid >= 8) wo_init();
    if (qi == 2) { lds_wait_ge(misc + M_C5, 16u); lds_wait_ge(misc + M_P0, 4u); wo_dma(13, 20); }
    lds_wait_ge(misc + M_C0, 4u);
    int wk = 13;
    if (rt > 0) {
      int n1 = 2, nn = 4, km = 0;
      if (rt >= 2) { n1 = qi < 3 ? 4 : 3; nn = 8 - qi; km = qi >= 2 ? n1 - 1 : nn - 1; }
      const u32 ev1 = (1u << n1) | (wid < 4 ? 4u : 0u) | (wow ? ~0u : 0u);
      const u32 ev2 = (1u << n1) | (1u << km);
      if (wow) __builtin_amdgcn_s_setprio(3); else __builtin_amdgcn_s_setprio(2);
      seek(7);
      s = MFMA32K8(ld64(kb_p), c, MFMA32(ld128(ka_p), qa, f32x16{}));
      auto before = [&](int k) {
        if (k == 2 && wid < 4) { waitv<0>(); lds_inc(misc + M_C7, lane); }
        if (k == n1) {
          ka_p -= (4 - n1) * 10240; kb_p -= (4 - n1) * pc[4];
          lds_wait_ge(misc + M_C7, 4u);
          if (qi == 2) { waitv<0>(); lds_inc(misc + M_C6, lane); }
          if (wid == 10) guard();
        }
        if (wow && wk > 0) {
          const u32 c1 = RFL(lds_word(misc + M_C1)), c7 = RFL(lds_word(misc + M_C7)), c3 = RFL(lds_word(misc + M_C3));
          const int lim = c3 >= 16 ? 0 : (c1 >= 16 && c7 >= 4) ? 5 : 20;
          const int k0 = wk - 8 > lim ? wk - 8 : lim;
          if (k0 < wk) { wo_dma(k0, wk); wk = k0; }
        }
      };
      auto after = [&](int k, f32x16& sn) {
        if (k == km) sn += ldmask(1);
        if (k == n1) { vp -= (4 - n1) * pc[5]; lds_inc(misc + M_C3, lane); if (!wow) __builtin_amdgcn_s_setprio(1); }
      };
#pragma nounroll
      for (int k = 1;; k += 2) {
        if (__builtin_expect((ev1 >> k) & 1, 0)) before(k);
        step(s, s2);
        if (__builtin_expect((ev2 >> k) & 1, 0)) after(k, s2);
        if (__builtin_expect(k + 1 == nn, 0)) { s = s2; break; }
        if (__builtin_expect((ev1 >> (k + 1)) & 1, 0)) before(k + 1);
        step(s2, s);
        if (__builtin_expect((ev2 >> (k + 1)) & 1, 0)) after(k + 1, s);
        if (__builtin_expect(k + 2 == nn, 0)) break;
      }
      last(s);
    } else {
      if (qi == 2) { waitv<0>(); lds_inc(misc + M_C6, lane); }
      if (wid == 10) guard();
    }
    float l = h ? o[8] : o[12];
    if (__builtin_expect(__any(!(l < 1.8e19f)), 0)) {
      const int dd = lane % 3;
      poll_tag(ws, kvf - (rt - dd < 0 ? 0 : dd) * 1024 + 4 * (lane & 3), tag, false);
      float q[20], a[20], mx = -3e38f; l = 0.f;
      const u32x4 qw = __builtin_bit_cast(u32x4, qa); const u32x2 qv = __builtin_bit_cast(u32x2, qb);
#pragma unroll
      for (int j = 0; j < 10; j++) {
        const u32 w = j < 8 ? __shfl(qw[j & 3], l32 + 32 * (j >> 2)) : __shfl(qv[j & 1], l32);
        q[2 * j] = blo(w); q[2 * j + 1] = bhi(w); a[2 * j] = 0.f; a[2 * j + 1] = 0.f;
      }
      const int row = rt * 128 + 32 * qi + l32;
#pragma nounroll
      for (int t = 0; t < 128; ++t) {
        const int j = row - 2 * t - h;
        if (j >= 0) {
          const u32 src = W_KVG + (((j >> 7) * 8 + hg) * 4 + ((j >> 5) & 3)) * 10240 + hd * 2560 + (j & 31) * 80;
          u32 w[20];
#pragma unroll
          for (int i = 0; i < 5; i++) { const u32x4 v = __builtin_amdgcn_raw_buffer_load_b128(rws, src, i * 16, 16); w[4 * i] = v.x; w[4 * i + 1] = v.y; w[4 * i + 2] = v.z; w[4 * i + 3] = v.w; }
          float sc = 0.f;
#pragma unroll
          for (int i = 0; i < 10; i++) sc += q[2 * i] * blo(w[i]) + q[2 * i + 1] * bhi(w[i]);
          const float mn = fmaxf(mx, sc), f = ex2(mx - mn), p = ex2(sc - mn);
          mx = mn; l = l * f + p;
#pragma unroll
          for (int i = 0; i < 10; i++) { a[2 * i] = a[2 * i] * f + p * blo(w[10 + i]); a[2 * i + 1] = a[2 * i + 1] * f + p * bhi(w[10 + i]); }
        }
      }
      const float mo = __shfl_xor(mx, 32), mn = fmaxf(mx, mo), f = ex2(mx - mn);
      l *= f; l += __shfl_xor(l, 32);
#pragma unroll
      for (int i = 0; i < 20; i++) { a[i] *= f; a[i] += __shfl_xor(a[i], 32); }
#pragma unroll
      for (int r = 0; r < 8; r++) o[r] = h ? a[(r & 3) + 8 * (r >> 2) + 4] : a[(r & 3) + 8 * (r >> 2)];
#pragma unroll
      for (int r = 8; r < 12; r++) o[r] = a[8 + r];
    }
    const float rl = __builtin_amdgcn_rcpf(l);
    const u32x4 w0 = {cvtpk(o[0] * rl, o[1] * rl), cvtpk(o[2] * rl, o[3] * rl), cvtpk(o[4] * rl, o[5] * rl), cvtpk(o[6] * rl, o[7] * rl)};
    const u32x2 w1 = {cvtpk(o[8] * rl, o[9] * rl), cvtpk(o[10] * rl, o[11] * rl)};
    __builtin_amdgcn_raw_buffer_store_b128(w0, rws, pc[7], 0, 16);
    __builtin_amdgcn_raw_buffer_store_b64(w1, rws, pc[8], 0, 16);
    lds_inc(misc + M_AD, lane);
    if (wow && wk > 5) { lds_wait_ge(misc + M_C1, 16u); wo_dma(5, wk); wk = 5; }
    if (wid >= 8) published(qi);
    if (wow) {
      lds_inc(misc + M_C6, lane);
      if (wk > 0) { if (rt) lds_wait_ge(misc + M_C3, 16u); wo_dma(0, wk); waitv<0>(); }
      lds_inc(misc + M_C6B, lane);
    }
    __builtin_amdgcn_s_setprio(0);
  } else {
    const int tg = wid & 3, pair = (wid >> 2) & 1, isq = wid >> 3;
    u32 phi[20];
    {
      lds_wait_ge(misc + M_WR + (isq ? 0 : 16) + tg * 4, 1u);
      const bf16x8 b0 = ld128(LP(pc[0])), a00 = ld128(LP(pc[1])), a10 = ld128(LP(pc[1]) + 2560);
      const bf16x8 b1 = ld128(LP(pc[0]) + 32), a01 = ld128(LP(pc[1]) + 1024), a11 = ld128(LP(pc[1]) + 2560 + 1024);
      const s16x4 b2 = ld64(LP(pc[8])), a02 = ld64(LP(pc[2])), a12 = ld64(LP(pc[2]) + 2560);
      lds_inc(misc + M_C5, lane);
      if (wow) { wo_init(); lds_wait_ge(misc + M_C5, 16u); wo_dma(6, 20); waitv<0>(); lds_inc(misc + M_C6, lane); }
      if (isq) { __builtin_amdgcn_s_setprio(0); lds_wait_ge(misc + M_C0 + 4, 4u); } else __builtin_amdgcn_s_setprio(2);
      f32x16 t0 = MFMA32(a00, b0, f32x16{}), t1 = MFMA32(a10, b0, f32x16{});
      t0 = MFMA32(a01, b1, t0); t1 = MFMA32(a11, b1, t1);
      t0 = MFMA32K8(a02, b2, t0); t1 = MFMA32K8(a12, b2, t1);
      float p[20];
#pragma unroll
      for (int j = 0; j < 10; j++) {
        const u32 w = j < 8 ? cvtpk(t0[2 * j], t0[2 * j + 1]) : cvtpk(t1[2 * j - 16], t1[2 * j - 15]);
        p[2 * j] = blo(w); p[2 * j + 1] = bhi(w);
      }
      float mx = fmaxf(p[0], p[1]), mn = fminf(p[0], p[1]);
#pragma unroll
      for (int j = 2; j < 20; j += 2) { mx = fmaxf(fmaxf(mx, p[j]), p[j + 1]); mn = fminf(fminf(mn, p[j]), p[j + 1]); }
      const float L2E = 1.4426950408889634f;
      const f32x2 sl = {L2E, -L2E}, of = {-mx * L2E, mn * L2E};
      f32x2 e[20], sa = {0.f, 0.f}, sb = {0.f, 0.f};
#pragma unroll
      for (int j = 0; j < 20; j++) {
        const f32x2 a = __builtin_elementwise_fma(f32x2{p[j], p[j]}, sl, of);
        e[j] = f32x2{ex2(a[0]), ex2(a[1])};
        if (j & 1) sb += e[j]; else sa += e[j];
      }
      sa += sb;
      const f32x2 rr = {__builtin_amdgcn_rcpf(sa[0]), __builtin_amdgcn_rcpf(sa[1])};
#pragma unroll
      for (int j = 0; j < 20; j++) phi[j] = cvtpk2(e[j] * rr);
    }
    if (!isq) {
      lds_wait_ge(misc + M_WV, 4u);
      bf16x8 vb[4];
#pragma unroll
      for (int ks = 0; ks < 4; ks++) vb[ks] = cat8(trrd(LP(pc[3]) + ks * 32 * 48), trrd(LP(pc[3]) + ks * 32 * 48 + 4 * 48));
      lds_wait_ge(misc + M_C8, 12u);
#pragma unroll
      for (int j = 0; j < 5; j++) *(LAS u32x4*)(LP(pc[4]) + 16 * j) = u32x4{phi[4 * j], phi[4 * j + 1], phi[4 * j + 2], phi[4 * j + 3]};
      lds_inc(misc + M_C0 + pair * 4, lane);
      lds_wait_ge(misc + M_C0 + pair * 4, 4u);
      f32x4 kv[3] = {};
#pragma unroll
      for (int ks = 0; ks < 4; ks++)
#pragma unroll
        for (int T = 0; T < 3; T++) kv[T] = MFMA16(cat8(trrd(LP(pc[5]) + ks * 2560 + T * 32), trrd(LP(pc[5]) + ks * 2560 + 320 + T * 32)), vb[ks], kv[T]);
#pragma unroll
      for (int T = 0; T < 3; T++)
        __builtin_amdgcn_raw_buffer_store_b128(u32x4{cvtpk(kv[T][0], kv[T][1]), tag, cvtpk(kv[T][2], kv[T][3]), tag}, rws, pc[T < 2 ? 6 : 7], T * ((tg & 1) ? 320 : 1024), 16);
      lds_inc(misc + M_C1, lane);
      if (wid == 0) {
        const int sl = (rt & 3) * 53 + lane; const bool act = lane < 53 && sl < 210;
        const u32 src = act ? W_PART + (hg * 4 + (rt >> 2)) * 16 * PARTB + sl * 16 : OOB;
        float s0 = 0.f, s1 = 0.f, s2 = 0.f, s3 = 0.f;
        lds_wait_ge(misc + M_C6, 4u);
        lds_wait_ge(misc + M_C7, 4u);
#pragma nounroll
        for (u32 s = 0; s < SPIN_CAP; ++s) {
          bool ok = true; s0 = 0.f; s1 = 0.f; s2 = 0.f; s3 = 0.f;
          u32x4 v[16];
#pragma unroll
          for (int r = 0; r < 16; r++) v[r] = __builtin_amdgcn_raw_buffer_load_b128(rws, src, r * PARTB, 16);
#pragma unroll
          for (int r = 0; r < 16; r++) { ok &= (v[r].y == tag) & (v[r].w == tag); s0 += blo(v[r].x); s1 += bhi(v[r].x); s2 += blo(v[r].z); s3 += bhi(v[r].z); }
          if (__all(ok || !act)) break;
          asm volatile("" ::: "memory");
        }
        __builtin_amdgcn_raw_buffer_store_b128(u32x4{cvtpk(s0, s1), tag, cvtpk(s2, s3), tag}, rws, act ? W_SLICE + (hg * 16 + rt) * 1024 + lane * 16 : OOB, 0, 16);
        waitv<0>();
        lds_inc(misc + M_C4, lane);
      } else if (wid == 1) {
#pragma nounroll
        for (int i = 0; i < 20; i++) nanfill(i);
        waitv<0>();
        if (lane == 0) gstore(ws, W_STARTED + ((br * 16 + rt) * 8 + hg) * 4, tag);
        lds_inc(misc + M_C3, lane);
        lds_wait_ge(misc + M_C4, 1u);
        guard();
      } else if (wid >= 4) {
        const int hh = wid - 4;
        const u32 src = W_SLICE + (hg * 16 + hh * 4) * 1024 + lane * 16;
        int da[4];
#pragma unroll
        for (int j = 0; j < 4; j++) {
          const int sl = j * 53 + lane; int d, ph;
          if (sl < 160) { d = sl & 15; ph = (sl >> 4) * 4; }
          else { const int s2 = sl - 160, gg = s2 / 5; d = 16 + s2 - 5 * gg; ph = 4 * gg; }
          da[j] = L_KVT + hh * 1680 + d * 80 + ph * 2;
        }
        waitv<0>(); lds_inc(misc + M_C7, lane);
        wo_init();
        lds_wait_ge(misc + M_C1, 8u);
        lds_wait_ge(misc + M_C5, 16u);
        lds_wait_ge(misc + M_C4, 1u);
        u32x4 v[4];
#pragma nounroll
        for (u32 s = 0; s < SPIN_CAP; ++s) {
          bool ok = true;
#pragma unroll
          for (int r = 0; r < 4; r++) v[r] = __builtin_amdgcn_raw_buffer_load_b128(rws, src, r * 1024, 16);
#pragma unroll
          for (int r = 0; r < 4; r++) ok &= ((v[r].y == tag) & (v[r].w == tag)) | (lane >= (r < 3 ? 53 : 51));
          if (__all(ok)) break;
          asm volatile("" ::: "memory");
        }
#pragma unroll
        for (int r = 0; r < 4; r++) if (lane < (r < 3 ? 53 : 51)) *(LAS u32x2*)(lds + da[r]) = u32x2{v[r].x, v[r].z};
        lds_inc(misc + M_C3, lane);
        wo_dma(0, 6); waitv<0>(); lds_inc(misc + M_C6, lane);
      }
      __builtin_amdgcn_s_setprio(0);
    } else {
      lds_ptr a0 = LP(pc[3]), a1 = LP(pc[4]); const u32 om = pc[5], ot = pc[6];
      lds_wait_ge(misc + M_C3, 5u);
      f32x16 t0 = {}, t1 = {};
#pragma unroll
      for (int j = 0; j < 5; j++) {
        const bf16x8 bq = __builtin_bit_cast(bf16x8, u32x4{phi[4 * j], phi[4 * j + 1], phi[4 * j + 2], phi[4 * j + 3]});
        t1 = MFMA32(ld128(a1 + 16 * j), bq, t1); t0 = MFMA32(ld128(a0 + 16 * j), bq, t0);
      }
      const float rl = __builtin_amdgcn_rcpf(t1[4] + 1e-6f);
      const u32x4 w0 = {cvtpk(t0[0] * rl, t0[1] * rl), cvtpk(t0[2] * rl, t0[3] * rl), cvtpk(t0[8] * rl, t0[9] * rl), cvtpk(t0[10] * rl, t0[11] * rl)};
      const u32x4 w1 = {cvtpk(t0[4] * rl, t0[5] * rl), cvtpk(t0[6] * rl, t0[7] * rl), cvtpk(t0[12] * rl, t0[13] * rl), cvtpk(t0[14] * rl, t0[15] * rl)};
      const u32x2 w2 = {cvtpk(t1[0] * rl, t1[1] * rl), cvtpk(t1[2] * rl, t1[3] * rl)};
      __builtin_amdgcn_raw_buffer_store_b128(w0, rws, om, 0, 16);
      __builtin_amdgcn_raw_buffer_store_b128(w1, rws, om, 256, 16);
      __builtin_amdgcn_raw_buffer_store_b64(w2, rws, ot, 0, 16);
      published(tg);
    }
  }
  relane();
  if (wid >= 8) {
    if (b == 0 && wid == 15) {
#pragma nounroll
      for (u32 s = 0; s < SPIN_CAP; ++s) {
        const u32x4 v = __builtin_amdgcn_raw_buffer_load_b128(rws, W_STARTED + lane * 16, 0, 16);
        if (__all(v.x == tag && v.y == tag && v.z == tag && v.w == tag)) break;
        __builtin_amdgcn_s_sleep(2);
      }
      if (lane == 0) gstore(ws, W_EPOCH, tag);
    }
  } else {
    const int rg = br ? (0x2013 >> (4 * (wid >> 1))) & 3 : wid >> 1, kh = wid & 1;
    u32 obm = og + (rt * 8 + rg * 2) * 20480 + kh * 8192 + lane * 16, obt = og + (rt * 8 + rg * 2) * 20480 + kh * 2048 + lane * 8;
    PIN(obm); PIN(obt);
    const u32 mo[2] = {(u32)kh * 20480u, (u32)(1 - kh) * 20480u};
    u32x4 of[10][2];
    auto ldo = [&](int s) {
#pragma unroll
      for (int m = 0; m < 2; m++) {
        if (s < 8) of[s][m] = __builtin_amdgcn_raw_buffer_load_b128(rws, obm, mo[m] + s * 1024, 16);
        else {
          const u32x2 a = __builtin_amdgcn_raw_buffer_load_b64(rws, obt, mo[m] + (s + 8) * 1024, 16);
          const u32x2 c = __builtin_amdgcn_raw_buffer_load_b64(rws, obt, mo[m] + (s + 8) * 1024 + 512, 16);
          of[s][m] = u32x4{a.x, a.y, c.x, c.y};
        }
      }
    };
    u32 wm = lds0 + l_wo + (16 * (g >> 1) + 4 * (g & 1) + q4) * 160 + 8 * p4, wt = wm + (16 + 2 * kh) * 5120; wm += kh * 8 * 5120;
    PIN(wm); PIN(wt);
    constexpr int WFN = 8;
    bf16x8 wf[WFN];
    auto ldw = [&](int i) { const int s = 9 - i / 5, n = i % 5; lds_ptr p = (s < 8 ? LP(wm) + s * 5120 : LP(wt) + (s - 8) * 5120) + n * 32; wf[i % WFN] = cat8(trrd(p), trrd(p + 1280)); };
    f32x4 ac2[2][5];
    constexpr int PF = 5;
    if (br) published(1 - (wid >> 2));
    lds_wait_ge(misc + M_C6, 9u);
    lds_wait_ge(misc + M_D + rg * 4, br ? 4u : 2u);
#pragma nounroll
    for (int pass = 0;; ++pass) {
      if (pass) poll_tag(ws, ofl + (lane & 7) * 4, tag, false);
#pragma unroll
      for (int i = 0; i < WFN; i++) ldw(i);
#pragma nounroll
      for (int tr = 0; tr < 64; ++tr) {
#pragma unroll
        for (int s = 9; s > 9 - PF; s--) ldo(s);
        asm volatile("" ::: "memory");
        u32 mx = 0;
#pragma unroll
        for (int s = 8; s < 10; s++)
#pragma unroll
          for (int m = 0; m < 2; m++) mx = max(max(mx, max(of[s][m].x, of[s][m].y)), max(of[s][m].z, of[s][m].w));
        if (pass || !__any(mx == ~0u)) break;
      }
#pragma unroll
      for (int n = 0; n < 5; n++) { ac2[0][n] = f32x4{}; ac2[1][n] = f32x4{}; }
#pragma unroll
      for (int s = 9; s >= 0; s--) {
        if (s == 6) lds_wait_ge(misc + M_C6B, br ? 4u : 0u);
        if (s == 1) lds_wait_ge(misc + M_AD, br ? 16u : 0u);
        SBAR();
#pragma unroll
        for (int n = 0; n < 5; n++) {
          const int i = (9 - s) * 5 + n;
          ac2[0][n] = MFMA16(wf[i % WFN], __builtin_bit_cast(bf16x8, of[s][0]), ac2[0][n]);
          ac2[1][n] = MFMA16(wf[i % WFN], __builtin_bit_cast(bf16x8, of[s][1]), ac2[1][n]);
          if (i + WFN < 50) ldw(i + WFN);
          SBAR();
        }
        if (s - PF >= 0) ldo(s - PF);
      }
      const float chk = ac2[0][0][0] + ac2[1][0][0];
      if (pass || !__any(chk != chk)) break;
    }
    relane();
    lds_ptr red = lds + L_RED + lane * 16;
#pragma unroll
    for (int n = 0; n < 5; n++) *(LAS f32x4*)(red + (wid ^ 1) * 5120 + n * 1024) = ac2[1][n];
    lds_inc(misc + M_X + (wid ^ 1) * 4, lane);
    const int r16 = rt * 8 + rg * 2 + kh;
    u32 ob1 = (r16 * 16 + i16) * 2560 + br * 1280 + hg * 160, ob2 = ob1 + g * 8; ob1 += (g & 1) * 32 + (g >> 1) * 16;
    PIN(ob1); PIN(ob2);
    lds_wait_ge(misc + M_X + wid * 4, 1u);
    f32x4 acc[5];
#pragma unroll
    for (int n = 0; n < 5; n++) acc[n] = *(LAS f32x4*)(red + wid * 5120 + n * 1024);
    SBAR();
#pragma unroll
    for (int n = 0; n < 5; n++) acc[n] += ac2[0][n];
    const rsrc_t ro = __builtin_amdgcn_make_buffer_rsrc((void*)out, (short)0, S_ * 1280 * 2, 0x00020000);
#pragma unroll
    for (int n = 0; n < 4; n += 2) {
      const auto x = __builtin_amdgcn_permlane16_swap(cvtpk(acc[n][0], acc[n][1]), cvtpk(acc[n + 1][0], acc[n + 1][1]), false, false);
      const auto y = __builtin_amdgcn_permlane16_swap(cvtpk(acc[n][2], acc[n][3]), cvtpk(acc[n + 1][2], acc[n + 1][3]), false, false);
      __builtin_amdgcn_raw_buffer_store_b128(u32x4{x[0], y[0], x[1], y[1]}, ro, ob1, n * 32, 0);
    }
    __builtin_amdgcn_raw_buffer_store_b64(u32x2{cvtpk(acc[4][0], acc[4][1]), cvtpk(acc[4][2], acc[4][3])}, ro, ob2, 128, 0);
  }
  asm volatile(".Lcode_end:");
}

extern "C" void kernel_launch(void* const* d_in, const int* in_sizes, int n_in, void* d_out, int out_size, void* d_ws, size_t ws_size, hipStream_t stream) {
  (void)hipFuncSetAttribute((const void*)fused, hipFuncAttributeMaxDynamicSharedMemorySize, LDS_BYTES);
  fused<<<256, 1024, LDS_BYTES, stream>>>((const u16*)d_in[0], (const u16*)d_in[1], (const u16*)d_in[2], (const u16*)d_in[3],
                                          (const u16*)d_in[4], (const u16*)d_in[5], (const u16*)d_in[6], (u16*)d_out, (char*)d_ws);
}
```

```cpp
#include <hip/hip_runtime.h>
#include <hip/hip_bf16.h>
#include <type_traits>

typedef unsigned short u16;
typedef unsigned u32;
typedef short bf16x8 __attribute__((ext_vector_type(8)));
typedef short s16x4 __attribute__((ext_vector_type(4)));
typedef float f32x4 __attribute__((ext_vector_type(4)));
typedef float f32x16 __attribute__((ext_vector_type(16)));
typedef float f32x2 __attribute__((ext_vector_type(2)));
typedef __bf16 bf2_t __attribute__((ext_vector_type(2)));
typedef u32 u32x4 __attribute__((ext_vector_type(4)));
typedef u32 u32x2 __attribute__((ext_vector_type(2)));
#define LAS __attribute__((address_space(3)))
#define GAS __attribute__((address_space(1)))
typedef LAS char* lds_ptr;
typedef __amdgpu_buffer_rsrc_t rsrc_t;
#define DI __device__ __forceinline__
#define RFL(x) __builtin_amdgcn_readfirstlane(x)
#define SBAR() __builtin_amdgcn_sched_barrier(0)
#define LP(x) ((lds_ptr)(size_t)(u32)(x))
#define PIN(x) asm volatile("" : "+v"(x))
#define RLX_AGENT __ATOMIC_RELAXED, __HIP_MEMORY_SCOPE_AGENT
#define RLX_WG __ATOMIC_RELAXED, __HIP_MEMORY_SCOPE_WORKGROUP
#define XMAP 2
#define HB 10
#define RPM ((12 + HB - 2) / (HB - 1))
#define S1DELAY 8
#define P3V 1
#define P3S 12
#define P3L 7
#define P3PF 5
#define WFN 8
#define XROT 0
#define SENT 4
#define ROT 4

constexpr int S_ = 2048, D_ = 640, N3 = 1920;
constexpr int XB = 128 * 64 * 2, WB = 64 * 240 * 2, STB = XB + WB;
constexpr int L_RAW = 3 * STB;
constexpr int L_ZERO = L_RAW + 3200;
constexpr int L_WTAB = L_RAW + 4096;
constexpr int L_MASK = 158720;
constexpr int L_ONES = 160768;
constexpr int L_MISC = L_ONES + 64;
constexpr int L_DUMMY = 162816;
constexpr int LDS_BYTES = 163840;
constexpr int L_HALO = 0, L_OWN = 81920, L_Q = 122880, LB_WO = 56320, L_RED = 0;
constexpr int L_PK = 0, L_AV = 47104, L_AK = 71680, L_AQ = L_AK + 22528, L_KVT = L_WTAB, LA_WO = 40960;
constexpr int M_TAG = 0, M_C0 = 16, M_C1 = 32, M_C2 = 36, M_C3 = 40, M_C4 = 44, M_C5 = 48, M_C6 = 52, M_C7 = 56, M_C9 = 60, M_WR = 64, M_C8 = 112, M_WV = 116, M_P0 = 120, M_P1 = 124, M_D = 128, M_X = 144, M_AD = 176, M_C6B = 180;
constexpr u32 W_EPOCH = 0, W_STARTED = 1024, W_KVFLAG = 4096, W_OFLAG = 32768, W_SLICE = 65536;
constexpr u32 W_PART = 1u << 20, W_KVG = 8u << 20, W_OG = 16u << 20, W_TOTAL = 24u << 20;
constexpr u32 PARTB = 3456;
constexpr u32 OOB = 0x80000000u;
constexpr u32 SPIN_CAP = 1u << 18;

DI void dma16(rsrc_t r, u32 voff, u32 soff, u32 ldsaddr) {
  asm volatile("s_mov_b32 m0, %0\n\ts_nop 0\n\tbuffer_load_dwordx4 %1, %2, %3 offen lds" ::"s"(ldsaddr), "v"(voff), "s"(r), "s"(soff) : "m0", "memory");
}
DI void dma16c(rsrc_t r, u32 voff, u32 soff, u32 ldsaddr) {
  asm volatile("s_mov_b32 m0, %0\n\ts_nop 0\n\tbuffer_load_dwordx4 %1, %2, %3 offen sc1 lds" ::"s"(ldsaddr), "v"(voff), "s"(r), "s"(soff) : "m0", "memory");
}
DI s16x4 trrd(lds_ptr p) { return __builtin_bit_cast(s16x4, __builtin_amdgcn_ds_read_tr16_b64_v4i16((LAS s16x4*)p)); }
DI bf16x8 cat8(s16x4 a, s16x4 b) { return __builtin_shufflevector(a, b, 0, 1, 2, 3, 4, 5, 6, 7); }
DI u32 cvtpk2(f32x2 v) { return __builtin_bit_cast(u32, __builtin_convertvector(v, bf2_t)); }
DI u32 cvtpk(float lo, float hi) { return cvtpk2(f32x2{lo, hi}); }
DI float blo(u32 w) { return __uint_as_float(w << 16); }
DI float bhi(u32 w) { return __uint_as_float(w & 0xffff0000u); }
template <int N> DI void waitv() { asm volatile("s_waitcnt vmcnt(%0)" ::"i"(N) : "memory"); }
DI void lbar() { asm volatile("s_waitcnt lgkmcnt(0)\n\ts_barrier" ::: "memory"); }
DI void waitl() { asm volatile("s_waitcnt lgkmcnt(0)" ::: "memory"); }
DI bf16x8 ld128(lds_ptr p) { return *(LAS bf16x8*)p; }
DI s16x4 ld64(lds_ptr p) { return *(LAS s16x4*)p; }
#define MFMA16(a, b, c) __builtin_amdgcn_mfma_f32_16x16x32_bf16(a, b, c, 0, 0, 0)
#define MFMA32(a, b, c) __builtin_amdgcn_mfma_f32_32x32x16_bf16(a, b, c, 0, 0, 0)
#define MFMA32K8(a, b, c) __builtin_amdgcn_mfma_f32_32x32x8bf16_1k(a, b, c, 0, 0, 0)
DI float ex2(float x) { return __builtin_amdgcn_exp2f(x); }
DI u32 gload(char* ws, u32 off) { return __hip_atomic_load((GAS u32*)(ws + off), RLX_AGENT); }
DI void gstore(char* ws, u32 off, u32 v) { __hip_atomic_store((GAS u32*)(ws + off), v, RLX_AGENT); }
DI u32 lds_word(lds_ptr p) { return __hip_atomic_load((LAS u32*)p, RLX_WG); }
DI void lds_inc(lds_ptr p, int) {
  unsigned long long sv;
  asm volatile("s_mov_b64 %0, exec\n\ts_mov_b64 exec, 1\n\tds_add_u32 %1, %2\n\ts_mov_b64 exec, %0" : "=&s"(sv) : "v"((u32)(size_t)p), "v"(1u) : "memory");
}
DI u32 lds_add_ret(lds_ptr p) { return __hip_atomic_fetch_add((LAS u32*)p, 1u, RLX_WG); }
DI void lds_wait_ge(lds_ptr p, u32 want) {
  u32 v, sv, cnt = SPIN_CAP * 8;
  asm volatile("1:\n\tds_read_b32 %0, %3\n\ts_waitcnt lgkmcnt(0)\n\tv_readfirstlane_b32 %1, %0\n\ts_nop 0\n\ts_cmp_ge_u32 %1, %4\n\ts_cbranch_scc1 2f\n\t"
               "s_sub_u32 %2, %2, 1\n\ts_cbranch_scc1 2f\n\ts_sleep 1\n\ts_branch 1b\n2:"
               : "=&v"(v), "=&s"(sv), "+s"(cnt) : "v"((u32)(size_t)p), "s"(want) : "scc", "memory");
}
DI void poll_tag(char* ws, u32 off, u32 tag, bool done) {
#pragma nounroll
  for (u32 s = 0; s < SPIN_CAP; ++s) { const u32 v = gload(ws, off); if (__all(done || v == tag)) break; __builtin_amdgcn_s_sleep(1); }
  asm volatile("" ::: "memory");
}

#define STAMP(i)
#define STAMPV(i)

struct Frags { bf16x8 x[2]; s16x4 wl[5], wh[5]; };

__global__ __launch_bounds__(1024) void fused(const u16* xA, const u16* wA, const u16* wphi, const u16* woA,
                                              const u16* xB, const u16* wB, const u16* woB, u16* out, char* ws) {
  extern __shared__ __attribute__((aligned(16))) char smem[];
  unsigned long long pc0; u32 len;
  asm volatile("s_getpc_b64 %0\n.Lcode_ref:\n\ts_mov_b32 %1, .Lcode_end-.Lcode_ref" : "=s"(pc0), "=s"(len));
  lds_ptr lds = (lds_ptr)smem;
  const u32 lds0 = (u32)(size_t)lds;
  const int b = blockIdx.x, xcd = b & 7, idx = b >> 3;
  const int br = idx >> 4, rt = (xcd & 3) * 4 + ((idx >> 2) & 3), hg = (xcd >> 2) * 4 + (idx & 3);
  int lane = threadIdx.x & 63; const int wid = RFL(threadIdx.x >> 6);
  int g = lane >> 4, i16 = lane & 15, q4 = i16 >> 2, p4 = lane & 3, h = lane >> 5, l32 = lane & 31;
  auto relane = [&]() { PIN(lane); lane &= 63; g = lane >> 4; i16 = lane & 15; q4 = i16 >> 2; p4 = lane & 3; h = lane >> 5; l32 = lane & 31; };
  const rsrc_t rws = __builtin_amdgcn_make_buffer_rsrc((void*)ws, (short)0, W_TOTAL, 0x00020000);
  lds_ptr misc = lds + L_MISC;
  if (wid < 2) {
    const rsrc_t rc = __builtin_amdgcn_make_buffer_rsrc((void*)(pc0 & ~15ull), (short)0, len & ~15u, 0x00020000);
    dma16(rc, (wid * 32 + idx) * 1024 + lane * 16, 0, lds0 + L_DUMMY);
  }
  STAMP(0);

  const rsrc_t rx = __builtin_amdgcn_make_buffer_rsrc((void*)((br ? xB : xA) + (size_t)rt * 128 * D_), (short)0, 128 * D_ * 2, 0x00020000);
  const rsrc_t rw = __builtin_amdgcn_make_buffer_rsrc((void*)(br ? wB : wA), (short)0, D_ * N3 * 2, 0x00020000);
  const int cls = ((idx >> (XMAP ? 2 : 3)) + idx) & 3;
  const unsigned long long orda = (cls & 1) ? 0x8796435201ull : 0x9768532410ull, ordb = (cls & 1) ? 0x9865742130ull : 0x9875463102ull, ord = (cls & 2) ? ordb : orda;
  const int xr = 0;
  auto slice = [&](int s) { int k = ((int)(ord >> (4 * s)) & 15) + xr; k -= k >= 20 ? 20 : k >= 10 ? 10 : 0; return k; };
  const int rot = slice(0);
  const u32 vx0 = (lane >> 3) * 1280 + (((lane & 7) ^ (lane >> 4)) << 4), vx1 = vx0 ^ 64;
  const int wc = lane >= 30 ? lane - 30 : lane;
  const u32 vw = (lane >= 30 ? 3840 : 0) + (wc >= 20 ? 2240 : wc >= 10 ? 1120 : 0) + hg * 160 + wc * 16;
  auto ldx = [&](int i, int kt, u32 buf) { dma16(rx, (i & 1) ? vx1 : vx0, 128 * kt + i * 8 * 1280, buf + i * 1024); };
  auto ldw1 = [&](int j, int kt, u32 buf) { const int p = 2 * j, k = (p & 0x33) | ((p & 4) << 1) | ((p & 8) >> 1); dma16(rw, vw, (64 * kt + k) * 3840, buf + XB + j * 960); };
  const u32 og = W_OG + br * (128 * 20480);
  u32 pc[9] = {};
  auto pre = [&]() {
    auto og_main = [&](int row, int hd, int hf) -> u32 { return og + (row >> 4) * 20480 + ((hg * 2 + (hd >> 1)) << 10) + (((2 * (hd & 1) + hf) * 16 + (row & 15)) << 4); };
    auto og_tail = [&](int row, int hd) -> u32 { return og + (row >> 4) * 20480 + 16384 + hg * 512 + ((hd * 16 + (row & 15)) << 3); };
    if (br) {
      const int hd = wid & 3, qi = wid < 8 ? 1 - (wid >> 2) : wid >> 2, u = 35 - l32 + 4 * h; const bool vones = (g & 1) && p4;
      pc[0] = L_Q + hd * 6144 + (32 * qi + l32) * 48 + 16 * h;
      pc[1] = hd * 2560 + l32 * 80 + 16 * h;
      pc[2] = h ? L_ONES : hd * 2560 + l32 * 80 + 32;
      pc[3] = vones ? L_ONES : hd * 2560 + (4 * h + q4) * 80 + 40 + 32 * (g & 1) + 8 * p4;
      pc[4] = h ? 0 : 10240; pc[5] = vones ? 0 : 10240;
      pc[6] = L_MASK + (u & 3) * 256 + (u & ~3) * 4;
      pc[7] = og_main(rt * 128 + 32 * qi + l32, hd, h);
      pc[8] = h ? OOB : og_tail(rt * 128 + 32 * qi + l32, hd);
      pc[0] += lds0; pc[1] += lds0; pc[2] += lds0; pc[3] += lds0; pc[6] += lds0;
    } else {
      const int tg = wid & 3, pair = (wid >> 2) & 1, isq = wid >> 3;
      pc[0] = (isq ? L_AQ : L_AK) + (32 * tg + l32) * 176 + pair * 80 + 16 * h;
      pc[1] = L_WTAB + pair * 5120 + lane * 16;
      pc[2] = L_WTAB + pair * 5120 + 2048 + lane * 8;
      if (isq) {
        const int hp = (l32 >> 2) & 1, r = (l32 & 3) + 4 * (l32 >> 3);
        pc[3] = (h == hp) ? L_KVT + (2 * pair + h) * 1680 + r * 80 : L_ZERO;
        pc[4] = (h == hp && r <= 4) ? L_KVT + (2 * pair + h) * 1680 + (16 + r) * 80 : L_ZERO;
        pc[5] = og_main(rt * 128 + 32 * tg + l32, 2 * pair + h, 0);
        pc[6] = og_tail(rt * 128 + 32 * tg + l32, 2 * pair + h);
        pc[7] = 0;
      } else {
        const int hd = 2 * pair + (tg >> 1), dt = tg & 1;
        pc[3] = L_AV + hd * 6144 + (8 * g + q4) * 48 + 32 * dt + 8 * p4;
        pc[4] = L_PK + (2 * pair + h) * 10240 + (32 * tg + l32) * 80;
        pc[5] = L_PK + hd * 10240 + (8 * g + q4) * 80 + 8 * p4;
        pc[6] = (!dt || i16 <= 4) ? W_PART + ((hg * 4 + hd) * 16 + rt) * PARTB + (dt * 160 + (dt ? g * 5 + i16 : lane)) * 16 : OOB;
        pc[7] = g < 2 ? pc[6] : OOB;
      }
      pc[0] += lds0; pc[1] += lds0; pc[2] += lds0; pc[3] += lds0; pc[4] += lds0; if (!isq) pc[5] += lds0;
      pc[8] = pc[0] + 64 - 8 * h;
    }
#pragma unroll
    for (int i = 0; i < 9; i++) PIN(pc[i]);
  };
  if (wid < 4) {
    __builtin_amdgcn_s_setprio(3);
    u32 ep = 0;
    if (wid == 3) ep = gload(ws, W_EPOCH);
    if (wid == 2) *(LAS u32*)(misc + lane * 4) = 0;
    auto nanfill = [&](int j) {
      const int i = wid * 5 + j;
      const u32 o_ = i < 16 ? og + (rt * 8 + (i >> 1)) * 20480 + hg * 2048 + (i & 1) * 1024 + lane * 16
                            : og + (rt * 8 + 2 * (i - 16) + h) * 20480 + 16384 + hg * 512 + l32 * 16;
      __builtin_amdgcn_raw_buffer_store_b128(u32x4{~0u, ~0u, ~0u, ~0u}, rws, o_, 0, 16);
    };
    int kt = rot, bi = 0;
    __builtin_amdgcn_s_barrier();
#pragma nounroll
    for (int s = 1; s < 10; s++) {
      bi = bi == 2 ? 0 : bi + 1; kt = slice(s);
      const u32 buf = lds0 + bi * STB;
      if (s == 2 && br == 0) {
        const rsrc_t rp = __builtin_amdgcn_make_buffer_rsrc((void*)(wphi + hg * 1600), (short)0, 3200, 0x00020000);
        dma16(rp, wid * 1024 + lane * 16, 0, lds0 + L_RAW + wid * 1024);
      }
#pragma unroll
      for (int i = 0; i < 4; i++) ldx(wid * 4 + i, kt, buf);
      if (lane < 60) {
#pragma unroll
        for (int j = 0; j < 8; j++) ldw1(wid * 8 + j, kt, buf);
      }
      if (s <= 5) { nanfill(s - 1); waitv<13>(); } else waitv<12>();
      lbar();
      if (s == 6 && wid == 3 && lane == 0) {
        const u32 tag = ep * 0x9E3779B1u + 0x7F4A7C15u;
        *(LAS u32*)(misc + M_TAG) = tag;
        gstore(ws, W_STARTED + ((br * 16 + rt) * 8 + hg) * 4, tag);
      }
      if (s == 9) pre();
      if (s == 9 && br == 0) {
        const int pair = wid >> 1, T = wid & 1, rb = g & 1;
        const int fb = 2 * rb + (p4 >> 1);
        const bool okT = (T == 0) || (fb == 0);
        const int f0 = T ? 16 : 4 * fb;
        s16x4 r[5];
#pragma unroll
        for (int u = 0; u < 5; u++) {
          const int cg = (u < 4) ? 4 * (u >> 1) + 2 * h + (u & 1) : 8 + h;
          const int hc = cg >= 5, d0 = 4 * (cg - 5 * hc);
          const bool ok = okT && (hc == (p4 & 1));
          r[u] = trrd(lds + (ok ? L_RAW + (2 * pair + hc) * 800 + (d0 + q4) * 40 + f0 * 2 : L_ZERO));
        }
        lds_ptr tb = lds + L_WTAB + wid * 2560;
        *(LAS bf16x8*)(tb + lane * 16) = cat8(r[0], r[1]);
        *(LAS bf16x8*)(tb + 1024 + lane * 16) = cat8(r[2], r[3]);
        *(LAS s16x4*)(tb + 2048 + lane * 8) = r[4];
      }
      STAMPV(32 + s - 1);
    }
    {
      const rsrc_t rwo = __builtin_amdgcn_make_buffer_rsrc((void*)(br ? woB : woA), (short)0, D_ * D_ * 2, 0x00020000);
      waitv<0>();
      lbar();
      STAMP(15);
      const int ln = (idx & 15) * 200 + wid * 50 + (lane < 50 ? lane : 49);
      dma16(rwo, (ln / 5) * 1280 + (hg >> 2) * 640 + (ln % 5) * 128, 0, lds0 + L_DUMMY);
    }
    if (wid == 0) {
      poll_tag(ws, W_STARTED + ((br * 16 + rt) * 8 + (lane & 7)) * 4, RFL(lds_word(misc + M_TAG)), false);
      lds_inc(misc + M_C6, lane);
    }
  } else {
    const int cw = wid - 4, mi = cw & 3, nj = cw < 8 ? (cw >> 2) + 1 : 0;
    if (cw < 4) {
#pragma unroll
      for (int i = 0; i < 4; i++) ldx(cw * 4 + i, rot, lds0);
    } else if (lane < 60) {
#pragma unroll
      for (int j = 0; j < 4; j++) ldw1((cw - 4) * 4 + j, rot, lds0);
    }
    __builtin_amdgcn_s_barrier();
    if (cw < 8) {
      const int c = cw & 3, x = lane + c - 35;
      *(LAS float*)(lds + L_MASK + cw * 256 + lane * 4) = ((x > 0) != (cw >= 4)) ? -1e30f : 0.f;
    } else if (cw == 8 && lane < 4) *(LAS u32x2*)(lds + L_ONES + lane * 640) = u32x2{0x3F803F80u, 0x3F803F80u};
    pre();
    const int row = mi * 32 + i16;
    const u32 xa0 = row * 128 + ((g ^ ((row >> 1) & 7)) << 4), xa1 = xa0 ^ 64;
    const u32 wa = XB + (16 * (g >> 1) + 4 * (g & 1) + q4) * 480 + 8 * p4 + nj * 160;
    int base, HS, RS;
    if (br) {
      if (nj == 0) { base = L_Q; HS = 6144; RS = 48; }
      else { base = L_OWN + (nj - 1) * 40 + mi * 7680; HS = 2560; RS = 80; }
    } else {
      if (nj == 2) { base = L_AV; HS = 6144; RS = 48; }
      else { base = nj ? L_AK : L_AQ; HS = 40; RS = 176; }
    }
    int ea[5];
#pragma unroll
    for (int n = 0; n < 5; n++) { const int c4 = 4 * n + g, hd = (c4 * 13) >> 6, d4 = c4 - 5 * hd; ea[n] = lds0 + base + hd * HS + row * RS + d4 * 8; PIN(ea[n]); }
    f32x4 acc[2][5] = {};
    auto rd = [&](Frags& f, lds_ptr bb, int ks, int j) {
      if (j < 2) f.x[j] = ld128(bb + (ks ? xa1 : xa0) + j * 2048);
      else { int n = (j - 2) >> 1; if (j & 1) f.wh[n] = trrd(bb + wa + ks * 15360 + 3840 + n * 32); else f.wl[n] = trrd(bb + wa + ks * 15360 + n * 32); }
    };
#define MM(F, i) acc[(i) & 1][(i) >> 1] = MFMA16(cat8(F.wl[(i) >> 1], F.wh[(i) >> 1]), F.x[(i) & 1], acc[(i) & 1][(i) >> 1])
    Frags F0, F1;
    int boff = 0;
    __builtin_amdgcn_s_setprio(3);
    waitv<0>();
    lbar();
    STAMP(14);
#pragma unroll
    for (int j = 0; j < 12; j++) rd(F0, lds, 0, j);
#pragma unroll
    for (int t = 0; t < 10; t++) {
      SBAR();
#pragma unroll
      for (int i = 0; i < 10; i++) { MM(F0, i); if (i < 6) { rd(F1, lds + boff, 1, 2 * i); rd(F1, lds + boff, 1, 2 * i + 1); } SBAR(); }
#pragma unroll
      for (int i = 0; i < 10 - HB; i++) MM(F1, i);
      SBAR();
      boff = ((t + 1) % 3) * STB; asm volatile("" : "+s"(boff));
      if (t < 9) {
        __builtin_amdgcn_s_waitcnt(0xC07F);
        asm volatile("s_barrier" : "+v"(acc[0][0]), "+v"(acc[1][0]), "+v"(acc[0][1]), "+v"(acc[1][1]), "+v"(acc[0][2]), "+v"(acc[1][2]), "+v"(acc[0][3]), "+v"(acc[1][3]), "+v"(acc[0][4]), "+v"(acc[1][4]) :: "memory");
      } else {
        lds_inc(misc + M_C8, lane);
        asm volatile("" : "+v"(acc[0][0]), "+v"(acc[1][0]), "+v"(acc[0][1]), "+v"(acc[1][1]), "+v"(acc[0][2]), "+v"(acc[1][2]), "+v"(acc[0][3]), "+v"(acc[1][3]), "+v"(acc[0][4]), "+v"(acc[1][4]));
      }
      asm volatile("" ::: "memory");
#pragma unroll
      for (int i = 10 - HB; i < 10; i++) {
#pragma unroll
        for (int j = (i - 10 + HB) * RPM; j < (i - 9 + HB) * RPM && j < 12; j++) if (t < 9) rd(F0, lds + boff, 0, j);
        MM(F1, i); SBAR();
      }
    }
    STAMP(1);
    relane();
    u32x2 pk[2][5];
    if (br && nj == 0) {
#pragma unroll
      for (int i = 0; i < 10; i++) { const f32x4 v = acc[i & 1][i >> 1] * (0.22360679774997896f * 1.4426950408889634f); pk[i & 1][i >> 1] = u32x2{cvtpk(v[0], v[1]), cvtpk(v[2], v[3])}; }
    } else {
#pragma unroll
      for (int i = 0; i < 10; i++) { const f32x4 v = acc[i & 1][i >> 1]; pk[i & 1][i >> 1] = u32x2{cvtpk(v[0], v[1]), cvtpk(v[2], v[3])}; asm volatile("" ::: "memory"); }
    }
    int eb[5];
#pragma unroll
    for (int n = 0; n < 5; n++) { eb[n] = ea[n] + 16 * RS; PIN(eb[n]); }
#pragma unroll
    for (int n = 0; n < 5; n++) { *(LAS u32x2*)LP(ea[n]) = pk[0][n]; *(LAS u32x2*)LP(eb[n]) = pk[1][n]; }
    if (br == 0 && nj == 2) {
#pragma unroll
      for (int m = 0; m < 2; m++) *(LAS u32x2*)(lds + L_AV + g * 6144 + (mi * 32 + i16 + 16 * m) * 48 + 40) = u32x2{0x3F803F80u, 0x3F803F80u};
    }
    lds_inc(misc + M_WR + nj * 16 + mi * 4, lane);
    if (nj == 2) lds_inc(misc + M_WV, lane);
    lds_inc(misc + M_C9, lane);
    if (br && nj == 1) {
      lds_wait_ge(misc + M_WR + 32 + mi * 4, 1u);
      if (mi < 2) __builtin_amdgcn_s_sleep(6);
      const u32 dst = W_KVG + ((rt * 8 + hg) * 4 + mi) * 10240 + lane * 16;
      u32x4 v[10];
#pragma unroll
      for (int i = 0; i < 10; i++) v[i] = *(LAS u32x4*)(lds + L_OWN + mi * 10240 + i * 1024 + lane * 16);
      SBAR();
#pragma unroll
      for (int i = 0; i < 10; i++) __builtin_amdgcn_raw_buffer_store_b128(v[i], rws, dst, i * 1024, 16);
      waitv<0>();
      if (lane == 0) gstore(ws, W_KVFLAG + (rt * 8 + hg) * 128 + mi * 4, lds_word(misc + M_TAG));
      if (mi >> 1) lds_inc(misc + M_P1, lane);
      STAMP(4);
    } else if (br) __builtin_amdgcn_s_setprio(0);
  }
  relane();
  f32x16 o = {};
  PIN(o);
  if (br) lds_wait_ge(misc + M_C9, 12u);
  STAMP(2);
  const u32 tag = RFL(lds_word(misc + M_TAG));
  const int l_wo = br ? LB_WO : LA_WO;
  const rsrc_t rwo = __builtin_amdgcn_make_buffer_rsrc((void*)(br ? woB : woA), (short)0, D_ * D_ * 2, 0x00020000);
  u32 wvm[2] = {}, wvt[2] = {};
  const int wj = wid & 3;
  auto wo_init = [&]() {
#pragma unroll
    for (int u = 0; u < 2; u++) {
      int c = (u ? 4 : wj) * 64 + lane; PIN(c);
      const int p = c / 10, ch = c - p * 10;
      const int gg = ((p >> 3) & 2) | ((p >> 2) & 1), tt = (p >> 3) & 1, e = p & 3;
      wvm[u] = ((gg >> 1) * 20 + 4 * (gg & 1) + 8 * tt + e) * 1280 + hg * 160 + ch * 16; wvt[u] = (tt * 80 + gg * 20 + 16 + e) * 1280 + hg * 160 + ch * 16;
    }
  };
  auto wo_dma = [&](int k0, int k1) {
    if (k1 == 20) {
#pragma unroll
      for (int ks = 16; ks < 20; ks++) dma16(rwo, wvt[0], (ks - 16) * 160 * 1280, lds0 + l_wo + ks * 5120 + wj * 1024);
      dma16(rwo, wvt[1], wj * 160 * 1280, lds0 + l_wo + (16 + wj) * 5120 + 4096);
      k1 = 16;
    }
#pragma nounroll
    for (int ks = k0; ks < k1; ks++) {
      dma16(rwo, wvm[0], ks * 40 * 1280, lds0 + l_wo + ks * 5120 + wj * 1024);
      if ((ks & 3) == wj) dma16(rwo, wvm[1], ks * 40 * 1280, lds0 + l_wo + ks * 5120 + 4096);
    }
  };
  const bool wow = wid >= 12;
  const u32 ofl = W_OFLAG + (br * 16 + rt) * 128;
  auto published = [&](int rg) {
    waitv<0>();
    lds_inc(misc + M_D + rg * 4, lane);
    if (lane == 0 && lds_add_ret(misc + M_C2) == (br ? 15 : 7)) gstore(ws, ofl + hg * 4, tag);
  };

  if (br) {
    const int hd = wid & 3, qi = wid < 8 ? 1 - (wid >> 2) : wid >> 2;
    const u32 kvf = W_KVFLAG + (rt * 8 + hg) * 128;
    const bf16x8 qa = ld128(LP(pc[0]));
    s16x4 qb = ld64(LP(pc[0]) + 32);
    if (h) qb = s16x4{0, 0, 0, 0};
    auto ldmask = [&](int pol) -> f32x16 {
      f32x4 t[4];
#pragma unroll
      for (int j = 0; j < 4; j++) t[j] = *(LAS f32x4*)(LP(pc[6]) + pol * 1024 + j * 32);
      return __builtin_shufflevector(__builtin_shufflevector(t[0], t[1], 0, 1, 2, 3, 4, 5, 6, 7), __builtin_shufflevector(t[2], t[3], 0, 1, 2, 3, 4, 5, 6, 7),
                                     0, 1, 2, 3, 4, 5, 6, 7, 8, 9, 10, 11, 12, 13, 14, 15);
    };
    auto pk4 = [&](const f32x16& s, int o_) -> bf16x8 {
      u32x4 p;
#pragma unroll
      for (int j = 0; j < 4; j++) p[j] = cvtpk(ex2(s[o_ + 2 * j]), ex2(s[o_ + 2 * j + 1]));
      return __builtin_bit_cast(bf16x8, p);
    };
    lds_ptr ka_p, kb_p, vp;
    auto seek = [&](int row) { ka_p = LP(pc[1]) + row * 10240; kb_p = LP(pc[2]) + row * pc[4]; vp = LP(pc[3]) + row * pc[5]; };
    f32x16 s, s2; s16x4 c;
    auto step = [&](f32x16& sc, f32x16& sn) {
      ka_p -= 10240; kb_p -= pc[4];
      const bf16x8 ka = ld128(ka_p); const s16x4 kb = ld64(kb_p);
      const bf16x8 v0 = cat8(trrd(vp), trrd(vp + 640)), v1 = cat8(trrd(vp + 1280), trrd(vp + 1920));
      vp -= pc[5];
      SBAR();
      float e[8]; u32x4 p1;
      bf16x8 p0 = pk4(sc, 0); PIN(p0);
      SBAR(); sn = MFMA32(ka, qa, f32x16{}); SBAR();
#pragma unroll
      for (int j = 0; j < 4; j++) e[j] = ex2(sc[8 + j]);
      SBAR(); sn = MFMA32K8(kb, c, sn); SBAR();
#pragma unroll
      for (int j = 4; j < 8; j++) e[j] = ex2(sc[8 + j]);
      SBAR(); o = MFMA32(v0, p0, o); SBAR();
#pragma unroll
      for (int j = 0; j < 4; j++) p1[j] = cvtpk(e[2 * j], e[2 * j + 1]);
      SBAR(); o = MFMA32(v1, __builtin_bit_cast(bf16x8, p1), o); SBAR();
    };
    auto last = [&](f32x16& sc) {
      const bf16x8 v0 = cat8(trrd(vp), trrd(vp + 640)), v1 = cat8(trrd(vp + 1280), trrd(vp + 1920));
      o = MFMA32(v0, pk4(sc, 0), o);
      o = MFMA32(v1, pk4(sc, 8), o);
    };
    lds_inc(misc + M_C5, lane);
    seek(L_OWN / 10240 + qi);
    {
      const s16x4 kb = ld64(kb_p);
      s = MFMA32K8(kb, qb, MFMA32(ld128(ka_p), qa, ldmask(0)));
      float mm = fmaxf(s[0], s[1]);
#pragma unroll
      for (int r = 2; r < 16; r += 2) mm = fmaxf(fmaxf(mm, s[r]), s[r + 1]);
      auto rr = __builtin_amdgcn_permlane32_swap(__float_as_uint(mm), __float_as_uint(mm), false, false);
      const u32 nm = cvtpk(-fmaxf(__uint_as_float(rr[0]), __uint_as_float(rr[1])), 0.f);
      const u32x2 qw = __builtin_bit_cast(u32x2, qb);
      c = __builtin_bit_cast(s16x4, u32x2{h ? nm : qw.x, qw.y});
      s = MFMA32K8(kb, __builtin_bit_cast(s16x4, u32x2{h ? nm : 0u, 0u}), s);
    }
    if (qi >= 2) { step(s, s2); step(s2, s); }
    if (qi & 1) { step(s, s2); last(s2); } else last(s);
    lds_inc(misc + M_C1, lane);
    STAMP(3);
    if (wid < 4) {
      const int dd = 2 - (wid >> 1);
      bool seen = false;
#pragma nounroll
      for (int hf = 1; hf >= 0; --hf) {
        const int tl = 2 * hf + (wid & 1);
        if (hf) lds_wait_ge(misc + M_P1, 2u);
        else {
          const u32 v = gload(ws, kvf - (rt >= dd ? dd : 0) * 1024 + tl * 4);
          lds_inc(misc + M_P0, lane);
          waitv<0>();
          seen = RFL(v) == tag;
          STAMP(5); lds_inc(misc + M_C0, lane);
        }
        if (rt >= dd && !seen) poll_tag(ws, kvf - dd * 1024 + tl * 4, tag, false);
        if (hf) { STAMP(11); }
        if (rt >= dd) {
          const u32 src = W_KVG + (((rt - dd) * 8 + hg) * 4 + tl) * 10240;
#pragma unroll
          for (int i = 0; i < 10; i++) dma16c(rws, lane * 16, src + i * 1024, lds0 + L_HALO + hf * 40960 + wid * 10240 + i * 1024);
        }
      }
    }
    if (wid >= 8) wo_init();
    if (qi == 2) { lds_wait_ge(misc + M_C5, 16u); lds_wait_ge(misc + M_P0, 4u); wo_dma(13, 20); }
    lds_wait_ge(misc + M_C0, 4u);
    int wk = 13;
    if (rt > 0) {
      int n1 = 2, nn = 4, km = 0;
      if (rt >= 2) { n1 = qi < 3 ? 4 : 3; nn = 8 - qi; km = qi >= 2 ? n1 - 1 : nn - 1; }
      const u32 ev1 = (1u << n1) | (wid < 4 ? 4u : 0u) | (wow ? ~0u : 0u);
      const u32 ev2 = (1u << n1) | (1u << km);
      if (wow) __builtin_amdgcn_s_setprio(3); else __builtin_amdgcn_s_setprio(2);
      seek(7);
      s = MFMA32K8(ld64(kb_p), c, MFMA32(ld128(ka_p), qa, f32x16{}));
      auto before = [&](int k) {
        if (k == 2 && wid < 4) { waitv<0>(); lds_inc(misc + M_C7, lane); }
        if (k == n1) {
          ka_p -= (4 - n1) * 10240; kb_p -= (4 - n1) * pc[4];
          lds_wait_ge(misc + M_C7, 4u);
          if (qi == 2) { waitv<0>(); lds_inc(misc + M_C6, lane); }
        }
        if (wow && wk > 0) {
          const u32 c1 = RFL(lds_word(misc + M_C1)), c7 = RFL(lds_word(misc + M_C7)), c3 = RFL(lds_word(misc + M_C3));
          const int lim = c3 >= 16 ? 0 : (c1 >= 16 && c7 >= 4) ? 5 : 20;
          const int k0 = wk - 8 > lim ? wk - 8 : lim;
          if (k0 < wk) { wo_dma(k0, wk); wk = k0; }
        }
      };
      auto after = [&](int k, f32x16& sn) {
        if (k == km) sn += ldmask(1);
        if (k == n1) { vp -= (4 - n1) * pc[5]; lds_inc(misc + M_C3, lane); if (!wow) __builtin_amdgcn_s_setprio(1); }
      };
#pragma nounroll
      for (int k = 1;; k += 2) {
        if (__builtin_expect((ev1 >> k) & 1, 0)) before(k);
        step(s, s2);
        if (__builtin_expect((ev2 >> k) & 1, 0)) after(k, s2);
        if (__builtin_expect(k + 1 == nn, 0)) { s = s2; break; }
        if (__builtin_expect((ev1 >> (k + 1)) & 1, 0)) before(k + 1);
        step(s2, s);
        if (__builtin_expect((ev2 >> (k + 1)) & 1, 0)) after(k + 1, s);
        if (__builtin_expect(k + 2 == nn, 0)) break;
      }
      last(s);
    } else {
      if (qi == 2) { waitv<0>(); lds_inc(misc + M_C6, lane); }
    }
    float l = h ? o[8] : o[12];
    if (__builtin_expect(__any(!(l < 1.8e19f)), 0)) {
      const int dd = lane % 3;
      poll_tag(ws, kvf - (rt - dd < 0 ? 0 : dd) * 1024 + 4 * (lane & 3), tag, false);
      float q[20], a[20], mx = -3e38f; l = 0.f;
      const u32x4 qw = __builtin_bit_cast(u32x4, qa); const u32x2 qv = __builtin_bit_cast(u32x2, qb);
#pragma unroll
      for (int j = 0; j < 10; j++) {
        const u32 w = j < 8 ? __shfl(qw[j & 3], l32 + 32 * (j >> 2)) : __shfl(qv[j & 1], l32);
        q[2 * j] = blo(w); q[2 * j + 1] = bhi(w); a[2 * j] = 0.f; a[2 * j + 1] = 0.f;
      }
      const int row = rt * 128 + 32 * qi + l32;
#pragma nounroll
      for (int t = 0; t < 128; ++t) {
        const int j = row - 2 * t - h;
        if (j >= 0) {
          const u32 src = W_KVG + (((j >> 7) * 8 + hg) * 4 + ((j >> 5) & 3)) * 10240 + hd * 2560 + (j & 31) * 80;
          u32 w[20];
#pragma unroll
          for (int i = 0; i < 5; i++) { const u32x4 v = __builtin_amdgcn_raw_buffer_load_b128(rws, src, i * 16, 16); w[4 * i] = v.x; w[4 * i + 1] = v.y; w[4 * i + 2] = v.z; w[4 * i + 3] = v.w; }
          float sc = 0.f;
#pragma unroll
          for (int i = 0; i < 10; i++) sc += q[2 * i] * blo(w[i]) + q[2 * i + 1] * bhi(w[i]);
          const float mn = fmaxf(mx, sc), f = ex2(mx - mn), p = ex2(sc - mn);
          mx = mn; l = l * f + p;
#pragma unroll
          for (int i = 0; i < 10; i++) { a[2 * i] = a[2 * i] * f + p * blo(w[10 + i]); a[2 * i + 1] = a[2 * i + 1] * f + p * bhi(w[10 + i]); }
        }
      }
      const float mo = __shfl_xor(mx, 32), mn = fmaxf(mx, mo), f = ex2(mx - mn);
      l *= f; l += __shfl_xor(l, 32);
#pragma unroll
      for (int i = 0; i < 20; i++) { a[i] *= f; a[i] += __shfl_xor(a[i], 32); }
#pragma unroll
      for (int r = 0; r < 8; r++) o[r] = h ? a[(r & 3) + 8 * (r >> 2) + 4] : a[(r & 3) + 8 * (r >> 2)];
#pragma unroll
      for (int r = 8; r < 12; r++) o[r] = a[8 + r];
    }
    const float rl = __builtin_amdgcn_rcpf(l);
    const u32x4 w0 = {cvtpk(o[0] * rl, o[1] * rl), cvtpk(o[2] * rl, o[3] * rl), cvtpk(o[4] * rl, o[5] * rl), cvtpk(o[6] * rl, o[7] * rl)};
    const u32x2 w1 = {cvtpk(o[8] * rl, o[9] * rl), cvtpk(o[10] * rl, o[11] * rl)};
    __builtin_amdgcn_raw_buffer_store_b128(w0, rws, pc[7], 0, 16);
    __builtin_amdgcn_raw_buffer_store_b64(w1, rws, pc[8], 0, 16);
    STAMP(6);
    lds_inc(misc + M_AD, lane);
    if (wow && wk > 5) { lds_wait_ge(misc + M_C1, 16u); wo_dma(5, wk); wk = 5; }
    if (wid >= 8) published(qi);
    if (wow) {
      lds_inc(misc + M_C6, lane);
      if (wk > 0) { if (rt) lds_wait_ge(misc + M_C3, 16u); wo_dma(0, wk); waitv<0>(); }
      lds_inc(misc + M_C6B, lane);
    }
    __builtin_amdgcn_s_setprio(0);
  } else {
    const int tg = wid & 3, pair = (wid >> 2) & 1, isq = wid >> 3;
    u32 phi[20];
    {
      lds_wait_ge(misc + M_WR + (isq ? 0 : 16) + tg * 4, 1u);
      const bf16x8 b0 = ld128(LP(pc[0])), a00 = ld128(LP(pc[1])), a10 = ld128(LP(pc[1]) + 2560);
      const bf16x8 b1 = ld128(LP(pc[0]) + 32), a01 = ld128(LP(pc[1]) + 1024), a11 = ld128(LP(pc[1]) + 2560 + 1024);
      const s16x4 b2 = ld64(LP(pc[8])), a02 = ld64(LP(pc[2])), a12 = ld64(LP(pc[2]) + 2560);
      lds_inc(misc + M_C5, lane);
      if (wow) { wo_init(); lds_wait_ge(misc + M_C5, 16u); wo_dma(6, 20); waitv<0>(); lds_inc(misc + M_C6, lane); }
      if (isq) { __builtin_amdgcn_s_setprio(0); lds_wait_ge(misc + M_C0 + 4, 4u); } else __builtin_amdgcn_s_setprio(2);
      f32x16 t0 = MFMA32(a00, b0, f32x16{}), t1 = MFMA32(a10, b0, f32x16{});
      t0 = MFMA32(a01, b1, t0); t1 = MFMA32(a11, b1, t1);
      t0 = MFMA32K8(a02, b2, t0); t1 = MFMA32K8(a12, b2, t1);
      float p[20];
#pragma unroll
      for (int j = 0; j < 10; j++) {
        const u32 w = j < 8 ? cvtpk(t0[2 * j], t0[2 * j + 1]) : cvtpk(t1[2 * j - 16], t1[2 * j - 15]);
        p[2 * j] = blo(w); p[2 * j + 1] = bhi(w);
      }
      float mx = fmaxf(p[0], p[1]), mn = fminf(p[0], p[1]);
#pragma unroll
      for (int j = 2; j < 20; j += 2) { mx = fmaxf(fmaxf(mx, p[j]), p[j + 1]); mn = fminf(fminf(mn, p[j]), p[j + 1]); }
      const float L2E = 1.4426950408889634f;
      const f32x2 sl = {L2E, -L2E}, of = {-mx * L2E, mn * L2E};
      f32x2 e[20], sa = {0.f, 0.f}, sb = {0.f, 0.f};
#pragma unroll
      for (int j = 0; j < 20; j++) {
        const f32x2 a = __builtin_elementwise_fma(f32x2{p[j], p[j]}, sl, of);
        e[j] = f32x2{ex2(a[0]), ex2(a[1])};
        if (j & 1) sb += e[j]; else sa += e[j];
      }
      sa += sb;
      const f32x2 rr = {__builtin_amdgcn_rcpf(sa[0]), __builtin_amdgcn_rcpf(sa[1])};
#pragma unroll
      for (int j = 0; j < 20; j++) phi[j] = cvtpk2(e[j] * rr);
    }
    if (!isq) {
      lds_wait_ge(misc + M_WV, 4u);
      bf16x8 vb[4];
#pragma unroll
      for (int ks = 0; ks < 4; ks++) vb[ks] = cat8(trrd(LP(pc[3]) + ks * 32 * 48), trrd(LP(pc[3]) + ks * 32 * 48 + 4 * 48));
      lds_wait_ge(misc + M_C8, 12u);
#pragma unroll
      for (int j = 0; j < 5; j++) *(LAS u32x4*)(LP(pc[4]) + 16 * j) = u32x4{phi[4 * j], phi[4 * j + 1], phi[4 * j + 2], phi[4 * j + 3]};
      lds_inc(misc + M_C0 + pair * 4, lane);
      STAMP(3);
      lds_wait_ge(misc + M_C0 + pair * 4, 4u);
      f32x4 kv[3] = {};
#pragma unroll
      for (int ks = 0; ks < 4; ks++)
#pragma unroll
        for (int T = 0; T < 3; T++) kv[T] = MFMA16(cat8(trrd(LP(pc[5]) + ks * 2560 + T * 32), trrd(LP(pc[5]) + ks * 2560 + 320 + T * 32)), vb[ks], kv[T]);
#pragma unroll
      for (int T = 0; T < 3; T++)
        __builtin_amdgcn_raw_buffer_store_b128(u32x4{cvtpk(kv[T][0], kv[T][1]), tag, cvtpk(kv[T][2], kv[T][3]), tag}, rws, pc[T < 2 ? 6 : 7], T * ((tg & 1) ? 320 : 1024), 16);
      lds_inc(misc + M_C1, lane);
      STAMP(4);
      if (wid == 0) {
        const int sl = (rt & 3) * 53 + lane; const bool act = lane < 53 && sl < 210;
        const u32 src = act ? W_PART + (hg * 4 + (rt >> 2)) * 16 * PARTB + sl * 16 : OOB;
        float s0 = 0.f, s1 = 0.f, s2 = 0.f, s3 = 0.f;
        lds_wait_ge(misc + M_C6, 5u);
        lds_wait_ge(misc + M_C7, 4u);
#pragma nounroll
        for (u32 s = 0; s < SPIN_CAP; ++s) {
          bool ok = true; s0 = 0.f; s1 = 0.f; s2 = 0.f; s3 = 0.f;
          u32x4 v[16];
#pragma unroll
          for (int r = 0; r < 16; r++) v[r] = __builtin_amdgcn_raw_buffer_load_b128(rws, src, r * PARTB, 16);
#pragma unroll
          for (int r = 0; r < 16; r++) { ok &= (v[r].y == tag) & (v[r].w == tag); s0 += blo(v[r].x); s1 += bhi(v[r].x); s2 += blo(v[r].z); s3 += bhi(v[r].z); }
          if (__all(ok || !act)) break;
          asm volatile("" ::: "memory");
        }
        __builtin_amdgcn_raw_buffer_store_b128(u32x4{cvtpk(s0, s1), tag, cvtpk(s2, s3), tag}, rws, act ? W_SLICE + (hg * 16 + rt) * 1024 + lane * 16 : OOB, 0, 16);
        waitv<0>();
        lds_inc(misc + M_C4, lane);
        STAMP(5);
      } else if (wid >= 4) {
        const int hh = wid - 4;
        const u32 src = W_SLICE + (hg * 16 + hh * 4) * 1024 + lane * 16;
        int da[4];
#pragma unroll
        for (int j = 0; j < 4; j++) {
          const int sl = j * 53 + lane; int d, ph;
          if (sl < 160) { d = sl & 15; ph = (sl >> 4) * 4; }
          else { const int s2 = sl - 160, gg = s2 / 5; d = 16 + s2 - 5 * gg; ph = 4 * gg; }
          da[j] = L_KVT + hh * 1680 + d * 80 + ph * 2;
        }
        waitv<0>(); lds_inc(misc + M_C7, lane);
        wo_init();
        lds_wait_ge(misc + M_C1, 8u);
        lds_wait_ge(misc + M_C5, 16u);
        lds_wait_ge(misc + M_C4, 1u);
        u32x4 v[4];
#pragma nounroll
        for (u32 s = 0; s < SPIN_CAP; ++s) {
          bool ok = true;
#pragma unroll
          for (int r = 0; r < 4; r++) v[r] = __builtin_amdgcn_raw_buffer_load_b128(rws, src, r * 1024, 16);
#pragma unroll
          for (int r = 0; r < 4; r++) ok &= ((v[r].y == tag) & (v[r].w == tag)) | (lane >= (r < 3 ? 53 : 51));
          if (__all(ok)) break;
          asm volatile("" ::: "memory");
        }
#pragma unroll
        for (int r = 0; r < 4; r++) if (lane < (r < 3 ? 53 : 51)) *(LAS u32x2*)(lds + da[r]) = u32x2{v[r].x, v[r].z};
        lds_inc(misc + M_C3, lane);
        STAMP(11);
        wo_dma(0, 6); waitv<0>(); lds_inc(misc + M_C6, lane);
      }
      __builtin_amdgcn_s_setprio(0);
    } else {
      lds_ptr a0 = LP(pc[3]), a1 = LP(pc[4]); const u32 om = pc[5], ot = pc[6];
      STAMP(12);
      lds_wait_ge(misc + M_C3, 4u);
      f32x16 t0 = {}, t1 = {};
#pragma unroll
      for (int j = 0; j < 5; j++) {
        const bf16x8 bq = __builtin_bit_cast(bf16x8, u32x4{phi[4 * j], phi[4 * j + 1], phi[4 * j + 2], phi[4 * j + 3]});
        t1 = MFMA32(ld128(a1 + 16 * j), bq, t1); t0 = MFMA32(ld128(a0 + 16 * j), bq, t0);
      }
      const float rl = __builtin_amdgcn_rcpf(t1[4] + 1e-6f);
      const u32x4 w0 = {cvtpk(t0[0] * rl, t0[1] * rl), cvtpk(t0[2] * rl, t0[3] * rl), cvtpk(t0[8] * rl, t0[9] * rl), cvtpk(t0[10] * rl, t0[11] * rl)};
      const u32x4 w1 = {cvtpk(t0[4] * rl, t0[5] * rl), cvtpk(t0[6] * rl, t0[7] * rl), cvtpk(t0[12] * rl, t0[13] * rl), cvtpk(t0[14] * rl, t0[15] * rl)};
      const u32x2 w2 = {cvtpk(t1[0] * rl, t1[1] * rl), cvtpk(t1[2] * rl, t1[3] * rl)};
      __builtin_amdgcn_raw_buffer_store_b128(w0, rws, om, 0, 16);
      __builtin_amdgcn_raw_buffer_store_b128(w1, rws, om, 256, 16);
      __builtin_amdgcn_raw_buffer_store_b64(w2, rws, ot, 0, 16);
      STAMP(6);
      published(tg);
    }
  }
  STAMP(7);
  relane();
  if (wid >= 8) {
    if (b == 0 && wid == 15) {
#pragma nounroll
      for (u32 s = 0; s < SPIN_CAP; ++s) {
        const u32x4 v = __builtin_amdgcn_raw_buffer_load_b128(rws, W_STARTED + lane * 16, 0, 16);
        if (__all(v.x == tag && v.y == tag && v.z == tag && v.w == tag)) break;
        __builtin_amdgcn_s_sleep(2);
      }
      if (lane == 0) gstore(ws, W_EPOCH, tag);
    }
  } else {
    const int rg = br ? (0x2013 >> (4 * (wid >> 1))) & 3 : wid >> 1, kh = wid & 1;
    u32 obm = og + (rt * 8 + rg * 2) * 20480 + kh * 8192 + lane * 16, obt = og + (rt * 8 + rg * 2) * 20480 + kh * 2048 + lane * 8;
    PIN(obm); PIN(obt);
    const u32 mo[2] = {(u32)kh * 20480u, (u32)(1 - kh) * 20480u};
    u32x4 of[10][2];
    auto ldo = [&](int s) {
#pragma unroll
      for (int m = 0; m < 2; m++) {
        if (s < 8) of[s][m] = __builtin_amdgcn_raw_buffer_load_b128(rws, obm, mo[m] + s * 1024, 16);
        else {
          const u32x2 a = __builtin_amdgcn_raw_buffer_load_b64(rws, obt, mo[m] + (s + 8) * 1024, 16);
          const u32x2 c = __builtin_amdgcn_raw_buffer_load_b64(rws, obt, mo[m] + (s + 8) * 1024 + 512, 16);
          of[s][m] = u32x4{a.x, a.y, c.x, c.y};
        }
      }
    };
    u32 wm = lds0 + l_wo + (16 * (g >> 1) + 4 * (g & 1) + q4) * 160 + 8 * p4, wt = wm + (16 + 2 * kh) * 5120; wm += kh * 8 * 5120;
    PIN(wm); PIN(wt);
    bf16x8 wf[WFN];
    auto ldw = [&](int i) { const int s = 9 - i / 5, n = i % 5; lds_ptr p = (s < 8 ? LP(wm) + s * 5120 : LP(wt) + (s - 8) * 5120) + n * 32; wf[i % WFN] = cat8(trrd(p), trrd(p + 1280)); };
    f32x4 ac2[2][5];
    constexpr int PF = P3PF;
    if (br) published(1 - (wid >> 2));
    lds_wait_ge(misc + M_C6, 9u);
    lds_wait_ge(misc + M_D + rg * 4, br ? 4u : 2u);
    STAMP(8);
#pragma nounroll
    for (int pass = 0;; ++pass) {
      if (pass) poll_tag(ws, ofl + (lane & 7) * 4, tag, false);
#pragma unroll
      for (int i = 0; i < WFN; i++) ldw(i);
#pragma nounroll
      for (int tr = 0; tr < 64; ++tr) {
#pragma unroll
        for (int s = 9; s > 9 - PF; s--) ldo(s);
        asm volatile("" ::: "memory");
        u32 mx = 0;
#pragma unroll
        for (int s = 8; s < 10; s++)
#pragma unroll
          for (int m = 0; m < 2; m++) mx = max(max(mx, max(of[s][m].x, of[s][m].y)), max(of[s][m].z, of[s][m].w));
        if (pass || !__any(mx == ~0u)) break;
      }
      STAMP(9);
#pragma unroll
      for (int n = 0; n < 5; n++) { ac2[0][n] = f32x4{}; ac2[1][n] = f32x4{}; }
#pragma unroll
      for (int s = 9; s >= 0; s--) {
        if (s == 6) lds_wait_ge(misc + M_C6B, br ? 4u : 0u);
        if (s == 1) lds_wait_ge(misc + M_AD, br ? 16u : 0u);
        SBAR();
#pragma unroll
        for (int n = 0; n < 5; n++) {
          const int i = (9 - s) * 5 + n;
          ac2[0][n] = MFMA16(wf[i % WFN], __builtin_bit_cast(bf16x8, of[s][0]), ac2[0][n]);
          ac2[1][n] = MFMA16(wf[i % WFN], __builtin_bit_cast(bf16x8, of[s][1]), ac2[1][n]);
          if (i + WFN < 50) ldw(i + WFN);
          SBAR();
        }
        if (s - PF >= 0) ldo(s - PF);
      }
      const float chk = ac2[0][0][0] + ac2[1][0][0];
      if (pass || !__any(chk != chk)) break;
    }
    STAMP(13);
    relane();
    lds_ptr red = lds + L_RED + lane * 16;
#pragma unroll
    for (int n = 0; n < 5; n++) *(LAS f32x4*)(red + (wid ^ 1) * 5120 + n * 1024) = ac2[1][n];
    lds_inc(misc + M_X + (wid ^ 1) * 4, lane);
    const int r16 = rt * 8 + rg * 2 + kh;
    u32 ob1 = (r16 * 16 + i16) * 2560 + br * 1280 + hg * 160, ob2 = ob1 + g * 8; ob1 += (g & 1) * 32 + (g >> 1) * 16;
    PIN(ob1); PIN(ob2);
    lds_wait_ge(misc + M_X + wid * 4, 1u);
    f32x4 acc[5];
#pragma unroll
    for (int n = 0; n < 5; n++) acc[n] = *(LAS f32x4*)(red + wid * 5120 + n * 1024);
    SBAR();
#pragma unroll
    for (int n = 0; n < 5; n++) acc[n] += ac2[0][n];
    const rsrc_t ro = __builtin_amdgcn_make_buffer_rsrc((void*)out, (short)0, S_ * 1280 * 2, 0x00020000);
#pragma unroll
    for (int n = 0; n < 4; n += 2) {
      const auto x = __builtin_amdgcn_permlane16_swap(cvtpk(acc[n][0], acc[n][1]), cvtpk(acc[n + 1][0], acc[n + 1][1]), false, false);
      const auto y = __builtin_amdgcn_permlane16_swap(cvtpk(acc[n][2], acc[n][3]), cvtpk(acc[n + 1][2], acc[n + 1][3]), false, false);
      __builtin_amdgcn_raw_buffer_store_b128(u32x4{x[0], y[0], x[1], y[1]}, ro, ob1, n * 32, 0);
    }
    __builtin_amdgcn_raw_buffer_store_b64(u32x2{cvtpk(acc[4][0], acc[4][1]), cvtpk(acc[4][2], acc[4][3])}, ro, ob2, 128, 0);
  }
  asm volatile(".Lcode_end:");
  STAMP(10);
}

extern "C" void kernel_launch(void* const* d_in, const int* in_sizes, int n_in, void* d_out, int out_size, void* d_ws, size_t ws_size, hipStream_t stream) {
  (void)hipFuncSetAttribute((const void*)fused, hipFuncAttributeMaxDynamicSharedMemorySize, LDS_BYTES);
  fused<<<256, 1024, LDS_BYTES, stream>>>((const u16*)d_in[0], (const u16*)d_in[1], (const u16*)d_in[2], (const u16*)d_in[3],
                                          (const u16*)d_in[4], (const u16*)d_in[5], (const u16*)d_in[6], (u16*)d_out, (char*)d_ws);
}
```

```cpp
#include <hip/hip_runtime.h>
#include <hip/hip_bf16.h>

typedef unsigned short u16;
typedef unsigned u32;
typedef short bf16x8 __attribute__((ext_vector_type(8)));
typedef short s16x4 __attribute__((ext_vector_type(4)));
typedef float f32x4 __attribute__((ext_vector_type(4)));
typedef float f32x16 __attribute__((ext_vector_type(16)));
typedef float f32x2 __attribute__((ext_vector_type(2)));
typedef __bf16 bf2_t __attribute__((ext_vector_type(2)));
typedef u32 u32x4 __attribute__((ext_vector_type(4)));
typedef u32 u32x2 __attribute__((ext_vector_type(2)));
#define LAS __attribute__((address_space(3)))
#define GAS __attribute__((address_space(1)))
typedef LAS char* lds_ptr;
typedef __amdgpu_buffer_rsrc_t rsrc_t;
#define DI __device__ __forceinline__
#define RFL(x) __builtin_amdgcn_readfirstlane(x)
#define SBAR() __builtin_amdgcn_sched_barrier(0)
#define LP(x) ((lds_ptr)(size_t)(u32)(x))
#define PIN(x) asm volatile("" : "+v"(x))
#define RLX_AGENT __ATOMIC_RELAXED, __HIP_MEMORY_SCOPE_AGENT
#define RLX_WG __ATOMIC_RELAXED, __HIP_MEMORY_SCOPE_WORKGROUP

constexpr int S_ = 2048, D_ = 640, N3 = 1920;
constexpr int XB = 128 * 64 * 2, WB = 64 * 240 * 2, STB = XB + WB;
constexpr int L_RAW = 3 * STB;
constexpr int L_ZERO = L_RAW + 3200;
constexpr int L_WTAB = L_RAW + 4096;
constexpr int L_MASK = 158720;
constexpr int L_ONES = 160768;
constexpr int L_MISC = L_ONES + 64;
constexpr int L_DUMMY = 162816;
constexpr int LDS_BYTES = 163840;
constexpr int L_HALO = 0, L_OWN = 81920, L_Q = 122880, LB_WO = 56320, L_RED = 0;
constexpr int L_PK = 0, L_AV = 47104, L_AK = 71680, L_AQ = L_AK + 22528, L_KVT = L_WTAB, LA_WO = 40960;
constexpr int M_TAG = 0, M_C0 = 16, M_C1 = 32, M_C2 = 36, M_C3 = 40, M_C4 = 44, M_C5 = 48, M_C6 = 52, M_C7 = 56, M_C9 = 60, M_WR = 64, M_C8 = 112, M_WV = 116, M_P0 = 120, M_P1 = 124, M_D = 128, M_X = 144, M_AD = 176, M_C6B = 180;
constexpr u32 W_EPOCH = 0, W_STARTED = 1024, W_KVFLAG = 4096, W_OFLAG = 32768, W_SLICE = 65536;
constexpr u32 W_PART = 1u << 20, W_KVG = 8u << 20, W_OG = 16u << 20, W_TOTAL = 24u << 20;
constexpr u32 PARTB = 3456;
constexpr u32 OOB = 0x80000000u;
constexpr u32 SPIN_CAP = 1u << 18;

DI void dma16(rsrc_t r, u32 voff, u32 soff, u32 ldsaddr) {
  asm volatile("s_mov_b32 m0, %0\n\ts_nop 0\n\tbuffer_load_dwordx4 %1, %2, %3 offen lds" ::"s"(ldsaddr), "v"(voff), "s"(r), "s"(soff) : "m0", "memory");
}
DI void dma16c(rsrc_t r, u32 voff, u32 soff, u32 ldsaddr) {
  asm volatile("s_mov_b32 m0, %0\n\ts_nop 0\n\tbuffer_load_dwordx4 %1, %2, %3 offen sc1 lds" ::"s"(ldsaddr), "v"(voff), "s"(r), "s"(soff) : "m0", "memory");
}
DI s16x4 trrd(lds_ptr p) { return __builtin_bit_cast(s16x4, __builtin_amdgcn_ds_read_tr16_b64_v4i16((LAS s16x4*)p)); }
DI bf16x8 cat8(s16x4 a, s16x4 b) { return __builtin_shufflevector(a, b, 0, 1, 2, 3, 4, 5, 6, 7); }
DI u32 cvtpk2(f32x2 v) { return __builtin_bit_cast(u32, __builtin_convertvector(v, bf2_t)); }
DI u32 cvtpk(float lo, float hi) { return cvtpk2(f32x2{lo, hi}); }
DI float blo(u32 w) { return __uint_as_float(w << 16); }
DI float bhi(u32 w) { return __uint_as_float(w & 0xffff0000u); }
template <int N> DI void waitv() { asm volatile("s_waitcnt vmcnt(%0)" ::"i"(N) : "memory"); }
DI void lbar() { asm volatile("s_waitcnt lgkmcnt(0)\n\ts_barrier" ::: "memory"); }
DI void waitl() { asm volatile("s_waitcnt lgkmcnt(0)" ::: "memory"); }
DI bf16x8 ld128(lds_ptr p) { return *(LAS bf16x8*)p; }
DI s16x4 ld64(lds_ptr p) { return *(LAS s16x4*)p; }
#define MFMA16(a, b, c) __builtin_amdgcn_mfma_f32_16x16x32_bf16(a, b, c, 0, 0, 0)
#define MFMA32(a, b, c) __builtin_amdgcn_mfma_f32_32x32x16_bf16(a, b, c, 0, 0, 0)
#define MFMA32K8(a, b, c) __builtin_amdgcn_mfma_f32_32x32x8bf16_1k(a, b, c, 0, 0, 0)
DI float ex2(float x) { return __builtin_amdgcn_exp2f(x); }
DI u32 gload(char* ws, u32 off) { return __hip_atomic_load((GAS u32*)(ws + off), RLX_AGENT); }
DI void gstore(char* ws, u32 off, u32 v) { __hip_atomic_store((GAS u32*)(ws + off), v, RLX_AGENT); }
DI u32 lds_word(lds_ptr p) { return __hip_atomic_load((LAS u32*)p, RLX_WG); }
DI void lds_inc(lds_ptr p, int) {
  unsigned long long sv;
  asm volatile("s_mov_b64 %0, exec\n\ts_mov_b64 exec, 1\n\tds_add_u32 %1, %2\n\ts_mov_b64 exec, %0" : "=&s"(sv) : "v"((u32)(size_t)p), "v"(1u) : "memory");
}
DI u32 lds_add_ret(lds_ptr p) { return __hip_atomic_fetch_add((LAS u32*)p, 1u, RLX_WG); }
DI void lds_wait_ge(lds_ptr p, u32 want) {
  u32 v, sv, cnt = SPIN_CAP * 8;
  asm volatile("1:\n\tds_read_b32 %0, %3\n\ts_waitcnt lgkmcnt(0)\n\tv_readfirstlane_b32 %1, %0\n\ts_nop 0\n\ts_cmp_ge_u32 %1, %4\n\ts_cbranch_scc1 2f\n\t"
               "s_sub_u32 %2, %2, 1\n\ts_cbranch_scc1 2f\n\ts_sleep 1\n\ts_branch 1b\n2:"
               : "=&v"(v), "=&s"(sv), "+s"(cnt) : "v"((u32)(size_t)p), "s"(want) : "scc", "memory");
}
DI void poll_tag(char* ws, u32 off, u32 tag, bool done) {
#pragma nounroll
  for (u32 s = 0; s < SPIN_CAP; ++s) { const u32 v = gload(ws, off); if (__all(done || v == tag)) break; __builtin_amdgcn_s_sleep(1); }
  asm volatile("" ::: "memory");
}

struct Frags { bf16x8 x[2]; s16x4 wl[5], wh[5]; };

__global__ __launch_bounds__(1024, 4) void fused(const u16* xA, const u16* wA, const u16* wphi, const u16* woA,
                                              const u16* xB, const u16* wB, const u16* woB, u16* out, char* ws) {
  extern __shared__ __attribute__((aligned(16))) char smem[];
  unsigned long long pc0; u32 len;
  asm volatile("s_getpc_b64 %0\n.Lcode_ref:\n\ts_mov_b32 %1, .Lcode_end-.Lcode_ref" : "=s"(pc0), "=s"(len));
  lds_ptr lds = (lds_ptr)smem;
  const u32 lds0 = (u32)(size_t)lds;
  const int b = blockIdx.x, xcd = b & 7, idx = b >> 3;
  const int br = idx >> 4, rt = (xcd & 3) * 4 + ((idx >> 2) & 3), hg = (xcd >> 2) * 4 + (idx & 3);
  int lane = threadIdx.x & 63; const int wid = RFL(threadIdx.x >> 6);
  int g = lane >> 4, i16 = lane & 15, q4 = i16 >> 2, p4 = lane & 3, h = lane >> 5, l32 = lane & 31;
  auto relane = [&]() { PIN(lane); lane &= 63; g = lane >> 4; i16 = lane & 15; q4 = i16 >> 2; p4 = lane & 3; h = lane >> 5; l32 = lane & 31; };
  const rsrc_t rws = __builtin_amdgcn_make_buffer_rsrc((void*)ws, (short)0, W_TOTAL, 0x00020000);
  lds_ptr misc = lds + L_MISC;
  if (wid < 2) {
    const rsrc_t rc = __builtin_amdgcn_make_buffer_rsrc((void*)(pc0 & ~15ull), (short)0, len & ~15u, 0x00020000);
    dma16(rc, (wid * 32 + idx) * 1024 + lane * 16, 0, lds0 + L_DUMMY);
  }

  const rsrc_t rx = __builtin_amdgcn_make_buffer_rsrc((void*)((br ? xB : xA) + (size_t)rt * 128 * D_), (short)0, 128 * D_ * 2, 0x00020000);
  const rsrc_t rw = __builtin_amdgcn_make_buffer_rsrc((void*)(br ? wB : wA), (short)0, D_ * N3 * 2, 0x00020000);
  const int cls = ((idx >> 2) + idx) & 3;
  const unsigned long long orda = (cls & 1) ? 0x8796435201ull : 0x9768532410ull, ordb = (cls & 1) ? 0x9865742130ull : 0x9875463102ull, ord = (cls & 2) ? ordb : orda;
  const int xr = (((xcd & 3) * 5) >> 1) + (xcd >> 2);
  auto slice = [&](int s) { int k = ((int)(ord >> (4 * s)) & 15) + xr; k -= k >= 20 ? 20 : k >= 10 ? 10 : 0; return k; };
  const int rot = slice(0);
  const u32 vx0 = (lane >> 3) * 1280 + (((lane & 7) ^ (lane >> 4)) << 4), vx1 = vx0 ^ 64;
  const int wc = lane >= 30 ? lane - 30 : lane;
  const u32 vw = (lane >= 30 ? 3840 : 0) + (wc >= 20 ? 2240 : wc >= 10 ? 1120 : 0) + hg * 160 + wc * 16;
  auto ldx = [&](int i, int kt, u32 buf) { dma16(rx, (i & 1) ? vx1 : vx0, 128 * kt + i * 8 * 1280, buf + i * 1024); };
  auto ldw1 = [&](int j, int kt, u32 buf) { const int p = 2 * j, k = (p & 0x33) | ((p & 4) << 1) | ((p & 8) >> 1); dma16(rw, vw, (64 * kt + k) * 3840, buf + XB + j * 960); };
  const u32 og = W_OG + br * (128 * 20480);
  u32 pc[9] = {};
  auto pre = [&]() {
    auto og_main = [&](int row, int hd, int hf) -> u32 { return og + (row >> 4) * 20480 + ((hg * 2 + (hd >> 1)) << 10) + (((2 * (hd & 1) + hf) * 16 + (row & 15)) << 4); };
    auto og_tail = [&](int row, int hd) -> u32 { return og + (row >> 4) * 20480 + 16384 + hg * 512 + ((hd * 16 + (row & 15)) << 3); };
    if (br) {
      const int hd = wid & 3, qi = wid < 8 ? 1 - (wid >> 2) : wid >> 2, u = 35 - l32 + 4 * h; const bool vones = (g & 1) && p4;
      pc[0] = L_Q + hd * 6144 + (32 * qi + l32) * 48 + 16 * h;
      pc[1] = hd * 2560 + l32 * 80 + 16 * h;
      pc[2] = h ? L_ONES : hd * 2560 + l32 * 80 + 32;
      pc[3] = vones ? L_ONES : hd * 2560 + (4 * h + q4) * 80 + 40 + 32 * (g & 1) + 8 * p4;
      pc[4] = h ? 0 : 10240; pc[5] = vones ? 0 : 10240;
      pc[6] = L_MASK + (u & 3) * 256 + (u & ~3) * 4;
      pc[7] = og_main(rt * 128 + 32 * qi + l32, hd, h);
      pc[8] = h ? OOB : og_tail(rt * 128 + 32 * qi + l32, hd);
      pc[0] += lds0; pc[1] += lds0; pc[2] += lds0; pc[3] += lds0; pc[6] += lds0;
    } else {
      const int tg = wid & 3, pair = (wid >> 2) & 1, isq = wid >> 3;
      pc[0] = (isq ? L_AQ : L_AK) + (32 * tg + l32) * 176 + pair * 80 + 16 * h;
      pc[1] = L_WTAB + pair * 5120 + lane * 16;
      pc[2] = L_WTAB + pair * 5120 + 2048 + lane * 8;
      if (isq) {
        const int hp = (l32 >> 2) & 1, r = (l32 & 3) + 4 * (l32 >> 3);
        pc[3] = (h == hp) ? L_KVT + (2 * pair + h) * 1680 + r * 80 : L_ZERO;
        pc[4] = (h == hp && r <= 4) ? L_KVT + (2 * pair + h) * 1680 + (16 + r) * 80 : L_ZERO;
        pc[5] = og_main(rt * 128 + 32 * tg + l32, 2 * pair + h, 0);
        pc[6] = og_tail(rt * 128 + 32 * tg + l32, 2 * pair + h);
        pc[7] = 0;
      } else {
        const int hd = 2 * pair + (tg >> 1), dt = tg & 1;
        pc[3] = L_AV + hd * 6144 + (8 * g + q4) * 48 + 32 * dt + 8 * p4;
        pc[4] = L_PK + (2 * pair + h) * 10240 + (32 * tg + l32) * 80;
        pc[5] = L_PK + hd * 10240 + (8 * g + q4) * 80 + 8 * p4;
        pc[6] = (!dt || i16 <= 4) ? W_PART + ((hg * 4 + hd) * 16 + rt) * PARTB + (dt * 160 + (dt ? g * 5 + i16 : lane)) * 16 : OOB;
        pc[7] = g < 2 ? pc[6] : OOB;
      }
      pc[0] += lds0; pc[1] += lds0; pc[2] += lds0; pc[3] += lds0; pc[4] += lds0; if (!isq) pc[5] += lds0;
      pc[8] = pc[0] + 64 - 8 * h;
    }
#pragma unroll
    for (int i = 0; i < 9; i++) PIN(pc[i]);
  };
  if (wid < 4) {
    __builtin_amdgcn_s_setprio(3);
    u32 ep = 0;
    if (wid == 3) ep = gload(ws, W_EPOCH);
    if (wid == 2) *(LAS u32*)(misc + lane * 4) = 0;
    auto nanfill = [&](int j) {
      const int i = wid * 5 + j;
      const u32 o_ = i < 16 ? og + (rt * 8 + (i >> 1)) * 20480 + hg * 2048 + (i & 1) * 1024 + lane * 16
                            : og + (rt * 8 + 2 * (i - 16) + h) * 20480 + 16384 + hg * 512 + l32 * 16;
      __builtin_amdgcn_raw_buffer_store_b128(u32x4{~0u, ~0u, ~0u, ~0u}, rws, o_, 0, 16);
    };
    int kt = rot, bi = 0;
    __builtin_amdgcn_s_barrier();
#pragma nounroll
    for (int s = 1; s < 10; s++) {
      bi = bi == 2 ? 0 : bi + 1; kt = slice(s);
      const u32 buf = lds0 + bi * STB;
      if (s == 2 && br == 0) {
        const rsrc_t rp = __builtin_amdgcn_make_buffer_rsrc((void*)(wphi + hg * 1600), (short)0, 3200, 0x00020000);
        dma16(rp, wid * 1024 + lane * 16, 0, lds0 + L_RAW + wid * 1024);
      }
#pragma unroll
      for (int i = 0; i < 4; i++) ldx(wid * 4 + i, kt, buf);
      if (lane < 60) {
#pragma unroll
        for (int j = 0; j < 8; j++) ldw1(wid * 8 + j, kt, buf);
      }
      if (s <= 5) { nanfill(s - 1); waitv<13>(); } else waitv<12>();
      lbar();
      if (s == 6 && wid == 3 && lane == 0) {
        const u32 tag = ep * 0x9E3779B1u + 0x7F4A7C15u;
        *(LAS u32*)(misc + M_TAG) = tag;
        gstore(ws, W_STARTED + ((br * 16 + rt) * 8 + hg) * 4, tag);
      }
      if (s == 9) pre();
      if (s == 9 && br == 0) {
        const int pair = wid >> 1, T = wid & 1, rb = g & 1;
        const int fb = 2 * rb + (p4 >> 1);
        const bool okT = (T == 0) || (fb == 0);
        const int f0 = T ? 16 : 4 * fb;
        s16x4 r[5];
#pragma unroll
        for (int u = 0; u < 5; u++) {
          const int cg = (u < 4) ? 4 * (u >> 1) + 2 * h + (u & 1) : 8 + h;
          const int hc = cg >= 5, d0 = 4 * (cg - 5 * hc);
          const bool ok = okT && (hc == (p4 & 1));
          r[u] = trrd(lds + (ok ? L_RAW + (2 * pair + hc) * 800 + (d0 + q4) * 40 + f0 * 2 : L_ZERO));
        }
        lds_ptr tb = lds + L_WTAB + wid * 2560;
        *(LAS bf16x8*)(tb + lane * 16) = cat8(r[0], r[1]);
        *(LAS bf16x8*)(tb + 1024 + lane * 16) = cat8(r[2], r[3]);
        *(LAS s16x4*)(tb + 2048 + lane * 8) = r[4];
      }
    }
    {
      const rsrc_t rwo = __builtin_amdgcn_make_buffer_rsrc((void*)(br ? woB : woA), (short)0, D_ * D_ * 2, 0x00020000);
      waitv<0>();
      lbar();
      const int ln = (idx & 15) * 200 + wid * 50 + (lane < 50 ? lane : 49);
      dma16(rwo, (ln / 5) * 1280 + (hg >> 2) * 640 + (ln % 5) * 128, 0, lds0 + L_DUMMY);
    }
    if (wid == 0) {
      poll_tag(ws, W_STARTED + ((br * 16 + rt) * 8 + (lane & 7)) * 4, RFL(lds_word(misc + M_TAG)), false);
      lds_inc(misc + M_C6, lane);
    }
  } else {
    const int cw = wid - 4, mi = cw & 3, nj = cw < 8 ? (cw >> 2) + 1 : 0;
    if (cw < 4) {
#pragma unroll
      for (int i = 0; i < 4; i++) ldx(cw * 4 + i, rot, lds0);
    } else if (lane < 60) {
#pragma unroll
      for (int j = 0; j < 4; j++) ldw1((cw - 4) * 4 + j, rot, lds0);
    }
    __builtin_amdgcn_s_barrier();
    if (cw < 8) {
      const int c = cw & 3, x = lane + c - 35;
      *(LAS float*)(lds + L_MASK + cw * 256 + lane * 4) = ((x > 0) != (cw >= 4)) ? -1e30f : 0.f;
    } else if (cw == 8 && lane < 4) *(LAS u32x2*)(lds + L_ONES + lane * 640) = u32x2{0x3F803F80u, 0x3F803F80u};
    pre();
    const int row = mi * 32 + i16;
    const u32 xa0 = row * 128 + ((g ^ ((row >> 1) & 7)) << 4), xa1 = xa0 ^ 64;
    const u32 wa = XB + (16 * (g >> 1) + 4 * (g & 1) + q4) * 480 + 8 * p4 + nj * 160;
    int base, HS, RS;
    if (br) {
      if (nj == 0) { base = L_Q; HS = 6144; RS = 48; }
      else { base = L_OWN + (nj - 1) * 40 + mi * 7680; HS = 2560; RS = 80; }
    } else {
      if (nj == 2) { base = L_AV; HS = 6144; RS = 48; }
      else { base = nj ? L_AK : L_AQ; HS = 40; RS = 176; }
    }
    int ea[5];
#pragma unroll
    for (int n = 0; n < 5; n++) { const int c4 = 4 * n + g, hd = (c4 * 13) >> 6, d4 = c4 - 5 * hd; ea[n] = lds0 + base + hd * HS + row * RS + d4 * 8; PIN(ea[n]); }
    f32x4 acc[2][5] = {};
    auto rd = [&](Frags& f, lds_ptr bb, int ks, int j) {
      if (j < 2) f.x[j] = ld128(bb + (ks ? xa1 : xa0) + j * 2048);
      else { int n = (j - 2) >> 1; if (j & 1) f.wh[n] = trrd(bb + wa + ks * 15360 + 3840 + n * 32); else f.wl[n] = trrd(bb + wa + ks * 15360 + n * 32); }
    };
#define MM(F, i) acc[(i) & 1][(i) >> 1] = MFMA16(cat8(F.wl[(i) >> 1], F.wh[(i) >> 1]), F.x[(i) & 1], acc[(i) & 1][(i) >> 1])
    Frags F0, F1;
    int boff = 0;
    __builtin_amdgcn_s_setprio(3);
    waitv<0>();
    lbar();
#pragma unroll
    for (int j = 0; j < 12; j++) rd(F0, lds, 0, j);
#pragma unroll
    for (int t = 0; t < 10; t++) {
      SBAR();
#pragma unroll
      for (int i = 0; i < 10; i++) { MM(F0, i); if (i < 6) { rd(F1, lds + boff, 1, 2 * i); rd(F1, lds + boff, 1, 2 * i + 1); } SBAR(); }
      boff = ((t + 1) % 3) * STB; asm volatile("" : "+s"(boff));
      if (t < 9) {
        __builtin_amdgcn_s_waitcnt(0xC07F);
        asm volatile("s_barrier" : "+v"(acc[0][0]), "+v"(acc[1][0]), "+v"(acc[0][1]), "+v"(acc[1][1]), "+v"(acc[0][2]), "+v"(acc[1][2]), "+v"(acc[0][3]), "+v"(acc[1][3]), "+v"(acc[0][4]), "+v"(acc[1][4]) :: "memory");
      } else {
        lds_inc(misc + M_C8, lane);
        asm volatile("" : "+v"(acc[0][0]), "+v"(acc[1][0]), "+v"(acc[0][1]), "+v"(acc[1][1]), "+v"(acc[0][2]), "+v"(acc[1][2]), "+v"(acc[0][3]), "+v"(acc[1][3]), "+v"(acc[0][4]), "+v"(acc[1][4]));
      }
      asm volatile("" ::: "memory");
#pragma unroll
      for (int i = 0; i < 10; i++) {
        if (t < 9 && i < 6) { rd(F0, lds + boff, 0, 2 * i); rd(F0, lds + boff, 0, 2 * i + 1); }
        MM(F1, i); SBAR();
      }
    }
    relane();
    u32x2 pk[2][5];
    if (br && nj == 0) {
#pragma unroll
      for (int i = 0; i < 10; i++) { const f32x4 v = acc[i & 1][i >> 1] * (0.22360679774997896f * 1.4426950408889634f); pk[i & 1][i >> 1] = u32x2{cvtpk(v[0], v[1]), cvtpk(v[2], v[3])}; }
    } else {
#pragma unroll
      for (int i = 0; i < 10; i++) { const f32x4 v = acc[i & 1][i >> 1]; pk[i & 1][i >> 1] = u32x2{cvtpk(v[0], v[1]), cvtpk(v[2], v[3])}; asm volatile("" ::: "memory"); }
    }
    int eb[5];
#pragma unroll
    for (int n = 0; n < 5; n++) { eb[n] = ea[n] + 16 * RS; PIN(eb[n]); }
#pragma unroll
    for (int n = 0; n < 5; n++) { *(LAS u32x2*)LP(ea[n]) = pk[0][n]; *(LAS u32x2*)LP(eb[n]) = pk[1][n]; }
    if (br == 0 && nj == 2) {
#pragma unroll
      for (int m = 0; m < 2; m++) *(LAS u32x2*)(lds + L_AV + g * 6144 + (mi * 32 + i16 + 16 * m) * 48 + 40) = u32x2{0x3F803F80u, 0x3F803F80u};
    }
    lds_inc(misc + M_WR + nj * 16 + mi * 4, lane);
    if (nj == 2) lds_inc(misc + M_WV, lane);
    lds_inc(misc + M_C9, lane);
    if (br && nj == 1) {
      lds_wait_ge(misc + M_WR + 32 + mi * 4, 1u);
      if (mi < 2) __builtin_amdgcn_s_sleep(6);
      const u32 dst = W_KVG + ((rt * 8 + hg) * 4 + mi) * 10240 + lane * 16;
      u32x4 v[10];
#pragma unroll
      for (int i = 0; i < 10; i++) v[i] = *(LAS u32x4*)(lds + L_OWN + mi * 10240 + i * 1024 + lane * 16);
      SBAR();
#pragma unroll
      for (int i = 0; i < 10; i++) __builtin_amdgcn_raw_buffer_store_b128(v[i], rws, dst, i * 1024, 16);
      waitv<0>();
      if (lane == 0) gstore(ws, W_KVFLAG + (rt * 8 + hg) * 128 + mi * 4, lds_word(misc + M_TAG));
      if (mi >> 1) lds_inc(misc + M_P1, lane);
    } else if (br) __builtin_amdgcn_s_setprio(0);
  }
  relane();
  f32x16 o = {};
  PIN(o);
  if (br) lds_wait_ge(misc + M_C9, 12u);
  const u32 tag = RFL(lds_word(misc + M_TAG));
  const int l_wo = br ? LB_WO : LA_WO;
  const rsrc_t rwo = __builtin_amdgcn_make_buffer_rsrc((void*)(br ? woB : woA), (short)0, D_ * D_ * 2, 0x00020000);
  u32 wvm[2] = {}, wvt[2] = {};
  const int wj = wid & 3;
  auto wo_init = [&]() {
#pragma unroll
    for (int u = 0; u < 2; u++) {
      int c = (u ? 4 : wj) * 64 + lane; PIN(c);
      const int p = c / 10, ch = c - p * 10;
      const int gg = ((p >> 3) & 2) | ((p >> 2) & 1), tt = (p >> 3) & 1, e = p & 3;
      wvm[u] = ((gg >> 1) * 20 + 4 * (gg & 1) + 8 * tt + e) * 1280 + hg * 160 + ch * 16; wvt[u] = (tt * 80 + gg * 20 + 16 + e) * 1280 + hg * 160 + ch * 16;
    }
  };
  auto wo_dma = [&](int k0, int k1) {
    if (k1 == 20) {
#pragma unroll
      for (int ks = 16; ks < 20; ks++) dma16(rwo, wvt[0], (ks - 16) * 160 * 1280, lds0 + l_wo + ks * 5120 + wj * 1024);
      dma16(rwo, wvt[1], wj * 160 * 1280, lds0 + l_wo + (16 + wj) * 5120 + 4096);
      k1 = 16;
    }
#pragma nounroll
    for (int ks = k0; ks < k1; ks++) {
      dma16(rwo, wvm[0], ks * 40 * 1280, lds0 + l_wo + ks * 5120 + wj * 1024);
      if ((ks & 3) == wj) dma16(rwo, wvm[1], ks * 40 * 1280, lds0 + l_wo + ks * 5120 + 4096);
    }
  };
  const bool wow = wid >= 12;
  const u32 ofl = W_OFLAG + (br * 16 + rt) * 128;
  auto published = [&](int rg) {
    waitv<0>();
    lds_inc(misc + M_D + rg * 4, lane);
    if (lane == 0 && lds_add_ret(misc + M_C2) == (br ? 15 : 7)) gstore(ws, ofl + hg * 4, tag);
  };

  if (br) {
    const int hd = wid & 3, qi = wid < 8 ? 1 - (wid >> 2) : wid >> 2;
    const u32 kvf = W_KVFLAG + (rt * 8 + hg) * 128;
    const bf16x8 qa = ld128(LP(pc[0]));
    s16x4 qb = ld64(LP(pc[0]) + 32);
    if (h) qb = s16x4{0, 0, 0, 0};
    auto ldmask = [&](int pol) -> f32x16 {
      f32x4 t[4];
#pragma unroll
      for (int j = 0; j < 4; j++) t[j] = *(LAS f32x4*)(LP(pc[6]) + pol * 1024 + j * 32);
      return __builtin_shufflevector(__builtin_shufflevector(t[0], t[1], 0, 1, 2, 3, 4, 5, 6, 7), __builtin_shufflevector(t[2], t[3], 0, 1, 2, 3, 4, 5, 6, 7),
                                     0, 1, 2, 3, 4, 5, 6, 7, 8, 9, 10, 11, 12, 13, 14, 15);
    };
    auto pk4 = [&](const f32x16& s, int o_) -> bf16x8 {
      u32x4 p;
#pragma unroll
      for (int j = 0; j < 4; j++) p[j] = cvtpk(ex2(s[o_ + 2 * j]), ex2(s[o_ + 2 * j + 1]));
      return __builtin_bit_cast(bf16x8, p);
    };
    lds_ptr ka_p, kb_p, vp;
    auto seek = [&](int row) { ka_p = LP(pc[1]) + row * 10240; kb_p = LP(pc[2]) + row * pc[4]; vp = LP(pc[3]) + row * pc[5]; };
    f32x16 s, s2; s16x4 c;
    auto step = [&](f32x16& sc, f32x16& sn) {
      ka_p -= 10240; kb_p -= pc[4];
      const bf16x8 ka = ld128(ka_p); const s16x4 kb = ld64(kb_p);
      const bf16x8 v0 = cat8(trrd(vp), trrd(vp + 640)), v1 = cat8(trrd(vp + 1280), trrd(vp + 1920));
      vp -= pc[5];
      SBAR();
      float e[8]; u32x4 p1;
      bf16x8 p0 = pk4(sc, 0); PIN(p0);
      SBAR(); sn = MFMA32(ka, qa, f32x16{}); SBAR();
#pragma unroll
      for (int j = 0; j < 4; j++) e[j] = ex2(sc[8 + j]);
      SBAR(); sn = MFMA32K8(kb, c, sn); SBAR();
#pragma unroll
      for (int j = 4; j < 8; j++) e[j] = ex2(sc[8 + j]);
      SBAR(); o = MFMA32(v0, p0, o); SBAR();
#pragma unroll
      for (int j = 0; j < 4; j++) p1[j] = cvtpk(e[2 * j], e[2 * j + 1]);
      SBAR(); o = MFMA32(v1, __builtin_bit_cast(bf16x8, p1), o); SBAR();
    };
    auto last = [&](f32x16& sc) {
      const bf16x8 v0 = cat8(trrd(vp), trrd(vp + 640)), v1 = cat8(trrd(vp + 1280), trrd(vp + 1920));
      o = MFMA32(v0, pk4(sc, 0), o);
      o = MFMA32(v1, pk4(sc, 8), o);
    };
    lds_inc(misc + M_C5, lane);
    seek(L_OWN / 10240 + qi);
    {
      const s16x4 kb = ld64(kb_p);
      s = MFMA32K8(kb, qb, MFMA32(ld128(ka_p), qa, ldmask(0)));
      float mm = fmaxf(s[0], s[1]);
#pragma unroll
      for (int r = 2; r < 16; r += 2) mm = fmaxf(fmaxf(mm, s[r]), s[r + 1]);
      auto rr = __builtin_amdgcn_permlane32_swap(__float_as_uint(mm), __float_as_uint(mm), false, false);
      const u32 nm = cvtpk(-fmaxf(__uint_as_float(rr[0]), __uint_as_float(rr[1])), 0.f);
      const u32x2 qw = __builtin_bit_cast(u32x2, qb);
      c = __builtin_bit_cast(s16x4, u32x2{h ? nm : qw.x, qw.y});
      s = MFMA32K8(kb, __builtin_bit_cast(s16x4, u32x2{h ? nm : 0u, 0u}), s);
    }
    if (qi >= 2) { step(s, s2); step(s2, s); }
    if (qi & 1) { step(s, s2); last(s2); } else last(s);
    lds_inc(misc + M_C1, lane);
    if (wid < 4) {
      const int dd = 2 - (wid >> 1);
      bool seen = false;
#pragma nounroll
      for (int hf = 1; hf >= 0; --hf) {
        const int tl = 2 * hf + (wid & 1);
        if (hf) lds_wait_ge(misc + M_P1, 2u);
        else {
          const u32 v = gload(ws, kvf - (rt >= dd ? dd : 0) * 1024 + tl * 4);
          lds_inc(misc + M_P0, lane);
          waitv<0>();
          seen = RFL(v) == tag;
          lds_inc(misc + M_C0, lane);
        }
        if (rt >= dd && !seen) poll_tag(ws, kvf - dd * 1024 + tl * 4, tag, false);
        if (rt >= dd) {
          const u32 src = W_KVG + (((rt - dd) * 8 + hg) * 4 + tl) * 10240;
#pragma unroll
          for (int i = 0; i < 10; i++) dma16c(rws, lane * 16, src + i * 1024, lds0 + L_HALO + hf * 40960 + wid * 10240 + i * 1024);
        }
      }
    }
    if (wid >= 8) wo_init();
    if (qi == 2) { lds_wait_ge(misc + M_C5, 16u); lds_wait_ge(misc + M_P0, 4u); wo_dma(13, 20); }
    lds_wait_ge(misc + M_C0, 4u);
    int wk = 13;
    if (rt > 0) {
      int n1 = 2, nn = 4, km = 0;
      if (rt >= 2) { n1 = qi < 3 ? 4 : 3; nn = 8 - qi; km = qi >= 2 ? n1 - 1 : nn - 1; }
      const u32 ev1 = (1u << n1) | (wid < 4 ? 4u : 0u) | (wow ? ~0u : 0u);
      const u32 ev2 = (1u << n1) | (1u << km);
      if (wow) __builtin_amdgcn_s_setprio(3); else __builtin_amdgcn_s_setprio(2);
      seek(7);
      s = MFMA32K8(ld64(kb_p), c, MFMA32(ld128(ka_p), qa, f32x16{}));
      auto before = [&](int k) {
        if (k == 2 && wid < 4) { waitv<0>(); lds_inc(misc + M_C7, lane); }
        if (k == n1) {
          ka_p -= (4 - n1) * 10240; kb_p -= (4 - n1) * pc[4];
          lds_wait_ge(misc + M_C7, 4u);
          if (qi == 2) { waitv<0>(); lds_inc(misc + M_C6, lane); }
        }
        if (wow && wk > 0) {
          const u32 c1 = RFL(lds_word(misc + M_C1)), c7 = RFL(lds_word(misc + M_C7)), c3 = RFL(lds_word(misc + M_C3));
          const int lim = c3 >= 16 ? 0 : (c1 >= 16 && c7 >= 4) ? 5 : 20;
          const int k0 = wk - 8 > lim ? wk - 8 : lim;
          if (k0 < wk) { wo_dma(k0, wk); wk = k0; }
        }
      };
      auto after = [&](int k, f32x16& sn) {
        if (k == km) sn += ldmask(1);
        if (k == n1) { vp -= (4 - n1) * pc[5]; lds_inc(misc + M_C3, lane); if (!wow) __builtin_amdgcn_s_setprio(1); }
      };
#pragma nounroll
      for (int k = 1;; k += 2) {
        if (__builtin_expect((ev1 >> k) & 1, 0)) before(k);
        step(s, s2);
        if (__builtin_expect((ev2 >> k) & 1, 0)) after(k, s2);
        if (__builtin_expect(k + 1 == nn, 0)) { s = s2; break; }
        if (__builtin_expect((ev1 >> (k + 1)) & 1, 0)) before(k + 1);
        step(s2, s);
        if (__builtin_expect((ev2 >> (k + 1)) & 1, 0)) after(k + 1, s);
        if (__builtin_expect(k + 2 == nn, 0)) break;
      }
      last(s);
    } else {
      if (qi == 2) { waitv<0>(); lds_inc(misc + M_C6, lane); }
    }
    float l = h ? o[8] : o[12];
    if (__builtin_expect(__any(!(l < 1.8e19f && l > 1e-18f)), 0)) {
      const int dd = lane % 3;
      poll_tag(ws, kvf - (rt - dd < 0 ? 0 : dd) * 1024 + 4 * (lane & 3), tag, false);
      float q[20], a[20], mx = -3e38f; l = 0.f;
      const u32x4 qw = __builtin_bit_cast(u32x4, qa); const u32x2 qv = __builtin_bit_cast(u32x2, qb);
#pragma unroll
      for (int j = 0; j < 10; j++) {
        const u32 w = j < 8 ? __shfl(qw[j & 3], l32 + 32 * (j >> 2)) : __shfl(qv[j & 1], l32);
        q[2 * j] = blo(w); q[2 * j + 1] = bhi(w); a[2 * j] = 0.f; a[2 * j + 1] = 0.f;
      }
      const int row = rt * 128 + 32 * qi + l32;
#pragma nounroll
      for (int t = 0; t < 128; ++t) {
        const int j = row - 2 * t - h;
        if (j >= 0) {
          const u32 src = W_KVG + (((j >> 7) * 8 + hg) * 4 + ((j >> 5) & 3)) * 10240 + hd * 2560 + (j & 31) * 80;
          u32 w[20];
#pragma unroll
          for (int i = 0; i < 5; i++) { const u32x4 v = __builtin_amdgcn_raw_buffer_load_b128(rws, src, i * 16, 16); w[4 * i] = v.x; w[4 * i + 1] = v.y; w[4 * i + 2] = v.z; w[4 * i + 3] = v.w; }
          float sc = 0.f;
#pragma unroll
          for (int i = 0; i < 10; i++) sc += q[2 * i] * blo(w[i]) + q[2 * i + 1] * bhi(w[i]);
          const float mn = fmaxf(mx, sc), f = ex2(mx - mn), p = ex2(sc - mn);
          mx = mn; l = l * f + p;
#pragma unroll
          for (int i = 0; i < 10; i++) { a[2 * i] = a[2 * i] * f + p * blo(w[10 + i]); a[2 * i + 1] = a[2 * i + 1] * f + p * bhi(w[10 + i]); }
        }
      }
      const float mo = __shfl_xor(mx, 32), mn = fmaxf(mx, mo), f = ex2(mx - mn);
      l *= f; l += __shfl_xor(l, 32);
#pragma unroll
      for (int i = 0; i < 20; i++) { a[i] *= f; a[i] += __shfl_xor(a[i], 32); }
#pragma unroll
      for (int r = 0; r < 8; r++) o[r] = h ? a[(r & 3) + 8 * (r >> 2) + 4] : a[(r & 3) + 8 * (r >> 2)];
#pragma unroll
      for (int r = 8; r < 12; r++) o[r] = a[8 + r];
    }
    const float rl = __builtin_amdgcn_rcpf(l);
    const u32x4 w0 = {cvtpk(o[0] * rl, o[1] * rl), cvtpk(o[2] * rl, o[3] * rl), cvtpk(o[4] * rl, o[5] * rl), cvtpk(o[6] * rl, o[7] * rl)};
    const u32x2 w1 = {cvtpk(o[8] * rl, o[9] * rl), cvtpk(o[10] * rl, o[11] * rl)};
    __builtin_amdgcn_raw_buffer_store_b128(w0, rws, pc[7], 0, 16);
    __builtin_amdgcn_raw_buffer_store_b64(w1, rws, pc[8], 0, 16);
    lds_inc(misc + M_AD, lane);
    if (wow && wk > 5) { lds_wait_ge(misc + M_C1, 16u); wo_dma(5, wk); wk = 5; }
    if (wid >= 8) published(qi);
    if (wow) {
      lds_inc(misc + M_C6, lane);
      if (wk > 0) { if (rt) lds_wait_ge(misc + M_C3, 16u); wo_dma(0, wk); waitv<0>(); }
      lds_inc(misc + M_C6B, lane);
    }
    __builtin_amdgcn_s_setprio(0);
  } else {
    const int tg = wid & 3, pair = (wid >> 2) & 1, isq = wid >> 3;
    u32 phi[20];
    {
      lds_wait_ge(misc + M_WR + (isq ? 0 : 16) + tg * 4, 1u);
      const bf16x8 b0 = ld128(LP(pc[0])), a00 = ld128(LP(pc[1])), a10 = ld128(LP(pc[1]) + 2560);
      const bf16x8 b1 = ld128(LP(pc[0]) + 32), a01 = ld128(LP(pc[1]) + 1024), a11 = ld128(LP(pc[1]) + 2560 + 1024);
      const s16x4 b2 = ld64(LP(pc[8])), a02 = ld64(LP(pc[2])), a12 = ld64(LP(pc[2]) + 2560);
      lds_inc(misc + M_C5, lane);
      if (wow) { wo_init(); lds_wait_ge(misc + M_C5, 16u); wo_dma(6, 20); waitv<0>(); lds_inc(misc + M_C6, lane); }
      if (isq) { __builtin_amdgcn_s_setprio(0); lds_wait_ge(misc + M_C0 + 4, 4u); } else __builtin_amdgcn_s_setprio(2);
      f32x16 t0 = MFMA32(a00, b0, f32x16{}), t1 = MFMA32(a10, b0, f32x16{});
      t0 = MFMA32(a01, b1, t0); t1 = MFMA32(a11, b1, t1);
      t0 = MFMA32K8(a02, b2, t0); t1 = MFMA32K8(a12, b2, t1);
      float p[20];
#pragma unroll
      for (int j = 0; j < 10; j++) {
        const u32 w = j < 8 ? cvtpk(t0[2 * j], t0[2 * j + 1]) : cvtpk(t1[2 * j - 16], t1[2 * j - 15]);
        p[2 * j] = blo(w); p[2 * j + 1] = bhi(w);
      }
      float mx = fmaxf(p[0], p[1]), mn = fminf(p[0], p[1]);
#pragma unroll
      for (int j = 2; j < 20; j += 2) { mx = fmaxf(fmaxf(mx, p[j]), p[j + 1]); mn = fminf(fminf(mn, p[j]), p[j + 1]); }
      const float L2E = 1.4426950408889634f;
      const f32x2 sl = {L2E, -L2E}, of = {-mx * L2E, mn * L2E};
      f32x2 e[20], sa = {0.f, 0.f}, sb = {0.f, 0.f};
#pragma unroll
      for (int j = 0; j < 20; j++) {
        const f32x2 a = __builtin_elementwise_fma(f32x2{p[j], p[j]}, sl, of);
        e[j] = f32x2{ex2(a[0]), ex2(a[1])};
        if (j & 1) sb += e[j]; else sa += e[j];
      }
      sa += sb;
      const f32x2 rr = {__builtin_amdgcn_rcpf(sa[0]), __builtin_amdgcn_rcpf(sa[1])};
#pragma unroll
      for (int j = 0; j < 20; j++) phi[j] = cvtpk2(e[j] * rr);
    }
    if (!isq) {
      lds_wait_ge(misc + M_WV, 4u);
      bf16x8 vb[4];
#pragma unroll
      for (int ks = 0; ks < 4; ks++) vb[ks] = cat8(trrd(LP(pc[3]) + ks * 32 * 48), trrd(LP(pc[3]) + ks * 32 * 48 + 4 * 48));
      lds_wait_ge(misc + M_C8, 12u);
#pragma unroll
      for (int j = 0; j < 5; j++) *(LAS u32x4*)(LP(pc[4]) + 16 * j) = u32x4{phi[4 * j], phi[4 * j + 1], phi[4 * j + 2], phi[4 * j + 3]};
      lds_inc(misc + M_C0 + pair * 4, lane);
      lds_wait_ge(misc + M_C0 + pair * 4, 4u);
      f32x4 kv[3] = {};
#pragma unroll
      for (int ks = 0; ks < 4; ks++)
#pragma unroll
        for (int T = 0; T < 3; T++) kv[T] = MFMA16(cat8(trrd(LP(pc[5]) + ks * 2560 + T * 32), trrd(LP(pc[5]) + ks * 2560 + 320 + T * 32)), vb[ks], kv[T]);
#pragma unroll
      for (int T = 0; T < 3; T++)
        __builtin_amdgcn_raw_buffer_store_b128(u32x4{cvtpk(kv[T][0], kv[T][1]), tag, cvtpk(kv[T][2], kv[T][3]), tag}, rws, pc[T < 2 ? 6 : 7], T * ((tg & 1) ? 320 : 1024), 16);
      lds_inc(misc + M_C1, lane);
      if (wid == 0) {
        const int sl = (rt & 3) * 53 + lane; const bool act = lane < 53 && sl < 210;
        const u32 src = act ? W_PART + (hg * 4 + (rt >> 2)) * 16 * PARTB + sl * 16 : OOB;
        float s0 = 0.f, s1 = 0.f, s2 = 0.f, s3 = 0.f;
        lds_wait_ge(misc + M_C6, 5u);
        lds_wait_ge(misc + M_C7, 4u);
#pragma nounroll
        for (u32 s = 0; s < SPIN_CAP; ++s) {
          bool ok = true; s0 = 0.f; s1 = 0.f; s2 = 0.f; s3 = 0.f;
          u32x4 v[16];
#pragma unroll
          for (int r = 0; r < 16; r++) v[r] = __builtin_amdgcn_raw_buffer_load_b128(rws, src, r * PARTB, 16);
#pragma unroll
          for (int r = 0; r < 16; r++) { ok &= (v[r].y == tag) & (v[r].w == tag); s0 += blo(v[r].x); s1 += bhi(v[r].x); s2 += blo(v[r].z); s3 += bhi(v[r].z); }
          if (__all(ok || !act)) break;
          asm volatile("" ::: "memory");
        }
        __builtin_amdgcn_raw_buffer_store_b128(u32x4{cvtpk(s0, s1), tag, cvtpk(s2, s3), tag}, rws, act ? W_SLICE + (hg * 16 + rt) * 1024 + lane * 16 : OOB, 0, 16);
        waitv<0>();
        lds_inc(misc + M_C4, lane);
      } else if (wid >= 4) {
        const int hh = wid - 4;
        const u32 src = W_SLICE + (hg * 16 + hh * 4) * 1024 + lane * 16;
        int da[4];
#pragma unroll
        for (int j = 0; j < 4; j++) {
          const int sl = j * 53 + lane; int d, ph;
          if (sl < 160) { d = sl & 15; ph = (sl >> 4) * 4; }
          else { const int s2 = sl - 160, gg = s2 / 5; d = 16 + s2 - 5 * gg; ph = 4 * gg; }
          da[j] = L_KVT + hh * 1680 + d * 80 + ph * 2;
        }
        waitv<0>(); lds_inc(misc + M_C7, lane);
        wo_init();
        lds_wait_ge(misc + M_C1, 8u);
        lds_wait_ge(misc + M_C5, 16u);
        lds_wait_ge(misc + M_C4, 1u);
        u32x4 v[4];
#pragma nounroll
        for (u32 s = 0; s < SPIN_CAP; ++s) {
          bool ok = true;
#pragma unroll
          for (int r = 0; r < 4; r++) v[r] = __builtin_amdgcn_raw_buffer_load_b128(rws, src, r * 1024, 16);
#pragma unroll
          for (int r = 0; r < 4; r++) ok &= ((v[r].y == tag) & (v[r].w == tag)) | (lane >= (r < 3 ? 53 : 51));
          if (__all(ok)) break;
          asm volatile("" ::: "memory");
        }
#pragma unroll
        for (int r = 0; r < 4; r++) if (lane < (r < 3 ? 53 : 51)) *(LAS u32x2*)(lds + da[r]) = u32x2{v[r].x, v[r].z};
        lds_inc(misc + M_C3, lane);
        wo_dma(0, 6); waitv<0>(); lds_inc(misc + M_C6, lane);
      }
      __builtin_amdgcn_s_setprio(0);
    } else {
      lds_ptr a0 = LP(pc[3]), a1 = LP(pc[4]); const u32 om = pc[5], ot = pc[6];
      lds_wait_ge(misc + M_C3, 4u);
      f32x16 t0 = {}, t1 = {};
#pragma unroll
      for (int j = 0; j < 5; j++) {
        const bf16x8 bq = __builtin_bit_cast(bf16x8, u32x4{phi[4 * j], phi[4 * j + 1], phi[4 * j + 2], phi[4 * j + 3]});
        t1 = MFMA32(ld128(a1 + 16 * j), bq, t1); t0 = MFMA32(ld128(a0 + 16 * j), bq, t0);
      }
      const float rl = __builtin_amdgcn_rcpf(t1[4] + 1e-6f);
      const u32x4 w0 = {cvtpk(t0[0] * rl, t0[1] * rl), cvtpk(t0[2] * rl, t0[3] * rl), cvtpk(t0[8] * rl, t0[9] * rl), cvtpk(t0[10] * rl, t0[11] * rl)};
      const u32x4 w1 = {cvtpk(t0[4] * rl, t0[5] * rl), cvtpk(t0[6] * rl, t0[7] * rl), cvtpk(t0[12] * rl, t0[13] * rl), cvtpk(t0[14] * rl, t0[15] * rl)};
      const u32x2 w2 = {cvtpk(t1[0] * rl, t1[1] * rl), cvtpk(t1[2] * rl, t1[3] * rl)};
      __builtin_amdgcn_raw_buffer_store_b128(w0, rws, om, 0, 16);
      __builtin_amdgcn_raw_buffer_store_b128(w1, rws, om, 256, 16);
      __builtin_amdgcn_raw_buffer_store_b64(w2, rws, ot, 0, 16);
      published(tg);
    }
  }
  relane();
  if (wid >= 8) {
    if (b == 0 && wid == 15) {
#pragma nounroll
      for (u32 s = 0; s < SPIN_CAP; ++s) {
        const u32x4 v = __builtin_amdgcn_raw_buffer_load_b128(rws, W_STARTED + lane * 16, 0, 16);
        if (__all(v.x == tag && v.y == tag && v.z == tag && v.w == tag)) break;
        __builtin_amdgcn_s_sleep(2);
      }
      if (lane == 0) gstore(ws, W_EPOCH, tag);
    }
  } else {
    const int rg = br ? (0x2013 >> (4 * (wid >> 1))) & 3 : wid >> 1, kh = wid & 1;
    u32 obm = og + (rt * 8 + rg * 2) * 20480 + kh * 8192 + lane * 16, obt = og + (rt * 8 + rg * 2) * 20480 + kh * 2048 + lane * 8;
    PIN(obm); PIN(obt);
    const u32 mo[2] = {(u32)kh * 20480u, (u32)(1 - kh) * 20480u};
    u32x4 of[10][2];
    auto ldo = [&](int s) {
#pragma unroll
      for (int m = 0; m < 2; m++) {
        if (s < 8) of[s][m] = __builtin_amdgcn_raw_buffer_load_b128(rws, obm, mo[m] + s * 1024, 16);
        else {
          const u32x2 a = __builtin_amdgcn_raw_buffer_load_b64(rws, obt, mo[m] + (s + 8) * 1024, 16);
          const u32x2 c = __builtin_amdgcn_raw_buffer_load_b64(rws, obt, mo[m] + (s + 8) * 1024 + 512, 16);
          of[s][m] = u32x4{a.x, a.y, c.x, c.y};
        }
      }
    };
    u32 wm = lds0 + l_wo + (16 * (g >> 1) + 4 * (g & 1) + q4) * 160 + 8 * p4, wt = wm + (16 + 2 * kh) * 5120; wm += kh * 8 * 5120;
    PIN(wm); PIN(wt);
    constexpr int WFN = 8;
    bf16x8 wf[WFN];
    auto ldw = [&](int i) { const int s = 9 - i / 5, n = i % 5; lds_ptr p = (s < 8 ? LP(wm) + s * 5120 : LP(wt) + (s - 8) * 5120) + n * 32; wf[i % WFN] = cat8(trrd(p), trrd(p + 1280)); };
    f32x4 ac2[2][5];
    constexpr int PF = 5;
    if (br) published(1 - (wid >> 2));
    lds_wait_ge(misc + M_C6, 9u);
    lds_wait_ge(misc + M_D + rg * 4, br ? 4u : 2u);
#pragma nounroll
    for (int pass = 0;; ++pass) {
      if (pass) poll_tag(ws, ofl + (lane & 7) * 4, tag, false);
#pragma unroll
      for (int i = 0; i < WFN; i++) ldw(i);
#pragma nounroll
      for (int tr = 0; tr < 64; ++tr) {
#pragma unroll
        for (int s = 9; s > 9 - PF; s--) ldo(s);
        asm volatile("" ::: "memory");
        u32 mx = 0;
#pragma unroll
        for (int s = 8; s < 10; s++)
#pragma unroll
          for (int m = 0; m < 2; m++) mx = max(max(mx, max(of[s][m].x, of[s][m].y)), max(of[s][m].z, of[s][m].w));
        if (pass || !__any(mx == ~0u)) break;
      }
#pragma unroll
      for (int n = 0; n < 5; n++) { ac2[0][n] = f32x4{}; ac2[1][n] = f32x4{}; }
#pragma unroll
      for (int s = 9; s >= 0; s--) {
        if (s == 6) lds_wait_ge(misc + M_C6B, br ? 4u : 0u);
        if (s == 1) lds_wait_ge(misc + M_AD, br ? 16u : 0u);
        SBAR();
#pragma unroll
        for (int n = 0; n < 5; n++) {
          const int i = (9 - s) * 5 + n;
          ac2[0][n] = MFMA16(wf[i % WFN], __builtin_bit_cast(bf16x8, of[s][0]), ac2[0][n]);
          ac2[1][n] = MFMA16(wf[i % WFN], __builtin_bit_cast(bf16x8, of[s][1]), ac2[1][n]);
          if (i + WFN < 50) ldw(i + WFN);
          SBAR();
        }
        if (s - PF >= 0) ldo(s - PF);
      }
      const float chk = ac2[0][0][0] + ac2[1][0][0];
      if (pass || !__any(chk != chk)) break;
    }
    relane();
    lds_ptr red = lds + L_RED + lane * 16;
#pragma unroll
    for (int n = 0; n < 5; n++) *(LAS f32x4*)(red + (wid ^ 1) * 5120 + n * 1024) = ac2[1][n];
    lds_inc(misc + M_X + (wid ^ 1) * 4, lane);
    const int r16 = rt * 8 + rg * 2 + kh;
    u32 ob1 = (r16 * 16 + i16) * 2560 + br * 1280 + hg * 160, ob2 = ob1 + g * 8; ob1 += (g & 1) * 32 + (g >> 1) * 16;
    PIN(ob1); PIN(ob2);
    lds_wait_ge(misc + M_X + wid * 4, 1u);
    f32x4 acc[5];
#pragma unroll
    for (int n = 0; n < 5; n++) acc[n] = *(LAS f32x4*)(red + wid * 5120 + n * 1024);
    SBAR();
#pragma unroll
    for (int n = 0; n < 5; n++) acc[n] += ac2[0][n];
    const rsrc_t ro = __builtin_amdgcn_make_buffer_rsrc((void*)out, (short)0, S_ * 1280 * 2, 0x00020000);
#pragma unroll
    for (int n = 0; n < 4; n += 2) {
      const auto x = __builtin_amdgcn_permlane16_swap(cvtpk(acc[n][0], acc[n][1]), cvtpk(acc[n + 1][0], acc[n + 1][1]), false, false);
      const auto y = __builtin_amdgcn_permlane16_swap(cvtpk(acc[n][2], acc[n][3]), cvtpk(acc[n + 1][2], acc[n + 1][3]), false, false);
      __builtin_amdgcn_raw_buffer_store_b128(u32x4{x[0], y[0], x[1], y[1]}, ro, ob1, n * 32, 0);
    }
    __builtin_amdgcn_raw_buffer_store_b64(u32x2{cvtpk(acc[4][0], acc[4][1]), cvtpk(acc[4][2], acc[4][3])}, ro, ob2, 128, 0);
  }
  asm volatile(".Lcode_end:");
}

extern "C" void kernel_launch(void* const* d_in, const int* in_sizes, int n_in, void* d_out, int out_size, void* d_ws, size_t ws_size, hipStream_t stream) {
  (void)hipFuncSetAttribute((const void*)fused, hipFuncAttributeMaxDynamicSharedMemorySize, LDS_BYTES);
  fused<<<256, 1024, LDS_BYTES, stream>>>((const u16*)d_in[0], (const u16*)d_in[1], (const u16*)d_in[2], (const u16*)d_in[3],
                                          (const u16*)d_in[4], (const u16*)d_in[5], (const u16*)d_in[6], (u16*)d_out, (char*)d_ws);
}
```
